```python
import torch

def _softmax(x, axis=-1):
    return torch.softmax(x, dim=axis)
def _gelu(x):
    return torch.nn.functional.gelu(x, approximate="tanh")
def _silu(x):
    return torch.nn.functional.silu(x)
def _sigmoid(x):
    return torch.sigmoid(x)
def _layernorm(x, axis=-1, eps=1e-5):
    m = x.mean(dim=axis, keepdim=True)
    v = x.var(dim=axis, keepdim=True, unbiased=False)
    return (x - m) / torch.sqrt(v + eps)
def _scatter_add(tgt, idx, src, axis=0):
    return tgt.clone().index_add_(axis, idx, src)
def _segment_sum(src, seg, n):
    out = torch.zeros((n,) + src.shape[1:], dtype=src.dtype)
    return out.index_add_(0, seg, src)
def _pad(x, pw):
    flat = tuple(v for pair in reversed(pw) for v in pair)
    return torch.nn.functional.pad(x, flat)

kernel_name = 'sequential_preLN__grouped_kv_mqa__glu_gated_bilinear__prompt'

def setup_inputs():

    _w = (lambda a: (lambda t: t.to(torch.bfloat16) if t.is_floating_point() else t)(torch.as_tensor(a)))
    def _A_setup():
        B, S, D, H = 1, 2048, 640, 32
        Dh = D // H
        return {
            "x": _w((lambda *s: torch.randn(*s, dtype=torch.bfloat16) * 0.05)(B, S, D)),
            "wq": _w((lambda *s: torch.randn(*s, dtype=torch.bfloat16) * 0.05)(D, H * Dh)),
            "wk": _w((lambda *s: torch.randn(*s, dtype=torch.bfloat16) * 0.05)(D, Dh)),
            "wv": _w((lambda *s: torch.randn(*s, dtype=torch.bfloat16) * 0.05)(D, Dh)),
            "wo": _w((lambda *s: torch.randn(*s, dtype=torch.bfloat16) * 0.05)(H * Dh, D)),
        }
    _A_in = _A_setup()

    def _B_setup():
        B, S, D, F = 1, 2048, 640, 1728
        return {
            "x": _w((lambda *s: torch.randn(*s, dtype=torch.bfloat16) * 0.05)(B, S, D)),
            "w_gate": _w((lambda *s: torch.randn(*s, dtype=torch.bfloat16) * 0.8)(D, F) * (D ** -0.5)),
            "w_up": _w((lambda *s: torch.randn(*s, dtype=torch.bfloat16) * 0.8)(D, F) * (D ** -0.5)),
            "w_down": _w((lambda *s: torch.randn(*s, dtype=torch.bfloat16) * 0.8)(F, D) * (F ** -0.5)),
        }
    _B_in = _B_setup()

    return {
        "A_x": _A_in["x"],
        "A_wq": _A_in["wq"],
        "A_wk": _A_in["wk"],
        "A_wv": _A_in["wv"],
        "A_wo": _A_in["wo"],
        "B_x": _B_in["x"],
        "B_w_gate": _B_in["w_gate"],
        "B_w_up": _B_in["w_up"],
        "B_w_down": _B_in["w_down"],
    }

def reference(A_x, A_wq, A_wk, A_wv, A_wo, B_x, B_w_gate, B_w_up, B_w_down):
    def _A_ref(x, wq, wk, wv, wo):
        B, S, D = x.shape
        H = 32
        Dh = D // H
        q = (lambda x, *d: x.permute(*d))((x @ wq).reshape(B, S, H, Dh), 0, 2, 1, 3)
        k = x @ wk
        v = x @ wv
        scores = torch.einsum("bhqd,bkd->bhqk", q, k) * (Dh ** -0.5)
        p = _softmax(scores, axis=-1)
        o = torch.einsum("bhqk,bkd->bhqd", p, v)
        return (lambda x, *d: x.permute(*d))(o, 0, 2, 1, 3).reshape(B, S, H * Dh) @ wo

    def _B_ref(x, w_gate, w_up, w_down):
        g = torch.einsum("bsd,df->bsf", x, w_gate)
        u = torch.einsum("bsd,df->bsf", x, w_up)
        h = g * u
        return torch.einsum("bsf,fd->bsd", h, w_down)

    x = A_x
    h = x + _A_ref(_layernorm(x, axis=-1), A_wq, A_wk, A_wv, A_wo)
    h2 = h + _B_ref(_layernorm(h, axis=-1), B_w_gate, B_w_up, B_w_down)
    return h2
```

```cpp
#include <hip/hip_runtime.h>
#include <hip/hip_bf16.h>
#include <stdint.h>

typedef __bf16 bf16x8 __attribute__((ext_vector_type(8)));
typedef __bf16 bf16x2 __attribute__((ext_vector_type(2)));
typedef short s16x4 __attribute__((ext_vector_type(4)));
typedef short s16x8 __attribute__((ext_vector_type(8)));
typedef float f32x2 __attribute__((ext_vector_type(2)));
typedef float f32x4 __attribute__((ext_vector_type(4)));
typedef float f32x16 __attribute__((ext_vector_type(16)));
typedef unsigned u32x2 __attribute__((ext_vector_type(2)));
typedef unsigned u32x4 __attribute__((ext_vector_type(4)));
typedef unsigned short u16;
#define LAS __attribute__((address_space(3)))
typedef LAS char* ldsp;
typedef const LAS char* ldscp;

constexpr int S = 2048, D = 640, NH = 32, DH = 20, F = 1728;
constexpr float QSCALE = 0.22360679774997896f * 1.4426950408889634f;
constexpr float LN_EPS = 1e-5f;

constexpr size_t WS_QF = 0;
constexpr size_t WS_KF = WS_QF + 64ull * 32 * 1536;
constexpr size_t WS_VF = WS_KF + 64ull * 1536;
constexpr size_t WS_KMAX = WS_VF + 64ull * 2048;
constexpr size_t WS_OF = WS_KMAX + 1024;
constexpr size_t WS_HF = WS_OF + 128ull * 20 * 1024;
constexpr size_t WS_ST = WS_HF + 128ull * 20 * 1024;
constexpr size_t WS_CS = WS_ST + 2048ull * 8 * 8;
constexpr size_t WS_HH = WS_CS + 16384;
constexpr size_t WS_END = WS_HH + 128ull * 54 * 1024;

__device__ __forceinline__ unsigned pk2(float lo, float hi) {
    f32x2 v = {lo, hi}; bf16x2 r = __builtin_convertvector(v, bf16x2); return __builtin_bit_cast(unsigned, r); }
__device__ __forceinline__ float bflo(unsigned u) { return __uint_as_float(u << 16); }
__device__ __forceinline__ float bfhi(unsigned u) { return __uint_as_float(u & 0xffff0000u); }
__device__ __forceinline__ void glds16(const void* g, unsigned lds_base) {
    unsigned sv; asm volatile("s_mov_b32 %0, m0\n\ts_mov_b32 m0, %2\n\ts_nop 0\n\tglobal_load_lds_dwordx4 %1, off\n\ts_mov_b32 m0, %0" : "=&s"(sv) : "v"(g), "s"(lds_base) : "memory"); }
typedef short v4i16_t __attribute__((ext_vector_type(4)));
__device__ __forceinline__ s16x4 trd(ldscp p) { return __builtin_bit_cast(s16x4, __builtin_amdgcn_ds_read_tr16_b64_v4i16((LAS v4i16_t*)p)); }
__device__ __forceinline__ bf16x8 cat8(s16x4 a, s16x4 b) { s16x8 v = {a[0], a[1], a[2], a[3], b[0], b[1], b[2], b[3]}; return __builtin_bit_cast(bf16x8, v); }
__device__ __forceinline__ bf16x8 ldsr16(ldscp p) { return *(const LAS bf16x8*)p; }
#define MFMA32(a, b, c) __builtin_amdgcn_mfma_f32_32x32x16_bf16(a, b, c, 0, 0, 0)
#define MFMA32K8(a, b, c) __builtin_amdgcn_mfma_f32_32x32x8bf16_1k(a, b, c, 0, 0, 0)
#define MFMA16(a, b, c) __builtin_amdgcn_mfma_f32_16x16x32_bf16(a, b, c, 0, 0, 0)
#define WAITV(N) asm volatile("s_waitcnt vmcnt(" #N ")" ::: "memory")
#define BAR() asm volatile("s_waitcnt lgkmcnt(0)\n\ts_barrier" ::: "memory")
__device__ __forceinline__ float swap32(float v) {
    auto r = __builtin_amdgcn_permlane32_swap(__float_as_uint(v), __float_as_uint(v), false, false);
    return (threadIdx.x & 32) ? __uint_as_float(r[0]) : __uint_as_float(r[1]); }

constexpr int XT_STRIDE = 1296;
constexpr int K1_XT = 0, K1_WKV = 64 * XT_STRIDE, K1_RING = K1_WKV + 25600, K1_SLOT = 128 * 192, K1_RED = K1_RING + 2 * K1_SLOT;
constexpr int K1_LDS = K1_RED + 6144;
static_assert(K1_LDS <= 163840, "lds");

__global__ void __launch_bounds__(512, 2) k_front(const u16* __restrict__ x, const u16* __restrict__ wq, const u16* __restrict__ wk, const u16* __restrict__ wv,
                                                  const u16* __restrict__ wg, const u16* __restrict__ wu, char* __restrict__ ws) {
    extern __shared__ __attribute__((aligned(16))) char smem[];
    const ldsp lds = (ldsp)smem; const unsigned lds0 = (unsigned)(uintptr_t)smem;
    const int tid = threadIdx.x, lane = tid & 63, w = __builtin_amdgcn_readfirstlane(tid >> 6);
    const int b = blockIdx.x, hg = (b >> 3) & 7, tt = (b & 7) + 8 * (b >> 6);
    const u16* wkv = (hg & 1) ? wv : wk;
    for (int i = w; i < 25; i += 8) glds16((const char*)wkv + i * 1024 + lane * 16, lds0 + K1_WKV + i * 1024);
    unsigned wqoff[3];
#pragma unroll
    for (int i = 0; i < 3; ++i) { const int p = (3 * w + i) * 64 + lane, r = p / 12; int c = p - r * 12; if (c >= 10) c -= 2; wqoff[i] = (unsigned)(r * (D * 2) + hg * 160 + c * 16); }
#define K1_DMAQ(c, slotoff) do { _Pragma("unroll") for (int i_ = 0; i_ < 3; ++i_) glds16((const char*)wq + (size_t)(c) * 128 * D * 2 + wqoff[i_], lds0 + (slotoff) + (3 * w + i_) * 1024); } while (0)
    K1_DMAQ(0, K1_RING); K1_DMAQ(1, K1_RING + K1_SLOT);
    {
        const int row = w * 8 + (lane >> 3);
        const u32x4* xp = (const u32x4*)(x + (size_t)(tt * 64 + row) * D) + (lane & 7);
        u32x4 v[10];
#pragma unroll
        for (int i = 0; i < 10; ++i) v[i] = xp[i * 8];
        float s = 0.f;
#pragma unroll
        for (int i = 0; i < 10; ++i)
#pragma unroll
            for (int j = 0; j < 4; ++j) s += bflo(v[i][j]) + bfhi(v[i][j]);
        s += __shfl_xor(s, 1); s += __shfl_xor(s, 2); s += __shfl_xor(s, 4);
        const float mean = s * (1.f / D); float q = 0.f;
#pragma unroll
        for (int i = 0; i < 10; ++i)
#pragma unroll
            for (int j = 0; j < 4; ++j) { const float a = bflo(v[i][j]) - mean, c = bfhi(v[i][j]) - mean; q += a * a + c * c; }
        q += __shfl_xor(q, 1); q += __shfl_xor(q, 2); q += __shfl_xor(q, 4);
        const float rstd = 1.0f / sqrtf(q * (1.f / D) + LN_EPS);
#pragma unroll
        for (int i = 0; i < 10; ++i) { u32x4 o;
#pragma unroll
            for (int j = 0; j < 4; ++j) o[j] = pk2((bflo(v[i][j]) - mean) * rstd, (bfhi(v[i][j]) - mean) * rstd);
            *(LAS u32x4*)(lds + K1_XT + row * XT_STRIDE + ((lane & 7) + 8 * i) * 16) = o; }
    }
    WAITV(0);
    BAR();
    const int tk = hg >> 1; const bool isV = hg & 1;
    f32x4 kv0 = {0.f, 0.f, 0.f, 0.f}, kv1 = kv0;
    if (w < 4) {
        const int g = lane >> 4, q = (lane & 15) >> 2, p = lane & 3;
        const ldscp xb = lds + K1_XT + (tk * 16 + (lane & 15)) * XT_STRIDE + g * 16;
        const ldscp wb = lds + K1_WKV + (8 * g + q) * 40 + p * 8;
#pragma unroll
        for (int s = 0; s < 5; ++s) { const int k0 = w * 160 + s * 32;
            const bf16x8 xf = ldsr16(xb + k0 * 2);
            const bf16x8 w0 = cat8(trd(wb + k0 * 40), trd(wb + k0 * 40 + 160));
            const bf16x8 w1 = cat8(trd(wb + k0 * 40 + 32), trd(wb + k0 * 40 + 160 + 32));
            if (isV) { kv0 = MFMA16(xf, w0, kv0); kv1 = MFMA16(xf, w1, kv1); }
            else     { kv0 = MFMA16(w0, xf, kv0); kv1 = MFMA16(w1, xf, kv1); } }
        if (w > 0) { *(LAS f32x4*)(lds + K1_RED + ((w - 1) * 2 + 0) * 1024 + lane * 16) = kv0; *(LAS f32x4*)(lds + K1_RED + ((w - 1) * 2 + 1) * 1024 + lane * 16) = kv1; }
    }
    BAR();
    K1_DMAQ(2, K1_WKV);
    if (w == 0) {
#pragma unroll
        for (int i = 0; i < 3; ++i) { kv0 += *(const LAS f32x4*)(lds + K1_RED + (i * 2 + 0) * 1024 + lane * 16); kv1 += *(const LAS f32x4*)(lds + K1_RED + (i * 2 + 1) * 1024 + lane * 16); }
        const int g = lane >> 4, c16 = lane & 15, tok0 = tt * 64 + tk * 16, t32 = tok0 >> 5;
        const u32x2 p0 = {pk2(kv0[0], kv0[1]), pk2(kv0[2], kv0[3])};
        u32x2 p1 = {pk2(kv1[0], kv1[1]), pk2(kv1[2], kv1[3])};
        if (!isV) {
            const int kk = (tok0 & 31) + c16;
            char* kf = ws + WS_KF + (size_t)t32 * 1536;
            *(u32x2*)(kf + ((g & 1) * 32 + kk) * 16 + (g >> 1) * 8) = p0;
            float n2 = bflo(p0[0]) * bflo(p0[0]) + bfhi(p0[0]) * bfhi(p0[0]) + bflo(p0[1]) * bflo(p0[1]) + bfhi(p0[1]) * bfhi(p0[1]);
            if (g == 0) { n2 += bflo(p1[0]) * bflo(p1[0]) + bfhi(p1[0]) * bfhi(p1[0]) + bflo(p1[1]) * bflo(p1[1]) + bfhi(p1[1]) * bfhi(p1[1]); *(u32x2*)(kf + 1024 + kk * 8) = p1; }
            if (g == 2) { const u32x2 one = {0x00003F80u, 0u}; *(u32x2*)(kf + 1024 + (32 + kk) * 8) = one; }
            n2 += __shfl_xor(n2, 16); n2 += __shfl_xor(n2, 32);
            n2 = fmaxf(n2, __shfl_xor(n2, 1)); n2 = fmaxf(n2, __shfl_xor(n2, 2)); n2 = fmaxf(n2, __shfl_xor(n2, 4)); n2 = fmaxf(n2, __shfl_xor(n2, 8));
            if (lane == 0) ((float*)(ws + WS_KMAX))[tt * 4 + tk] = n2;
        } else {
            char* vf = ws + WS_VF + (size_t)t32 * 2048 + (tk & 1) * 1024 + (g >> 1) * 8;
            *(u32x2*)(vf + ((g & 1) * 32 + c16) * 16) = p0;
            if (c16 == 4) { p1[0] = 0x3F803F80u; p1[1] = 0x3F803F80u; } else if (c16 > 4) { p1[0] = 0u; p1[1] = 0u; }
            *(u32x2*)(vf + ((g & 1) * 32 + 16 + c16) * 16) = p1;
        }
    }
    const int hi = w & 3, tj = w >> 2;
    f32x16 acc = {};
    {
        const int G = lane >> 4, h = G >> 1, q = (lane & 15) >> 2, p = lane & 3;
        const int aoff = (8 * h + q) * 192 + hi * 40 + (G & 1) * 32 + p * 8;
        const ldscp xb = lds + K1_XT + (tj * 32 + (lane & 31)) * XT_STRIDE + (lane >> 5) * 16;
#pragma unroll
        for (int c = 0; c < 5; ++c) {
            if (c >= 1) {
                if (c == 1) { } else if (c == 2) { WAITV(3); } else if (c == 3) { WAITV(3); } else { WAITV(0); }
                BAR();
                if (c == 1) K1_DMAQ(3, K1_RING); else if (c == 2) K1_DMAQ(4, K1_RING + K1_SLOT);
            }
            const int so = (c == 0 || c == 3) ? K1_RING : (c == 1 || c == 4) ? K1_RING + K1_SLOT : K1_WKV;
            const ldscp ab = lds + so + aoff;
#pragma unroll
            for (int s = 0; s < 8; ++s) {
                const bf16x8 a = cat8(trd(ab + s * 16 * 192), trd(ab + s * 16 * 192 + 4 * 192));
                const bf16x8 xf = ldsr16(xb + (c * 128 + s * 16) * 2);
                acc = MFMA32(a, xf, acc);
            }
        }
    }
    {
        char* qf = ws + WS_QF + ((size_t)(tt * 2 + tj) * 32 + hg * 4 + hi) * 1536;
        u32x4 m; m[0] = pk2(acc[0] * QSCALE, acc[1] * QSCALE); m[1] = pk2(acc[2] * QSCALE, acc[3] * QSCALE); m[2] = pk2(acc[4] * QSCALE, acc[5] * QSCALE); m[3] = pk2(acc[6] * QSCALE, acc[7] * QSCALE);
        u32x2 r; r[0] = pk2(acc[8] * QSCALE, acc[9] * QSCALE); r[1] = pk2(acc[10] * QSCALE, acc[11] * QSCALE);
        *(u32x4*)(qf + lane * 16) = m; *(u32x2*)(qf + 1024 + lane * 8) = r;
    }
    if (b < 216) {
        const u16* wm = (b < 108) ? wg : wu; const int c0 = (b < 108 ? b : b - 108) * 16;
        float cs[8] = {0.f, 0.f, 0.f, 0.f, 0.f, 0.f, 0.f, 0.f};
        for (int r = tid >> 1; r < D; r += 256) { const u32x4 v = *(const u32x4*)(wm + (size_t)r * F + c0 + (tid & 1) * 8);
#pragma unroll
            for (int j = 0; j < 4; ++j) { cs[2 * j] += bflo(v[j]); cs[2 * j + 1] += bfhi(v[j]); } }
#pragma unroll
        for (int j = 0; j < 8; ++j) { float t = cs[j]; t += __shfl_xor(t, 2); t += __shfl_xor(t, 4); t += __shfl_xor(t, 8); t += __shfl_xor(t, 16); t += __shfl_xor(t, 32); cs[j] = t; }
        BAR();
        if (lane < 2) {
#pragma unroll
            for (int j = 0; j < 8; ++j) *(LAS float*)(lds + (w * 16 + lane * 8 + j) * 4) = cs[j]; }
        BAR();
        if (tid < 16) { float t = 0.f;
#pragma unroll
            for (int i = 0; i < 8; ++i) t += *(const LAS float*)(lds + (i * 16 + tid) * 4);
            ((float*)(ws + WS_CS))[(b < 108 ? 0 : F) + c0 + tid] = t; }
    }
}

constexpr int K2_LDS = 8 * 3 * 13 * 256;
__global__ void __launch_bounds__(512, 2) k_attn(char* __restrict__ ws) {
    extern __shared__ __attribute__((aligned(16))) char smem[];
    const ldsp lds = (ldsp)smem;
    const int tid = threadIdx.x, lane = tid & 63, w = __builtin_amdgcn_readfirstlane(tid >> 6);
    const int b = blockIdx.x, hg = (b >> 3) & 7, tt = (b & 7) + 8 * (b >> 6);
    const int rg = w & 1, kp = w >> 1, half = lane >> 5;
    float kmax2;
    { const float* km = (const float*)(ws + WS_KMAX); float t = fmaxf(km[lane], km[64 + lane]);
#pragma unroll
      for (int o = 1; o < 64; o <<= 1) t = fmaxf(t, __shfl_xor(t, o));
      kmax2 = t; }
    bf16x8 qm[4]; s16x4 qr[4]; float mrow[4]; bool unsafe = false;
#pragma unroll
    for (int qi = 0; qi < 4; ++qi) {
        const char* qf = ws + WS_QF + ((size_t)(tt * 2 + (qi & 1)) * 32 + hg * 4 + rg * 2 + (qi >> 1)) * 1536;
        const u32x4 m = *(const u32x4*)(qf + lane * 16); u32x2 r = *(const u32x2*)(qf + 1024 + lane * 8);
        float n2 = 0.f;
#pragma unroll
        for (int j = 0; j < 4; ++j) n2 += bflo(m[j]) * bflo(m[j]) + bfhi(m[j]) * bfhi(m[j]);
        if (!half) n2 += bflo(r[0]) * bflo(r[0]) + bfhi(r[0]) * bfhi(r[0]) + bflo(r[1]) * bflo(r[1]) + bfhi(r[1]) * bfhi(r[1]);
        n2 += swap32(n2);
        const float bound = sqrtf(n2 * kmax2) * 1.01f + 1e-3f;
        unsafe |= !(bound <= 50.f);
        const unsigned mb = pk2(-bound, 0.f) & 0xffffu;
        mrow[qi] = -bflo(mb);
        if (half) { r[0] = mb; r[1] = 0u; }
        qm[qi] = __builtin_bit_cast(bf16x8, m); qr[qi] = __builtin_bit_cast(s16x4, r);
    }
    const bool slow = __any(unsafe);
    f32x16 o[4];
#pragma unroll
    for (int qi = 0; qi < 4; ++qi) o[qi] = f32x16{};
    const char* kf = ws + WS_KF + (size_t)kp * 16 * 1536 + lane * 16;
    const char* kr = ws + WS_KF + (size_t)kp * 16 * 1536 + 1024 + lane * 8;
    const char* vf = ws + WS_VF + (size_t)kp * 16 * 2048 + lane * 16;
    if (!slow) {
        bf16x8 km = *(const bf16x8*)kf, v0 = *(const bf16x8*)vf, v1 = *(const bf16x8*)(vf + 1024); s16x4 kq = *(const s16x4*)kr;
#pragma unroll 2
        for (int t = 0; t < 16; ++t) {
            const int tn = t < 15 ? t + 1 : 15;
            const bf16x8 kmn = *(const bf16x8*)(kf + tn * 1536), v0n = *(const bf16x8*)(vf + tn * 2048), v1n = *(const bf16x8*)(vf + tn * 2048 + 1024);
            const s16x4 kqn = *(const s16x4*)(kr + tn * 1536);
#pragma unroll
            for (int qi = 0; qi < 4; ++qi) {
                f32x16 s = MFMA32K8(kq, qr[qi], f32x16{});
                s = MFMA32(km, qm[qi], s);
                u32x4 p0, p1;
#pragma unroll
                for (int j = 0; j < 4; ++j) { p0[j] = pk2(__builtin_amdgcn_exp2f(s[2 * j]), __builtin_amdgcn_exp2f(s[2 * j + 1])); p1[j] = pk2(__builtin_amdgcn_exp2f(s[8 + 2 * j]), __builtin_amdgcn_exp2f(s[9 + 2 * j])); }
                o[qi] = MFMA32(v0, __builtin_bit_cast(bf16x8, p0), o[qi]);
                o[qi] = MFMA32(v1, __builtin_bit_cast(bf16x8, p1), o[qi]);
            }
            km = kmn; v0 = v0n; v1 = v1n; kq = kqn;
        }
    } else {
#pragma unroll
        for (int qi = 0; qi < 4; ++qi) { mrow[qi] = -1e30f; if (half) { s16x4 z = {0, 0, 0, 0}; qr[qi] = z; } }
        for (int t = 0; t < 16; ++t) {
            const bf16x8 km = *(const bf16x8*)(kf + t * 1536), v0 = *(const bf16x8*)(vf + t * 2048), v1 = *(const bf16x8*)(vf + t * 2048 + 1024);
            const s16x4 kq = *(const s16x4*)(kr + t * 1536);
#pragma unroll
            for (int qi = 0; qi < 4; ++qi) {
                f32x16 s = MFMA32K8(kq, qr[qi], f32x16{});
                s = MFMA32(km, qm[qi], s);
                float mx = s[0];
#pragma unroll
                for (int j = 1; j < 16; ++j) mx = fmaxf(mx, s[j]);
                mx = fmaxf(mx, swap32(mx));
                const float mn = fmaxf(mrow[qi], mx), al = __builtin_amdgcn_exp2f(mrow[qi] - mn); mrow[qi] = mn;
                u32x4 p0, p1;
#pragma unroll
                for (int j = 0; j < 4; ++j) { p0[j] = pk2(__builtin_amdgcn_exp2f(s[2 * j] - mn), __builtin_amdgcn_exp2f(s[2 * j + 1] - mn)); p1[j] = pk2(__builtin_amdgcn_exp2f(s[8 + 2 * j] - mn), __builtin_amdgcn_exp2f(s[9 + 2 * j] - mn)); }
#pragma unroll
                for (int j = 0; j < 16; ++j) o[qi][j] *= al;
                o[qi] = MFMA32(v0, __builtin_bit_cast(bf16x8, p0), o[qi]);
                o[qi] = MFMA32(v1, __builtin_bit_cast(bf16x8, p1), o[qi]);
            }
        }
    }
#pragma unroll
    for (int qi = 0; qi < 4; ++qi) if (qi != kp) {
        const int si = qi > kp ? qi - 1 : qi; const ldsp p = lds + ((w * 3 + si) * 13) * 256 + lane * 4;
#pragma unroll
        for (int j = 0; j < 12; ++j) *(LAS float*)(p + j * 256) = o[qi][j];
        *(LAS float*)(p + 12 * 256) = mrow[qi];
    }
    __syncthreads();
    f32x16 of; float mf;
    { of = kp == 0 ? o[0] : kp == 1 ? o[1] : kp == 2 ? o[2] : o[3]; mf = kp == 0 ? mrow[0] : kp == 1 ? mrow[1] : kp == 2 ? mrow[2] : mrow[3]; }
    float pm[3], mx = mf;
#pragma unroll
    for (int i = 0; i < 3; ++i) { const int kq = i >= kp ? i + 1 : i, si = kp > kq ? kp - 1 : kp; pm[i] = *(const LAS float*)(lds + (((kq * 2 + rg) * 3 + si) * 13 + 12) * 256 + lane * 4); mx = fmaxf(mx, pm[i]); }
    float r[12]; { const float sc = __builtin_amdgcn_exp2f(mf - mx);
#pragma unroll
      for (int j = 0; j < 12; ++j) r[j] = of[j] * sc; }
#pragma unroll
    for (int i = 0; i < 3; ++i) { const int kq = i >= kp ? i + 1 : i, si = kp > kq ? kp - 1 : kp; const ldscp p = lds + (((kq * 2 + rg) * 3 + si) * 13) * 256 + lane * 4; const float sc = __builtin_amdgcn_exp2f(pm[i] - mx);
#pragma unroll
      for (int j = 0; j < 12; ++j) r[j] += *(const LAS float*)(p + j * 256) * sc; }
    const float lo = swap32(r[8]); const float l = half ? r[8] : lo; const float inv = 1.0f / l;
    const int head = hg * 4 + rg * 2 + (kp >> 1), tok = tt * 64 + (kp & 1) * 32 + (lane & 31);
    char* ob = ws + WS_OF + (size_t)(tok >> 4) * 20 * 1024 + (tok & 15) * 16;
#pragma unroll
    for (int a = 0; a < 3; ++a) { if (a == 2 && half) break;
        const int k = head * 20 + 8 * a + 4 * half;
        const u32x2 v = {pk2(r[4 * a] * inv, r[4 * a + 1] * inv), pk2(r[4 * a + 2] * inv, r[4 * a + 3] * inv)};
        *(u32x2*)(ob + (k >> 5) * 1024 + ((k & 15) >> 2) * 256 + ((k >> 4) & 1) * 8) = v; }
}

constexpr int K3_LDS = 640 * 160 + 4 * 5 * 1024;
__global__ void __launch_bounds__(512, 2) k_oproj(const u16* __restrict__ x, const u16* __restrict__ wo, char* __restrict__ ws) {
    extern __shared__ __attribute__((aligned(16))) char smem[];
    const ldsp lds = (ldsp)smem; const unsigned lds0 = (unsigned)(uintptr_t)smem;
    const int tid = threadIdx.x, lane = tid & 63, w = __builtin_amdgcn_readfirstlane(tid >> 6);
    const int b = blockIdx.x, ng = (b >> 3) & 7, tt = (b & 7) + 8 * (b >> 6);
    for (int i = w; i < 100; i += 8) { const int p = i * 64 + lane, r = p / 10, c = p - r * 10; glds16((const char*)wo + (size_t)r * (D * 2) + ng * 160 + c * 16, lds0 + i * 1024); }
    const int tl = w & 3, kh = w >> 2, g = lane >> 4, q = (lane & 15) >> 2, p = lane & 3;
    const char* ob = ws + WS_OF + ((size_t)(tt * 4 + tl) * 20 + kh * 10) * 1024 + lane * 16;
    bf16x8 bf[10];
#pragma unroll
    for (int s = 0; s < 10; ++s) bf[s] = *(const bf16x8*)(ob + s * 1024);
    const int tok = tt * 64 + tl * 16 + (lane & 15);
    u32x2 xr[5];
    if (kh == 0) {
#pragma unroll
        for (int n = 0; n < 5; ++n) xr[n] = *(const u32x2*)(x + (size_t)tok * D + ng * 80 + n * 16 + 4 * g); }
    WAITV(0); BAR();
    f32x4 acc[5];
#pragma unroll
    for (int n = 0; n < 5; ++n) acc[n] = f32x4{0.f, 0.f, 0.f, 0.f};
    const ldscp ab = lds + (kh * 320 + 4 * g + q) * 160 + p * 8;
#pragma unroll
    for (int s = 0; s < 10; ++s)
#pragma unroll
        for (int n = 0; n < 5; ++n) acc[n] = MFMA16(cat8(trd(ab + s * 32 * 160 + n * 32), trd(ab + s * 32 * 160 + 16 * 160 + n * 32)), bf[s], acc[n]);
    const ldsp red = lds + 640 * 160 + tl * 5 * 1024 + lane * 16;
    if (kh == 1) {
#pragma unroll
        for (int n = 0; n < 5; ++n) *(LAS f32x4*)(red + n * 1024) = acc[n]; }
    BAR();
    if (kh == 0) {
        u32x2 hv[5]; float s = 0.f;
        char* hb = ws + WS_HF + (size_t)(tt * 4 + tl) * 20 * 1024 + (lane & 15) * 16 + g * 256;
#pragma unroll
        for (int n = 0; n < 5; ++n) { const f32x4 a = acc[n] + *(const LAS f32x4*)(red + n * 1024);
            hv[n][0] = pk2(a[0] + bflo(xr[n][0]), a[1] + bfhi(xr[n][0])); hv[n][1] = pk2(a[2] + bflo(xr[n][1]), a[3] + bfhi(xr[n][1]));
            const int k = ng * 80 + n * 16;
            *(u32x2*)(hb + (k >> 5) * 1024 + ((k >> 4) & 1) * 8) = hv[n];
            s += bflo(hv[n][0]) + bfhi(hv[n][0]) + bflo(hv[n][1]) + bfhi(hv[n][1]); }
        s += __shfl_xor(s, 16); s += __shfl_xor(s, 32);
        const float mean = s * (1.f / 80.f); float m2 = 0.f;
#pragma unroll
        for (int n = 0; n < 5; ++n) { float d; d = bflo(hv[n][0]) - mean; m2 += d * d; d = bfhi(hv[n][0]) - mean; m2 += d * d; d = bflo(hv[n][1]) - mean; m2 += d * d; d = bfhi(hv[n][1]) - mean; m2 += d * d; }
        m2 += __shfl_xor(m2, 16); m2 += __shfl_xor(m2, 32);
        if (g == 0) *(f32x2*)(ws + WS_ST + ((size_t)tok * 8 + ng) * 8) = f32x2{mean, m2};
    }
}

constexpr int K4_HB = 16384, K4_WB = 64 * 224, K4_STAGE = K4_HB + 2 * K4_WB, K4_LDS = 3 * K4_STAGE;
template <int NP>
__device__ __forceinline__ void k4_compute(ldscp st, int wm, int np0, int lane, f32x4 (&ag)[4][2], f32x4 (&au)[4][2]) {
    const int g = lane >> 4, q = (lane & 15) >> 2, p = lane & 3;
    const ldscp hb = st + wm * 4096 + lane * 16;
    const ldscp wb = st + K4_HB + (4 * g + q) * 224 + np0 * 32 + p * 8;
#pragma unroll
    for (int s = 0; s < 2; ++s) {
        const bf16x8 b0 = ldsr16(hb + s * 1024), b1 = ldsr16(hb + 2048 + s * 1024);
#pragma unroll
        for (int n = 0; n < NP; ++n) {
            const bf16x8 a = cat8(trd(wb + s * 32 * 224 + n * 32), trd(wb + s * 32 * 224 + 16 * 224 + n * 32));
            ag[n][0] = MFMA16(a, b0, ag[n][0]); ag[n][1] = MFMA16(a, b1, ag[n][1]);
            const bf16x8 u = cat8(trd(wb + K4_WB + s * 32 * 224 + n * 32), trd(wb + K4_WB + s * 32 * 224 + 16 * 224 + n * 32));
            au[n][0] = MFMA16(u, b0, au[n][0]); au[n][1] = MFMA16(u, b1, au[n][1]);
        }
    }
}
__global__ void __launch_bounds__(512, 2) k_gateup(const u16* __restrict__ wg, const u16* __restrict__ wu, char* __restrict__ ws) {
    extern __shared__ __attribute__((aligned(16))) char smem[];
    const ldsp lds = (ldsp)smem; const unsigned lds0 = (unsigned)(uintptr_t)smem;
    const int tid = threadIdx.x, lane = tid & 63, w = __builtin_amdgcn_readfirstlane(tid >> 6);
    const int b = blockIdx.x;
    const int xc = b & 7, r = b >> 3;
    const int mt = (xc >> 1) * 4 + (r & 3), nt = (xc & 1) * 8 + (r >> 2);
    const int f0 = nt * 112;
    const int wm = w & 3, wn = w >> 2;
    const char* hsrc = ws + WS_HF + ((size_t)(mt * 8 + w) * 20) * 1024 + lane * 16;
    unsigned woff[4];
#pragma unroll
    for (int i = 0; i < 4; ++i) { int ins = w + 8 * i; if (ins >= 28) ins = w; const int m = ins >= 14 ? ins - 14 : ins; const int pz = m * 64 + lane, rr = pz / 14; int c = pz - rr * 14;
        int col = f0 + c * 8; if (col > F - 8) col = F - 8;
        woff[i] = (unsigned)(rr * (F * 2) + col * 2); }
#define K4_DMA(kc, so) do { glds16(hsrc + (kc) * 2048, lds0 + (so) + w * 2048); glds16(hsrc + (kc) * 2048 + 1024, lds0 + (so) + w * 2048 + 1024); \
        _Pragma("unroll") for (int i_ = 0; i_ < 4; ++i_) { const int ins_ = w + 8 * i_; if (ins_ < 28) glds16((const char*)(ins_ >= 14 ? wu : wg) + (size_t)(kc) * 64 * F * 2 + woff[i_], lds0 + (so) + K4_HB + ins_ * 1024); } } while (0)
    K4_DMA(0, 0); K4_DMA(1, K4_STAGE);
    f32x4 ag[4][2], au[4][2];
#pragma unroll
    for (int n = 0; n < 4; ++n) { ag[n][0] = ag[n][1] = au[n][0] = au[n][1] = f32x4{0.f, 0.f, 0.f, 0.f}; }
    int so = 0, sn = 2 * K4_STAGE;
    for (int kc = 0; kc < 10; ++kc) {
        if (kc < 9) { if (w < 4) WAITV(6); else WAITV(5); } else WAITV(0);
        BAR();
        if (kc + 2 < 10) K4_DMA(kc + 2, sn);
        if (wn == 0) k4_compute<4>(lds + so, wm, 0, lane, ag, au); else k4_compute<3>(lds + so, wm, 4, lane, ag, au);
        sn = so; so = so == 2 * K4_STAGE ? 0 : so + K4_STAGE;
    }
    const int g = lane >> 4;
    const float* st = (const float*)(ws + WS_ST); const float* cs = (const float*)(ws + WS_CS);
#pragma unroll
    for (int t = 0; t < 2; ++t) {
        const int tok = mt * 128 + wm * 32 + t * 16 + (lane & 15);
        const f32x4* sp = (const f32x4*)(st + (size_t)tok * 16);
        const f32x4 s0 = sp[0], s1 = sp[1], s2 = sp[2], s3 = sp[3];
        const float mean = (s0[0] + s0[2] + s1[0] + s1[2] + s2[0] + s2[2] + s3[0] + s3[2]) * 0.125f;
        float m2 = s0[1] + s0[3] + s1[1] + s1[3] + s2[1] + s2[3] + s3[1] + s3[3];
        { float d; d = s0[0] - mean; m2 += 80.f * d * d; d = s0[2] - mean; m2 += 80.f * d * d; d = s1[0] - mean; m2 += 80.f * d * d; d = s1[2] - mean; m2 += 80.f * d * d;
          d = s2[0] - mean; m2 += 80.f * d * d; d = s2[2] - mean; m2 += 80.f * d * d; d = s3[0] - mean; m2 += 80.f * d * d; d = s3[2] - mean; m2 += 80.f * d * d; }
        const float rstd = 1.0f / sqrtf(m2 * (1.f / D) + LN_EPS);
        char* hb = ws + WS_HH + (size_t)(tok >> 4) * 54 * 1024 + lane * 16;
#pragma unroll
        for (int n = 0; n < 4; ++n) { if (wn == 1 && n == 3) break;
            const int f = f0 + (wn * 4 + n) * 16; if (f >= F) break;
            const f32x4 cg = *(const f32x4*)(cs + f + 4 * g), cu = *(const f32x4*)(cs + F + f + 4 * g);
            float hv[4];
#pragma unroll
            for (int j = 0; j < 4; ++j) hv[j] = ((ag[n][t][j] - mean * cg[j]) * rstd) * ((au[n][t][j] - mean * cu[j]) * rstd);
            const u32x2 v = {pk2(hv[0], hv[1]), pk2(hv[2], hv[3])};
            *(u32x2*)(hb + (f >> 5) * 1024 + ((f >> 4) & 1) * 8) = v; }
    }
}

constexpr int K5_HALF = 96 * 160, K5_STAGE = 2 * K5_HALF, K5_NST = 4, K5_RED = K5_NST * K5_STAGE, K5_LDS = K5_RED + 4 * 5 * 1024;
__global__ void __launch_bounds__(512, 2) k_down(const u16* __restrict__ wd, char* __restrict__ ws, u16* __restrict__ out) {
    extern __shared__ __attribute__((aligned(16))) char smem[];
    const ldsp lds = (ldsp)smem; const unsigned lds0 = (unsigned)(uintptr_t)smem;
    const int tid = threadIdx.x, lane = tid & 63, w = __builtin_amdgcn_readfirstlane(tid >> 6);
    const int b = blockIdx.x, ng = (b >> 3) & 7, tt = (b & 7) + 8 * (b >> 6);
    const int tl = w & 3, kh = w >> 2, g = lane >> 4, q = (lane & 15) >> 2, p = lane & 3;
    unsigned woff[4];
#pragma unroll
    for (int i = 0; i < 4; ++i) { int ins = w + 8 * i; if (ins >= 30) ins = w; const int hh = ins >= 15, m = ins - 15 * hh, pz = m * 64 + lane, rr = pz / 10, c = pz - rr * 10;
        woff[i] = (unsigned)((hh * 864 + rr) * (D * 2) + ng * 160 + c * 16); }
#define K5_DMA(stg, so) do { _Pragma("unroll") for (int i_ = 0; i_ < 4; ++i_) { const int ins_ = w + 8 * i_; if (ins_ < 30) glds16((const char*)wd + (size_t)(stg) * 96 * D * 2 + woff[i_], lds0 + (so) + ins_ * 1024); } } while (0)
    const char* hsrc = ws + WS_HH + ((size_t)(tt * 4 + tl) * 54 + kh * 27) * 1024 + lane * 16;
    K5_DMA(0, 0); K5_DMA(1, K5_STAGE); K5_DMA(2, 2 * K5_STAGE);
    f32x4 acc[5];
#pragma unroll
    for (int n = 0; n < 5; ++n) acc[n] = f32x4{0.f, 0.f, 0.f, 0.f};
    bf16x8 bc[3], bn[3];
#pragma unroll
    for (int s = 0; s < 3; ++s) bc[s] = *(const bf16x8*)(hsrc + s * 1024);
    const int aoff = kh * K5_HALF + (4 * g + q) * 160 + p * 8;
#pragma unroll
    for (int st = 0; st < 9; ++st) {
        WAITV(0);
        BAR();
        if (st + 3 < 9) K5_DMA(st + 3, ((st + 3) & 3) * K5_STAGE);
        if (st + 1 < 9) {
#pragma unroll
            for (int s = 0; s < 3; ++s) bn[s] = *(const bf16x8*)(hsrc + ((st + 1) * 3 + s) * 1024); }
        const ldscp ab = lds + (st & 3) * K5_STAGE + aoff;
#pragma unroll
        for (int s = 0; s < 3; ++s)
#pragma unroll
            for (int n = 0; n < 5; ++n) acc[n] = MFMA16(cat8(trd(ab + s * 32 * 160 + n * 32), trd(ab + s * 32 * 160 + 16 * 160 + n * 32)), bc[s], acc[n]);
#pragma unroll
        for (int s = 0; s < 3; ++s) bc[s] = bn[s];
    }
    const ldsp red = lds + K5_RED + tl * 5 * 1024 + lane * 16;
    if (kh == 1) {
#pragma unroll
        for (int n = 0; n < 5; ++n) *(LAS f32x4*)(red + n * 1024) = acc[n]; }
    BAR();
    if (kh == 0) {
        const int tok = tt * 64 + tl * 16 + (lane & 15);
        const char* hb = ws + WS_HF + (size_t)(tt * 4 + tl) * 20 * 1024 + (lane & 15) * 16 + g * 256;
#pragma unroll
        for (int n = 0; n < 5; ++n) { const f32x4 a = acc[n] + *(const LAS f32x4*)(red + n * 1024);
            const int k = ng * 80 + n * 16; const u32x2 hv = *(const u32x2*)(hb + (k >> 5) * 1024 + ((k >> 4) & 1) * 8);
            const u32x2 v = {pk2(a[0] + bflo(hv[0]), a[1] + bfhi(hv[0])), pk2(a[2] + bflo(hv[1]), a[3] + bfhi(hv[1]))};
            *(u32x2*)(out + (size_t)tok * D + k + 4 * g) = v; }
    }
}

extern "C" void kernel_launch(void* const* d_in, const int* in_sizes, int n_in, void* d_out, int out_size, void* d_ws, size_t ws_size, hipStream_t stream) {
    const u16* x = (const u16*)d_in[0]; const u16* wq = (const u16*)d_in[1]; const u16* wk = (const u16*)d_in[2]; const u16* wv = (const u16*)d_in[3]; const u16* wo = (const u16*)d_in[4];
    const u16* wg = (const u16*)d_in[6]; const u16* wu = (const u16*)d_in[7]; const u16* wd = (const u16*)d_in[8];
    char* ws = (char*)d_ws; u16* out = (u16*)d_out;
    hipFuncSetAttribute((const void*)k_front, hipFuncAttributeMaxDynamicSharedMemorySize, K1_LDS);
    hipFuncSetAttribute((const void*)k_attn, hipFuncAttributeMaxDynamicSharedMemorySize, K2_LDS);
    hipFuncSetAttribute((const void*)k_oproj, hipFuncAttributeMaxDynamicSharedMemorySize, K3_LDS);
    hipFuncSetAttribute((const void*)k_gateup, hipFuncAttributeMaxDynamicSharedMemorySize, K4_LDS);
    hipFuncSetAttribute((const void*)k_down, hipFuncAttributeMaxDynamicSharedMemorySize, K5_LDS);
    k_front<<<256, 512, K1_LDS, stream>>>(x, wq, wk, wv, wg, wu, ws);
    k_attn<<<256, 512, K2_LDS, stream>>>(ws);
    k_oproj<<<256, 512, K3_LDS, stream>>>(x, wo, ws);
    k_gateup<<<256, 512, K4_LDS, stream>>>(wg, wu, ws);
    k_down<<<256, 512, K5_LDS, stream>>>(wd, ws, out);
}
```

```cpp
#include <hip/hip_runtime.h>
#include <hip/hip_bf16.h>
#include <stdint.h>

typedef __bf16 bf16x8 __attribute__((ext_vector_type(8)));
typedef __bf16 bf16x2 __attribute__((ext_vector_type(2)));
typedef short s16x4 __attribute__((ext_vector_type(4)));
typedef short s16x8 __attribute__((ext_vector_type(8)));
typedef float f32x2 __attribute__((ext_vector_type(2)));
typedef float f32x4 __attribute__((ext_vector_type(4)));
typedef float f32x16 __attribute__((ext_vector_type(16)));
typedef float f32x8 __attribute__((ext_vector_type(8)));
typedef unsigned u32x2 __attribute__((ext_vector_type(2)));
typedef unsigned u32x4 __attribute__((ext_vector_type(4)));
typedef unsigned u32x8 __attribute__((ext_vector_type(8)));
typedef unsigned short u16;
#define LAS __attribute__((address_space(3)))
typedef LAS char* ldsp;
typedef const LAS char* ldscp;

constexpr int S = 2048, D = 640, NH = 32, DH = 20, F = 1728;
constexpr float QSCALE = 0.22360679774997896f * 1.4426950408889634f;
constexpr float LN_EPS = 1e-5f;

constexpr size_t WS_FLG = 0;
constexpr size_t WS_KF = 65536;
constexpr size_t WS_VF = WS_KF + 68ull * 1536;
constexpr size_t WS_OF = WS_VF + 67ull * 2048;
constexpr size_t WS_HF = WS_OF + 128ull * 20 * 1024;
constexpr size_t WS_ST = WS_HF + 128ull * 20 * 1024;
constexpr size_t WS_HH = WS_ST + 2048ull * 8 * 8;
constexpr size_t WS_WGU = WS_HH + 128ull * 54 * 1024;
constexpr size_t WS_WD = WS_WGU + 112ull * 40 * 1024;
constexpr size_t WS_CS = WS_WD + 8ull * 2 * 27 * 5 * 1024;
constexpr size_t WS_END = WS_CS + 112ull * 2 * 16 * 20 * 4;
constexpr int FL_NONCE = 0, FL_KV = 1024, FL_RT = 4096, FL_O = 8192, FL_H = 16384, FL_HH = 24576;
typedef unsigned long long u64;
#define GAS __attribute__((address_space(1)))
typedef GAS u64 gu64;
#define RLX_AGENT __ATOMIC_RELAXED, __HIP_MEMORY_SCOPE_AGENT
constexpr int SC1 = 16;
__device__ __forceinline__ unsigned pk2(float lo, float hi) { f32x2 v = {lo, hi}; bf16x2 r = __builtin_convertvector(v, bf16x2); return __builtin_bit_cast(unsigned, r); }
__device__ __forceinline__ unsigned nos(unsigned u) { return u < 0xFFFFFFFEu ? u : 0xFFFFFFFEu; }
__device__ __forceinline__ unsigned pk2s(float lo, float hi) { return nos(pk2(lo, hi)); }
__device__ __forceinline__ float bflo(unsigned u) { return __uint_as_float(u << 16); }
__device__ __forceinline__ float bfhi(unsigned u) { return __uint_as_float(u & 0xffff0000u); }
__device__ __forceinline__ float dot2(unsigned a, unsigned b, float acc) { return __builtin_amdgcn_fdot2_f32_bf16(__builtin_bit_cast(bf16x2, a), __builtin_bit_cast(bf16x2, b), acc, false); }
__device__ __forceinline__ void glds16(const void* g, unsigned lds_base) {
    unsigned sv; asm volatile("s_mov_b32 %0, m0\n\ts_mov_b32 m0, %2\n\ts_nop 0\n\tglobal_load_lds_dwordx4 %1, off\n\ts_mov_b32 m0, %0" : "=&s"(sv) : "v"(g), "s"(lds_base) : "memory"); }
__device__ __forceinline__ void glds16s(const void* sbase, unsigned voff, unsigned lds_base) {
    unsigned sv; asm volatile("s_mov_b32 %0, m0\n\ts_mov_b32 m0, %3\n\ts_nop 0\n\tglobal_load_lds_dwordx4 %1, %2\n\ts_mov_b32 m0, %0" : "=&s"(sv) : "v"(voff), "s"(sbase), "s"(lds_base) : "memory"); }
typedef short v4i16_t __attribute__((ext_vector_type(4)));
__device__ __forceinline__ s16x4 trd(ldscp p) { return __builtin_bit_cast(s16x4, __builtin_amdgcn_ds_read_tr16_b64_v4i16((LAS v4i16_t*)p)); }
__device__ __forceinline__ bf16x8 cat8(s16x4 a, s16x4 b) { s16x8 v = {a[0], a[1], a[2], a[3], b[0], b[1], b[2], b[3]}; return __builtin_bit_cast(bf16x8, v); }
__device__ __forceinline__ bf16x8 ldsr16(ldscp p) { return *(const LAS bf16x8*)p; }
#define MFMA32(a, b, c) __builtin_amdgcn_mfma_f32_32x32x16_bf16(a, b, c, 0, 0, 0)
#define MFMA32K8(a, b, c) __builtin_amdgcn_mfma_f32_32x32x8bf16_1k(a, b, c, 0, 0, 0)
#define MFMA16(a, b, c) __builtin_amdgcn_mfma_f32_16x16x32_bf16(a, b, c, 0, 0, 0)
#define WAITV(N) asm volatile("s_waitcnt vmcnt(" #N ")" ::: "memory")
#define BAR() asm volatile("s_waitcnt lgkmcnt(0)\n\ts_barrier" ::: "memory")
template <int CTRL> __device__ __forceinline__ float dpp(float x) { return __builtin_bit_cast(float, __builtin_amdgcn_mov_dpp(__builtin_bit_cast(int, x), CTRL, 0xf, 0xf, true)); }
constexpr int XOR1 = 0xB1, XOR2 = 0x4E, XOR7 = 0x141, XOR8 = 0x128, ROR4 = 0x124;
__device__ __forceinline__ float x16_sum(float x) { auto s = __builtin_amdgcn_permlane16_swap(__float_as_uint(x), __float_as_uint(x), false, false); return __uint_as_float(s[0]) + __uint_as_float(s[1]); }
__device__ __forceinline__ float x32_sum(float x) { auto s = __builtin_amdgcn_permlane32_swap(__float_as_uint(x), __float_as_uint(x), false, false); return __uint_as_float(s[0]) + __uint_as_float(s[1]); }
__device__ __forceinline__ float x16_max(float x) { auto s = __builtin_amdgcn_permlane16_swap(__float_as_uint(x), __float_as_uint(x), false, false); return fmaxf(__uint_as_float(s[0]), __uint_as_float(s[1])); }
__device__ __forceinline__ float x32_max(float x) { auto s = __builtin_amdgcn_permlane32_swap(__float_as_uint(x), __float_as_uint(x), false, false); return fmaxf(__uint_as_float(s[0]), __uint_as_float(s[1])); }
__device__ __forceinline__ float row16_max(float x) { x = fmaxf(x, dpp<XOR1>(x)); x = fmaxf(x, dpp<XOR2>(x)); x = fmaxf(x, dpp<XOR7>(x)); return fmaxf(x, dpp<XOR8>(x)); }
__device__ __forceinline__ float row16_sum(float x) { x += dpp<XOR1>(x); x += dpp<XOR2>(x); x += dpp<XOR7>(x); return x + dpp<XOR8>(x); }
__device__ __forceinline__ float oct_sum(float x) { x += dpp<XOR1>(x); x += dpp<XOR2>(x); return x + dpp<XOR7>(x); }

__device__ __forceinline__ int fresh_lane() { int l; asm volatile("v_mbcnt_lo_u32_b32 %0, -1, 0\n\tv_mbcnt_hi_u32_b32 %0, -1, %0" : "=v"(l)); return l; }
#define STAMP(i)
#define DRAIN() asm volatile("s_waitcnt vmcnt(0)" ::: "memory")
#define BEHIND_POLL() __builtin_amdgcn_fence(__ATOMIC_ACQUIRE, "wavefront")
template <int REP, bool TWO = false>
__device__ __forceinline__ unsigned poll_flags(const char* base, int lane, int n, u64 tag) {
    const gu64* p = (const gu64*)base; unsigned mx = 0;
    if (TWO) lane = lane < 16 ? (lane >> 3) * 16 + (lane & 7) : 0;
    const u64 t0 = __builtin_amdgcn_s_memrealtime();
    for (;;) {
        bool ok = true; mx = 0;
#pragma unroll
        for (int i = 0; i < REP; ++i) { const int k = lane + 64 * i; const u64 v = __hip_atomic_load(p + (k < n ? k : 0), RLX_AGENT); ok &= (v >> 16) == tag; const unsigned lo = (unsigned)v & 0xffffu; mx = lo > mx ? lo : mx; }
        if (__all(ok)) break;
        if (__builtin_amdgcn_s_memrealtime() - t0 > 400000ull) break;
        __builtin_amdgcn_s_sleep(1);
    }
    BEHIND_POLL();
    return mx;
}
__device__ __forceinline__ void set_flag(char* p, u64 tag, unsigned lo) { __hip_atomic_store((gu64*)p, (tag << 16) | lo, RLX_AGENT); }

#define ATTN_PRO_ASM \
    "s_mov_b64 s[40:41], %[kb]\n\t" \
    "s_mov_b64 s[42:43], %[vb]\n\t" \
    "global_load_dwordx4 v[200:203], %[o16], s[40:41] sc1\n\t" \
    "global_load_dwordx2 v[216:217], %[o8], s[40:41] offset:1024 sc1\n\t" \
    "s_add_u32 s40, s40, 1536\n\t" \
    "s_addc_u32 s41, s41, 0\n\t" \
    "global_load_dwordx4 v[224:227], %[o16], s[42:43] sc1\n\t" \
    "global_load_dwordx4 v[228:231], %[o16], s[42:43] offset:1024 sc1\n\t" \
    "s_add_u32 s42, s42, 2048\n\t" \
    "s_addc_u32 s43, s43, 0\n\t" \
    "s_barrier\n\t" \
    "global_load_dwordx4 v[204:207], %[o16], s[40:41] sc1\n\t" \
    "global_load_dwordx2 v[218:219], %[o8], s[40:41] offset:1024 sc1\n\t" \
    "s_add_u32 s40, s40, 1536\n\t" \
    "s_addc_u32 s41, s41, 0\n\t" \
    "global_load_dwordx4 v[232:235], %[o16], s[42:43] sc1\n\t" \
    "global_load_dwordx4 v[236:239], %[o16], s[42:43] offset:1024 sc1\n\t" \
    "s_add_u32 s42, s42, 2048\n\t" \
    "s_addc_u32 s43, s43, 0\n\t" \
    "s_barrier\n\t" \
    "global_load_dwordx4 v[208:211], %[o16], s[40:41] sc1\n\t" \
    "global_load_dwordx2 v[220:221], %[o8], s[40:41] offset:1024 sc1\n\t" \
    "s_add_u32 s40, s40, 1536\n\t" \
    "s_addc_u32 s41, s41, 0\n\t" \
    "global_load_dwordx4 v[240:243], %[o16], s[42:43] sc1\n\t" \
    "global_load_dwordx4 v[244:247], %[o16], s[42:43] offset:1024 sc1\n\t" \
    "s_add_u32 s42, s42, 2048\n\t" \
    "s_addc_u32 s43, s43, 0\n\t" \
    "global_load_dwordx4 v[212:215], %[o16], s[40:41] sc1\n\t" \
    "global_load_dwordx2 v[222:223], %[o8], s[40:41] offset:1024 sc1\n\t" \
    "s_add_u32 s40, s40, 1536\n\t" \
    "s_addc_u32 s41, s41, 0\n\t" \
    "s_mov_b64 %[kb], s[40:41]\n\t" \
    "s_mov_b64 %[vb], s[42:43]\n\t"
#define ATTN_SEG0_ASM \
    "s_mov_b64 s[40:41], %[kb]\n\t" \
    "s_mov_b64 s[42:43], %[vb]\n\t" \
    "s_mov_b32 s54, m0\n\t" \
    "s_mov_b64 s[56:57], %[pf]\n\t" \
    "v_mov_b32 v152, -1\n\t" \
    "v_mov_b32 v153, -1\n\t" \
    "v_mov_b32 v154, -1\n\t" \
    "v_mov_b32 v155, -1\n\t" \
    "v_mov_b32 v188, 0\n\t" \
    "v_mov_b32 v189, 0\n\t" \
    "v_mov_b32 v190, 0\n\t" \
    "v_mov_b32 v191, 0\n\t" \
    "v_mov_b32 v252, 0\n\t" \
    "v_mov_b32 v253, 0\n\t" \
    "v_mov_b32 v254, 0\n\t" \
    "v_mov_b32 v255, 0\n\t" \
    "v_mov_b32 v196, 0\n\t" \
    "v_mov_b32 v197, 0\n\t" \
    "s_waitcnt vmcnt(12)\n\t" \
    "v_mfma_f32_32x32x8_bf16 v[160:175], v[216:217], %[qr0], 0\n\t" \
    "v_mfma_f32_32x32x16_bf16 v[160:175], v[200:203], %[qm0], v[160:175]\n\t" \
    "s_nop 15\n\t" \
    "s_nop 3\n\t" \
    "v_cvt_pk_bf16_f32 v198, v188, v189\n\t" \
    "v_cvt_pk_bf16_f32 v199, v190, v191\n\t" \
    "v_mfma_f32_32x32x8_bf16 v[176:191], v[216:217], %[qr1], 0\n\t" \
    "v_exp_f32 v160, v160\n\t" \
    "v_exp_f32 v161, v161\n\t" \
    "v_exp_f32 v162, v162\n\t" \
    "v_exp_f32 v163, v163\n\t" \
    "v_mfma_f32_32x32x16_bf16 %[o3], v[252:255], v[196:199], %[o3]\n\t" \
    "v_exp_f32 v164, v164\n\t" \
    "v_cvt_pk_bf16_f32 v192, v160, v161\n\t" \
    "v_exp_f32 v165, v165\n\t" \
    "v_cvt_pk_bf16_f32 v193, v162, v163\n\t" \
    "v_exp_f32 v166, v166\n\t" \
    "v_exp_f32 v167, v167\n\t" \
    "v_mfma_f32_32x32x16_bf16 v[176:191], v[200:203], %[qm1], v[176:191]\n\t" \
    "global_load_dwordx4 v[248:251], %[o16], s[42:43] sc1\n\t" \
    "global_load_dwordx4 v[252:255], %[o16], s[42:43] offset:1024 sc1\n\t" \
    "s_add_u32 s42, s42, 2048\n\t" \
    "s_addc_u32 s43, s43, 0\n\t" \
    "v_exp_f32 v168, v168\n\t" \
    "v_cvt_pk_bf16_f32 v194, v164, v165\n\t" \
    "v_exp_f32 v169, v169\n\t" \
    "v_cvt_pk_bf16_f32 v195, v166, v167\n\t" \
    "v_exp_f32 v170, v170\n\t" \
    "v_exp_f32 v171, v171\n\t" \
    "s_waitcnt vmcnt(12)\n\t" \
    "v_mfma_f32_32x32x16_bf16 %[o0], v[224:227], v[192:195], %[o0]\n\t" \
    "v_exp_f32 v172, v172\n\t" \
    "v_cvt_pk_bf16_f32 v196, v168, v169\n\t" \
    "v_exp_f32 v173, v173\n\t" \
    "v_cvt_pk_bf16_f32 v197, v170, v171\n\t" \
    "v_exp_f32 v174, v174\n\t" \
    "v_exp_f32 v175, v175\n\t" \
    "v_cvt_pk_bf16_f32 v198, v172, v173\n\t" \
    "v_cvt_pk_bf16_f32 v199, v174, v175\n\t" \
    "v_mfma_f32_32x32x8_bf16 v[160:175], v[216:217], %[qr2], 0\n\t" \
    "v_exp_f32 v176, v176\n\t" \
    "v_exp_f32 v177, v177\n\t" \
    "v_exp_f32 v178, v178\n\t" \
    "v_exp_f32 v179, v179\n\t" \
    "v_mfma_f32_32x32x16_bf16 %[o0], v[228:231], v[196:199], %[o0]\n\t" \
    "v_exp_f32 v180, v180\n\t" \
    "v_cvt_pk_bf16_f32 v192, v176, v177\n\t" \
    "v_exp_f32 v181, v181\n\t" \
    "v_cvt_pk_bf16_f32 v193, v178, v179\n\t" \
    "v_exp_f32 v182, v182\n\t" \
    "v_exp_f32 v183, v183\n\t" \
    "v_mfma_f32_32x32x16_bf16 v[160:175], v[200:203], %[qm2], v[160:175]\n\t" \
    "v_exp_f32 v184, v184\n\t" \
    "v_cvt_pk_bf16_f32 v194, v180, v181\n\t" \
    "v_exp_f32 v185, v185\n\t" \
    "v_cvt_pk_bf16_f32 v195, v182, v183\n\t" \
    "v_exp_f32 v186, v186\n\t" \
    "v_exp_f32 v187, v187\n\t" \
    "v_mfma_f32_32x32x16_bf16 %[o1], v[224:227], v[192:195], %[o1]\n\t" \
    "v_exp_f32 v188, v188\n\t" \
    "v_cvt_pk_bf16_f32 v196, v184, v185\n\t" \
    "v_exp_f32 v189, v189\n\t" \
    "v_cvt_pk_bf16_f32 v197, v186, v187\n\t" \
    "v_exp_f32 v190, v190\n\t" \
    "v_exp_f32 v191, v191\n\t" \
    "v_cvt_pk_bf16_f32 v198, v188, v189\n\t" \
    "v_cvt_pk_bf16_f32 v199, v190, v191\n\t" \
    "v_mfma_f32_32x32x8_bf16 v[176:191], v[216:217], %[qr3], 0\n\t" \
    "v_exp_f32 v160, v160\n\t" \
    "v_exp_f32 v161, v161\n\t" \
    "v_exp_f32 v162, v162\n\t" \
    "v_exp_f32 v163, v163\n\t" \
    "v_mfma_f32_32x32x16_bf16 %[o1], v[228:231], v[196:199], %[o1]\n\t" \
    "v_exp_f32 v164, v164\n\t" \
    "v_cvt_pk_bf16_f32 v192, v160, v161\n\t" \
    "v_exp_f32 v165, v165\n\t" \
    "v_cvt_pk_bf16_f32 v193, v162, v163\n\t" \
    "v_exp_f32 v166, v166\n\t" \
    "v_exp_f32 v167, v167\n\t" \
    "v_mfma_f32_32x32x16_bf16 v[176:191], v[200:203], %[qm3], v[176:191]\n\t" \
    "v_exp_f32 v168, v168\n\t" \
    "v_cvt_pk_bf16_f32 v194, v164, v165\n\t" \
    "v_exp_f32 v169, v169\n\t" \
    "v_cvt_pk_bf16_f32 v195, v166, v167\n\t" \
    "v_exp_f32 v170, v170\n\t" \
    "v_exp_f32 v171, v171\n\t" \
    "v_mfma_f32_32x32x16_bf16 %[o2], v[224:227], v[192:195], %[o2]\n\t" \
    "v_exp_f32 v172, v172\n\t" \
    "v_cvt_pk_bf16_f32 v196, v168, v169\n\t" \
    "v_exp_f32 v173, v173\n\t" \
    "v_cvt_pk_bf16_f32 v197, v170, v171\n\t" \
    "v_exp_f32 v174, v174\n\t" \
    "v_exp_f32 v175, v175\n\t" \
    "v_cvt_pk_bf16_f32 v198, v172, v173\n\t" \
    "v_cvt_pk_bf16_f32 v199, v174, v175\n\t" \
    "s_waitcnt vmcnt(10)\n\t" \
    "v_mfma_f32_32x32x8_bf16 v[160:175], v[218:219], %[qr0], 0\n\t" \
    "v_exp_f32 v176, v176\n\t" \
    "v_exp_f32 v177, v177\n\t" \
    "v_exp_f32 v178, v178\n\t" \
    "v_exp_f32 v179, v179\n\t" \
    "v_mfma_f32_32x32x16_bf16 %[o2], v[228:231], v[196:199], %[o2]\n\t" \
    "v_exp_f32 v180, v180\n\t" \
    "v_cvt_pk_bf16_f32 v192, v176, v177\n\t" \
    "v_exp_f32 v181, v181\n\t" \
    "v_cvt_pk_bf16_f32 v193, v178, v179\n\t" \
    "v_exp_f32 v182, v182\n\t" \
    "v_exp_f32 v183, v183\n\t" \
    "v_mfma_f32_32x32x16_bf16 v[160:175], v[204:207], %[qm0], v[160:175]\n\t" \
    "global_load_dwordx4 v[200:203], %[o16], s[40:41] sc1\n\t" \
    "global_load_dwordx2 v[216:217], %[o8], s[40:41] offset:1024 sc1\n\t" \
    "s_add_u32 s40, s40, 1536\n\t" \
    "s_addc_u32 s41, s41, 0\n\t" \
    "v_exp_f32 v184, v184\n\t" \
    "v_cvt_pk_bf16_f32 v194, v180, v181\n\t" \
    "v_exp_f32 v185, v185\n\t" \
    "v_cvt_pk_bf16_f32 v195, v182, v183\n\t" \
    "v_exp_f32 v186, v186\n\t" \
    "v_exp_f32 v187, v187\n\t" \
    "v_mfma_f32_32x32x16_bf16 %[o3], v[224:227], v[192:195], %[o3]\n\t" \
    "v_exp_f32 v188, v188\n\t" \
    "v_cvt_pk_bf16_f32 v196, v184, v185\n\t" \
    "v_exp_f32 v189, v189\n\t" \
    "v_cvt_pk_bf16_f32 v197, v186, v187\n\t" \
    "v_exp_f32 v190, v190\n\t" \
    "v_exp_f32 v191, v191\n\t" \
    "v_cvt_pk_bf16_f32 v198, v188, v189\n\t" \
    "v_cvt_pk_bf16_f32 v199, v190, v191\n\t" \
    "v_mfma_f32_32x32x8_bf16 v[176:191], v[218:219], %[qr1], 0\n\t" \
    "v_exp_f32 v160, v160\n\t" \
    "v_exp_f32 v161, v161\n\t" \
    "v_exp_f32 v162, v162\n\t" \
    "v_exp_f32 v163, v163\n\t" \
    "v_mfma_f32_32x32x16_bf16 %[o3], v[228:231], v[196:199], %[o3]\n\t" \
    "v_exp_f32 v164, v164\n\t" \
    "v_cvt_pk_bf16_f32 v192, v160, v161\n\t" \
    "v_exp_f32 v165, v165\n\t" \
    "v_cvt_pk_bf16_f32 v193, v162, v163\n\t" \
    "v_exp_f32 v166, v166\n\t" \
    "v_exp_f32 v167, v167\n\t" \
    "v_mfma_f32_32x32x16_bf16 v[176:191], v[204:207], %[qm1], v[176:191]\n\t" \
    "global_load_dwordx4 v[224:227], %[o16], s[42:43] sc1\n\t" \
    "global_load_dwordx4 v[228:231], %[o16], s[42:43] offset:1024 sc1\n\t" \
    "s_add_u32 s42, s42, 2048\n\t" \
    "s_addc_u32 s43, s43, 0\n\t" \
    "v_exp_f32 v168, v168\n\t" \
    "v_cvt_pk_bf16_f32 v194, v164, v165\n\t" \
    "v_exp_f32 v169, v169\n\t" \
    "v_cvt_pk_bf16_f32 v195, v166, v167\n\t" \
    "v_exp_f32 v170, v170\n\t" \
    "v_exp_f32 v171, v171\n\t" \
    "s_waitcnt vmcnt(12)\n\t" \
    "v_mfma_f32_32x32x16_bf16 %[o0], v[232:235], v[192:195], %[o0]\n\t" \
    "v_exp_f32 v172, v172\n\t" \
    "v_cvt_pk_bf16_f32 v196, v168, v169\n\t" \
    "v_exp_f32 v173, v173\n\t" \
    "v_cvt_pk_bf16_f32 v197, v170, v171\n\t" \
    "v_exp_f32 v174, v174\n\t" \
    "v_exp_f32 v175, v175\n\t" \
    "v_cvt_pk_bf16_f32 v198, v172, v173\n\t" \
    "v_cvt_pk_bf16_f32 v199, v174, v175\n\t" \
    "v_mfma_f32_32x32x8_bf16 v[160:175], v[218:219], %[qr2], 0\n\t" \
    "v_exp_f32 v176, v176\n\t" \
    "v_exp_f32 v177, v177\n\t" \
    "v_exp_f32 v178, v178\n\t" \
    "v_exp_f32 v179, v179\n\t" \
    "v_mfma_f32_32x32x16_bf16 %[o0], v[236:239], v[196:199], %[o0]\n\t" \
    "v_exp_f32 v180, v180\n\t" \
    "v_cvt_pk_bf16_f32 v192, v176, v177\n\t" \
    "v_exp_f32 v181, v181\n\t" \
    "v_cvt_pk_bf16_f32 v193, v178, v179\n\t" \
    "v_exp_f32 v182, v182\n\t" \
    "v_exp_f32 v183, v183\n\t" \
    "v_mfma_f32_32x32x16_bf16 v[160:175], v[204:207], %[qm2], v[160:175]\n\t" \
    "v_exp_f32 v184, v184\n\t" \
    "v_cvt_pk_bf16_f32 v194, v180, v181\n\t" \
    "v_exp_f32 v185, v185\n\t" \
    "v_cvt_pk_bf16_f32 v195, v182, v183\n\t" \
    "v_exp_f32 v186, v186\n\t" \
    "v_exp_f32 v187, v187\n\t" \
    "v_mfma_f32_32x32x16_bf16 %[o1], v[232:235], v[192:195], %[o1]\n\t" \
    "v_exp_f32 v188, v188\n\t" \
    "v_cvt_pk_bf16_f32 v196, v184, v185\n\t" \
    "v_exp_f32 v189, v189\n\t" \
    "v_cvt_pk_bf16_f32 v197, v186, v187\n\t" \
    "v_exp_f32 v190, v190\n\t" \
    "v_exp_f32 v191, v191\n\t" \
    "v_cvt_pk_bf16_f32 v198, v188, v189\n\t" \
    "v_cvt_pk_bf16_f32 v199, v190, v191\n\t" \
    "v_mfma_f32_32x32x8_bf16 v[176:191], v[218:219], %[qr3], 0\n\t" \
    "v_exp_f32 v160, v160\n\t" \
    "v_exp_f32 v161, v161\n\t" \
    "v_exp_f32 v162, v162\n\t" \
    "v_exp_f32 v163, v163\n\t" \
    "v_mfma_f32_32x32x16_bf16 %[o1], v[236:239], v[196:199], %[o1]\n\t" \
    "v_exp_f32 v164, v164\n\t" \
    "v_cvt_pk_bf16_f32 v192, v160, v161\n\t" \
    "v_exp_f32 v165, v165\n\t" \
    "v_cvt_pk_bf16_f32 v193, v162, v163\n\t" \
    "v_exp_f32 v166, v166\n\t" \
    "v_exp_f32 v167, v167\n\t" \
    "v_mfma_f32_32x32x16_bf16 v[176:191], v[204:207], %[qm3], v[176:191]\n\t" \
    "v_exp_f32 v168, v168\n\t" \
    "v_cvt_pk_bf16_f32 v194, v164, v165\n\t" \
    "v_exp_f32 v169, v169\n\t" \
    "v_cvt_pk_bf16_f32 v195, v166, v167\n\t" \
    "v_exp_f32 v170, v170\n\t" \
    "v_exp_f32 v171, v171\n\t" \
    "v_mfma_f32_32x32x16_bf16 %[o2], v[232:235], v[192:195], %[o2]\n\t" \
    "v_exp_f32 v172, v172\n\t" \
    "v_cvt_pk_bf16_f32 v196, v168, v169\n\t" \
    "v_exp_f32 v173, v173\n\t" \
    "v_cvt_pk_bf16_f32 v197, v170, v171\n\t" \
    "v_exp_f32 v174, v174\n\t" \
    "v_exp_f32 v175, v175\n\t" \
    "v_cvt_pk_bf16_f32 v198, v172, v173\n\t" \
    "v_cvt_pk_bf16_f32 v199, v174, v175\n\t" \
    "s_waitcnt vmcnt(10)\n\t" \
    "v_mfma_f32_32x32x8_bf16 v[160:175], v[220:221], %[qr0], 0\n\t" \
    "v_exp_f32 v176, v176\n\t" \
    "v_exp_f32 v177, v177\n\t" \
    "v_exp_f32 v178, v178\n\t" \
    "v_exp_f32 v179, v179\n\t" \
    "v_mfma_f32_32x32x16_bf16 %[o2], v[236:239], v[196:199], %[o2]\n\t" \
    "v_exp_f32 v180, v180\n\t" \
    "v_cvt_pk_bf16_f32 v192, v176, v177\n\t" \
    "v_exp_f32 v181, v181\n\t" \
    "v_cvt_pk_bf16_f32 v193, v178, v179\n\t" \
    "v_exp_f32 v182, v182\n\t" \
    "v_exp_f32 v183, v183\n\t" \
    "v_mfma_f32_32x32x16_bf16 v[160:175], v[208:211], %[qm0], v[160:175]\n\t" \
    "global_load_dwordx4 v[204:207], %[o16], s[40:41] sc1\n\t" \
    "global_load_dwordx2 v[218:219], %[o8], s[40:41] offset:1024 sc1\n\t" \
    "s_add_u32 s40, s40, 1536\n\t" \
    "s_addc_u32 s41, s41, 0\n\t" \
    "global_store_dwordx4 %[o16], v[152:155], s[56:57] offset:0 sc1\n\t" \
    "v_exp_f32 v184, v184\n\t" \
    "v_cvt_pk_bf16_f32 v194, v180, v181\n\t" \
    "v_exp_f32 v185, v185\n\t" \
    "v_cvt_pk_bf16_f32 v195, v182, v183\n\t" \
    "v_exp_f32 v186, v186\n\t" \
    "v_exp_f32 v187, v187\n\t" \
    "v_mfma_f32_32x32x16_bf16 %[o3], v[232:235], v[192:195], %[o3]\n\t" \
    "v_exp_f32 v188, v188\n\t" \
    "v_cvt_pk_bf16_f32 v196, v184, v185\n\t" \
    "v_exp_f32 v189, v189\n\t" \
    "v_cvt_pk_bf16_f32 v197, v186, v187\n\t" \
    "v_exp_f32 v190, v190\n\t" \
    "v_exp_f32 v191, v191\n\t" \
    "v_cvt_pk_bf16_f32 v198, v188, v189\n\t" \
    "v_cvt_pk_bf16_f32 v199, v190, v191\n\t" \
    "v_mfma_f32_32x32x8_bf16 v[176:191], v[220:221], %[qr1], 0\n\t" \
    "v_exp_f32 v160, v160\n\t" \
    "v_exp_f32 v161, v161\n\t" \
    "v_exp_f32 v162, v162\n\t" \
    "v_exp_f32 v163, v163\n\t" \
    "v_mfma_f32_32x32x16_bf16 %[o3], v[236:239], v[196:199], %[o3]\n\t" \
    "v_exp_f32 v164, v164\n\t" \
    "v_cvt_pk_bf16_f32 v192, v160, v161\n\t" \
    "v_exp_f32 v165, v165\n\t" \
    "v_cvt_pk_bf16_f32 v193, v162, v163\n\t" \
    "v_exp_f32 v166, v166\n\t" \
    "v_exp_f32 v167, v167\n\t" \
    "v_mfma_f32_32x32x16_bf16 v[176:191], v[208:211], %[qm1], v[176:191]\n\t" \
    "global_load_dwordx4 v[232:235], %[o16], s[42:43] sc1\n\t" \
    "global_load_dwordx4 v[236:239], %[o16], s[42:43] offset:1024 sc1\n\t" \
    "s_add_u32 s42, s42, 2048\n\t" \
    "s_addc_u32 s43, s43, 0\n\t" \
    "v_exp_f32 v168, v168\n\t" \
    "v_cvt_pk_bf16_f32 v194, v164, v165\n\t" \
    "v_exp_f32 v169, v169\n\t" \
    "v_cvt_pk_bf16_f32 v195, v166, v167\n\t" \
    "v_exp_f32 v170, v170\n\t" \
    "v_exp_f32 v171, v171\n\t" \
    "s_waitcnt vmcnt(13)\n\t" \
    "v_mfma_f32_32x32x16_bf16 %[o0], v[240:243], v[192:195], %[o0]\n\t" \
    "v_exp_f32 v172, v172\n\t" \
    "v_cvt_pk_bf16_f32 v196, v168, v169\n\t" \
    "v_exp_f32 v173, v173\n\t" \
    "v_cvt_pk_bf16_f32 v197, v170, v171\n\t" \
    "v_exp_f32 v174, v174\n\t" \
    "v_exp_f32 v175, v175\n\t" \
    "v_cvt_pk_bf16_f32 v198, v172, v173\n\t" \
    "v_cvt_pk_bf16_f32 v199, v174, v175\n\t" \
    "v_mfma_f32_32x32x8_bf16 v[160:175], v[220:221], %[qr2], 0\n\t" \
    "v_exp_f32 v176, v176\n\t" \
    "v_exp_f32 v177, v177\n\t" \
    "v_exp_f32 v178, v178\n\t" \
    "v_exp_f32 v179, v179\n\t" \
    "v_mfma_f32_32x32x16_bf16 %[o0], v[244:247], v[196:199], %[o0]\n\t" \
    "v_exp_f32 v180, v180\n\t" \
    "v_cvt_pk_bf16_f32 v192, v176, v177\n\t" \
    "v_exp_f32 v181, v181\n\t" \
    "v_cvt_pk_bf16_f32 v193, v178, v179\n\t" \
    "v_exp_f32 v182, v182\n\t" \
    "v_exp_f32 v183, v183\n\t" \
    "v_mfma_f32_32x32x16_bf16 v[160:175], v[208:211], %[qm2], v[160:175]\n\t" \
    "v_exp_f32 v184, v184\n\t" \
    "v_cvt_pk_bf16_f32 v194, v180, v181\n\t" \
    "v_exp_f32 v185, v185\n\t" \
    "v_cvt_pk_bf16_f32 v195, v182, v183\n\t" \
    "v_exp_f32 v186, v186\n\t" \
    "v_exp_f32 v187, v187\n\t" \
    "v_mfma_f32_32x32x16_bf16 %[o1], v[240:243], v[192:195], %[o1]\n\t" \
    "v_exp_f32 v188, v188\n\t" \
    "v_cvt_pk_bf16_f32 v196, v184, v185\n\t" \
    "v_exp_f32 v189, v189\n\t" \
    "v_cvt_pk_bf16_f32 v197, v186, v187\n\t" \
    "v_exp_f32 v190, v190\n\t" \
    "v_exp_f32 v191, v191\n\t" \
    "v_cvt_pk_bf16_f32 v198, v188, v189\n\t" \
    "v_cvt_pk_bf16_f32 v199, v190, v191\n\t" \
    "v_mfma_f32_32x32x8_bf16 v[176:191], v[220:221], %[qr3], 0\n\t" \
    "v_exp_f32 v160, v160\n\t" \
    "v_exp_f32 v161, v161\n\t" \
    "v_exp_f32 v162, v162\n\t" \
    "v_exp_f32 v163, v163\n\t" \
    "v_mfma_f32_32x32x16_bf16 %[o1], v[244:247], v[196:199], %[o1]\n\t" \
    "v_exp_f32 v164, v164\n\t" \
    "v_cvt_pk_bf16_f32 v192, v160, v161\n\t" \
    "v_exp_f32 v165, v165\n\t" \
    "v_cvt_pk_bf16_f32 v193, v162, v163\n\t" \
    "v_exp_f32 v166, v166\n\t" \
    "v_exp_f32 v167, v167\n\t" \
    "v_mfma_f32_32x32x16_bf16 v[176:191], v[208:211], %[qm3], v[176:191]\n\t" \
    "v_exp_f32 v168, v168\n\t" \
    "v_cvt_pk_bf16_f32 v194, v164, v165\n\t" \
    "v_exp_f32 v169, v169\n\t" \
    "v_cvt_pk_bf16_f32 v195, v166, v167\n\t" \
    "v_exp_f32 v170, v170\n\t" \
    "v_exp_f32 v171, v171\n\t" \
    "v_mfma_f32_32x32x16_bf16 %[o2], v[240:243], v[192:195], %[o2]\n\t" \
    "v_exp_f32 v172, v172\n\t" \
    "v_cvt_pk_bf16_f32 v196, v168, v169\n\t" \
    "v_exp_f32 v173, v173\n\t" \
    "v_cvt_pk_bf16_f32 v197, v170, v171\n\t" \
    "v_exp_f32 v174, v174\n\t" \
    "v_exp_f32 v175, v175\n\t" \
    "v_cvt_pk_bf16_f32 v198, v172, v173\n\t" \
    "v_cvt_pk_bf16_f32 v199, v174, v175\n\t" \
    "s_waitcnt vmcnt(11)\n\t" \
    "v_mfma_f32_32x32x8_bf16 v[160:175], v[222:223], %[qr0], 0\n\t" \
    "v_exp_f32 v176, v176\n\t" \
    "v_exp_f32 v177, v177\n\t" \
    "v_exp_f32 v178, v178\n\t" \
    "v_exp_f32 v179, v179\n\t" \
    "v_mfma_f32_32x32x16_bf16 %[o2], v[244:247], v[196:199], %[o2]\n\t" \
    "v_exp_f32 v180, v180\n\t" \
    "v_cvt_pk_bf16_f32 v192, v176, v177\n\t" \
    "v_exp_f32 v181, v181\n\t" \
    "v_cvt_pk_bf16_f32 v193, v178, v179\n\t" \
    "v_exp_f32 v182, v182\n\t" \
    "v_exp_f32 v183, v183\n\t" \
    "v_mfma_f32_32x32x16_bf16 v[160:175], v[212:215], %[qm0], v[160:175]\n\t" \
    "global_load_dwordx4 v[208:211], %[o16], s[40:41] sc1\n\t" \
    "global_load_dwordx2 v[220:221], %[o8], s[40:41] offset:1024 sc1\n\t" \
    "s_add_u32 s40, s40, 1536\n\t" \
    "s_addc_u32 s41, s41, 0\n\t" \
    "global_store_dwordx4 %[o16], v[152:155], s[56:57] offset:1024 sc1\n\t" \
    "v_exp_f32 v184, v184\n\t" \
    "v_cvt_pk_bf16_f32 v194, v180, v181\n\t" \
    "v_exp_f32 v185, v185\n\t" \
    "v_cvt_pk_bf16_f32 v195, v182, v183\n\t" \
    "v_exp_f32 v186, v186\n\t" \
    "v_exp_f32 v187, v187\n\t" \
    "v_mfma_f32_32x32x16_bf16 %[o3], v[240:243], v[192:195], %[o3]\n\t" \
    "v_exp_f32 v188, v188\n\t" \
    "v_cvt_pk_bf16_f32 v196, v184, v185\n\t" \
    "v_exp_f32 v189, v189\n\t" \
    "v_cvt_pk_bf16_f32 v197, v186, v187\n\t" \
    "v_exp_f32 v190, v190\n\t" \
    "v_exp_f32 v191, v191\n\t" \
    "v_cvt_pk_bf16_f32 v198, v188, v189\n\t" \
    "v_cvt_pk_bf16_f32 v199, v190, v191\n\t" \
    "v_mfma_f32_32x32x8_bf16 v[176:191], v[222:223], %[qr1], 0\n\t" \
    "v_exp_f32 v160, v160\n\t" \
    "v_exp_f32 v161, v161\n\t" \
    "v_exp_f32 v162, v162\n\t" \
    "v_exp_f32 v163, v163\n\t" \
    "v_mfma_f32_32x32x16_bf16 %[o3], v[244:247], v[196:199], %[o3]\n\t" \
    "v_exp_f32 v164, v164\n\t" \
    "v_cvt_pk_bf16_f32 v192, v160, v161\n\t" \
    "v_exp_f32 v165, v165\n\t" \
    "v_cvt_pk_bf16_f32 v193, v162, v163\n\t" \
    "v_exp_f32 v166, v166\n\t" \
    "v_exp_f32 v167, v167\n\t" \
    "v_mfma_f32_32x32x16_bf16 v[176:191], v[212:215], %[qm1], v[176:191]\n\t" \
    "global_load_dwordx4 v[240:243], %[o16], s[42:43] sc1\n\t" \
    "global_load_dwordx4 v[244:247], %[o16], s[42:43] offset:1024 sc1\n\t" \
    "s_add_u32 s42, s42, 2048\n\t" \
    "s_addc_u32 s43, s43, 0\n\t" \
    "v_exp_f32 v168, v168\n\t" \
    "v_cvt_pk_bf16_f32 v194, v164, v165\n\t" \
    "v_exp_f32 v169, v169\n\t" \
    "v_cvt_pk_bf16_f32 v195, v166, v167\n\t" \
    "v_exp_f32 v170, v170\n\t" \
    "v_exp_f32 v171, v171\n\t" \
    "s_waitcnt vmcnt(14)\n\t" \
    "v_mfma_f32_32x32x16_bf16 %[o0], v[248:251], v[192:195], %[o0]\n\t" \
    "v_exp_f32 v172, v172\n\t" \
    "v_cvt_pk_bf16_f32 v196, v168, v169\n\t" \
    "v_exp_f32 v173, v173\n\t" \
    "v_cvt_pk_bf16_f32 v197, v170, v171\n\t" \
    "v_exp_f32 v174, v174\n\t" \
    "v_exp_f32 v175, v175\n\t" \
    "v_cvt_pk_bf16_f32 v198, v172, v173\n\t" \
    "v_cvt_pk_bf16_f32 v199, v174, v175\n\t" \
    "v_mfma_f32_32x32x8_bf16 v[160:175], v[222:223], %[qr2], 0\n\t" \
    "v_exp_f32 v176, v176\n\t" \
    "v_exp_f32 v177, v177\n\t" \
    "v_exp_f32 v178, v178\n\t" \
    "v_exp_f32 v179, v179\n\t" \
    "v_mfma_f32_32x32x16_bf16 %[o0], v[252:255], v[196:199], %[o0]\n\t" \
    "v_exp_f32 v180, v180\n\t" \
    "v_cvt_pk_bf16_f32 v192, v176, v177\n\t" \
    "v_exp_f32 v181, v181\n\t" \
    "v_cvt_pk_bf16_f32 v193, v178, v179\n\t" \
    "v_exp_f32 v182, v182\n\t" \
    "v_exp_f32 v183, v183\n\t" \
    "v_mfma_f32_32x32x16_bf16 v[160:175], v[212:215], %[qm2], v[160:175]\n\t" \
    "v_exp_f32 v184, v184\n\t" \
    "v_cvt_pk_bf16_f32 v194, v180, v181\n\t" \
    "v_exp_f32 v185, v185\n\t" \
    "v_cvt_pk_bf16_f32 v195, v182, v183\n\t" \
    "v_exp_f32 v186, v186\n\t" \
    "v_exp_f32 v187, v187\n\t" \
    "v_mfma_f32_32x32x16_bf16 %[o1], v[248:251], v[192:195], %[o1]\n\t" \
    "v_exp_f32 v188, v188\n\t" \
    "v_cvt_pk_bf16_f32 v196, v184, v185\n\t" \
    "v_exp_f32 v189, v189\n\t" \
    "v_cvt_pk_bf16_f32 v197, v186, v187\n\t" \
    "v_exp_f32 v190, v190\n\t" \
    "v_exp_f32 v191, v191\n\t" \
    "v_cvt_pk_bf16_f32 v198, v188, v189\n\t" \
    "v_cvt_pk_bf16_f32 v199, v190, v191\n\t" \
    "v_mfma_f32_32x32x8_bf16 v[176:191], v[222:223], %[qr3], 0\n\t" \
    "v_exp_f32 v160, v160\n\t" \
    "v_exp_f32 v161, v161\n\t" \
    "v_exp_f32 v162, v162\n\t" \
    "v_exp_f32 v163, v163\n\t" \
    "v_mfma_f32_32x32x16_bf16 %[o1], v[252:255], v[196:199], %[o1]\n\t" \
    "v_exp_f32 v164, v164\n\t" \
    "v_cvt_pk_bf16_f32 v192, v160, v161\n\t" \
    "v_exp_f32 v165, v165\n\t" \
    "v_cvt_pk_bf16_f32 v193, v162, v163\n\t" \
    "v_exp_f32 v166, v166\n\t" \
    "v_exp_f32 v167, v167\n\t" \
    "v_mfma_f32_32x32x16_bf16 v[176:191], v[212:215], %[qm3], v[176:191]\n\t" \
    "v_exp_f32 v168, v168\n\t" \
    "v_cvt_pk_bf16_f32 v194, v164, v165\n\t" \
    "v_exp_f32 v169, v169\n\t" \
    "v_cvt_pk_bf16_f32 v195, v166, v167\n\t" \
    "v_exp_f32 v170, v170\n\t" \
    "v_exp_f32 v171, v171\n\t" \
    "v_mfma_f32_32x32x16_bf16 %[o2], v[248:251], v[192:195], %[o2]\n\t" \
    "v_exp_f32 v172, v172\n\t" \
    "v_cvt_pk_bf16_f32 v196, v168, v169\n\t" \
    "v_exp_f32 v173, v173\n\t" \
    "v_cvt_pk_bf16_f32 v197, v170, v171\n\t" \
    "v_exp_f32 v174, v174\n\t" \
    "v_exp_f32 v175, v175\n\t" \
    "v_cvt_pk_bf16_f32 v198, v172, v173\n\t" \
    "v_cvt_pk_bf16_f32 v199, v174, v175\n\t" \
    "s_waitcnt vmcnt(12)\n\t" \
    "v_mfma_f32_32x32x8_bf16 v[160:175], v[216:217], %[qr0], 0\n\t" \
    "v_exp_f32 v176, v176\n\t" \
    "v_exp_f32 v177, v177\n\t" \
    "v_exp_f32 v178, v178\n\t" \
    "v_exp_f32 v179, v179\n\t" \
    "v_mfma_f32_32x32x16_bf16 %[o2], v[252:255], v[196:199], %[o2]\n\t" \
    "v_exp_f32 v180, v180\n\t" \
    "v_cvt_pk_bf16_f32 v192, v176, v177\n\t" \
    "v_exp_f32 v181, v181\n\t" \
    "v_cvt_pk_bf16_f32 v193, v178, v179\n\t" \
    "v_exp_f32 v182, v182\n\t" \
    "v_exp_f32 v183, v183\n\t" \
    "v_mfma_f32_32x32x16_bf16 v[160:175], v[200:203], %[qm0], v[160:175]\n\t" \
    "global_load_dwordx4 v[212:215], %[o16], s[40:41] sc1\n\t" \
    "global_load_dwordx2 v[222:223], %[o8], s[40:41] offset:1024 sc1\n\t" \
    "s_add_u32 s40, s40, 1536\n\t" \
    "s_addc_u32 s41, s41, 0\n\t" \
    "global_store_dwordx4 %[o16], v[152:155], s[56:57] offset:2048 sc1\n\t" \
    "v_exp_f32 v184, v184\n\t" \
    "v_cvt_pk_bf16_f32 v194, v180, v181\n\t" \
    "v_exp_f32 v185, v185\n\t" \
    "v_cvt_pk_bf16_f32 v195, v182, v183\n\t" \
    "v_exp_f32 v186, v186\n\t" \
    "v_exp_f32 v187, v187\n\t" \
    "v_mfma_f32_32x32x16_bf16 %[o3], v[248:251], v[192:195], %[o3]\n\t" \
    "v_exp_f32 v188, v188\n\t" \
    "v_cvt_pk_bf16_f32 v196, v184, v185\n\t" \
    "v_exp_f32 v189, v189\n\t" \
    "v_cvt_pk_bf16_f32 v197, v186, v187\n\t" \
    "v_exp_f32 v190, v190\n\t" \
    "v_exp_f32 v191, v191\n\t" \
    "s_mov_b32 m0, s54\n\t" \
    "s_mov_b64 %[kb], s[40:41]\n\t" \
    "s_mov_b64 %[vb], s[42:43]\n\t"
#define ATTN_SEG1_ASM \
    "s_mov_b64 s[40:41], %[kb]\n\t" \
    "s_mov_b64 s[42:43], %[vb]\n\t" \
    "s_mov_b32 s54, m0\n\t" \
    "s_mov_b64 s[58:59], %[rs]\n\t" \
    "s_mov_b32 s46, %[rm]\n\t" \
    "s_mov_b64 s[56:57], %[pf]\n\t" \
    "s_add_u32 s56, s56, 3072\n\t" \
    "s_addc_u32 s57, s57, 0\n\t" \
    "v_mov_b32 v152, -1\n\t" \
    "v_mov_b32 v153, -1\n\t" \
    "v_mov_b32 v154, -1\n\t" \
    "v_mov_b32 v155, -1\n\t" \
    "s_mov_b32 s47, %[wr]\n\t" \
    "s_mov_b64 s[50:51], %[wo]\n\t" \
    "s_mov_b32 s49, s50\n\t" \
    "s_mov_b32 s50, s51\n\t" \
    "s_mov_b32 s52, -1\n\t" \
    "s_mov_b32 s53, 0x0fffffff\n\t" \
    "v_cvt_pk_bf16_f32 v198, v188, v189\n\t" \
    "v_cvt_pk_bf16_f32 v199, v190, v191\n\t" \
    "v_mfma_f32_32x32x8_bf16 v[176:191], v[216:217], %[qr1], 0\n\t" \
    "v_exp_f32 v160, v160\n\t" \
    "v_exp_f32 v161, v161\n\t" \
    "v_exp_f32 v162, v162\n\t" \
    "v_exp_f32 v163, v163\n\t" \
    "v_mfma_f32_32x32x16_bf16 %[o3], v[252:255], v[196:199], %[o3]\n\t" \
    "v_exp_f32 v164, v164\n\t" \
    "v_cvt_pk_bf16_f32 v192, v160, v161\n\t" \
    "v_exp_f32 v165, v165\n\t" \
    "v_cvt_pk_bf16_f32 v193, v162, v163\n\t" \
    "v_exp_f32 v166, v166\n\t" \
    "v_exp_f32 v167, v167\n\t" \
    "v_mfma_f32_32x32x16_bf16 v[176:191], v[200:203], %[qm1], v[176:191]\n\t" \
    "global_load_dwordx4 v[248:251], %[o16], s[42:43] sc1\n\t" \
    "global_load_dwordx4 v[252:255], %[o16], s[42:43] offset:1024 sc1\n\t" \
    "s_add_u32 s42, s42, 2048\n\t" \
    "s_addc_u32 s43, s43, 0\n\t" \
    "s_mov_b32 m0, s46\n\t" \
    "s_add_u32 s46, s46, 1024\n\t" \
    "global_load_lds_dwordx4 %[rv], s[58:59]\n\t" \
    "s_add_u32 s58, s58, %[rl]\n\t" \
    "s_addc_u32 s59, s59, 0\n\t" \
    "v_exp_f32 v168, v168\n\t" \
    "v_cvt_pk_bf16_f32 v194, v164, v165\n\t" \
    "v_exp_f32 v169, v169\n\t" \
    "v_cvt_pk_bf16_f32 v195, v166, v167\n\t" \
    "v_exp_f32 v170, v170\n\t" \
    "v_exp_f32 v171, v171\n\t" \
    "s_waitcnt vmcnt(16)\n\t" \
    "v_mfma_f32_32x32x16_bf16 %[o0], v[224:227], v[192:195], %[o0]\n\t" \
    "v_exp_f32 v172, v172\n\t" \
    "v_cvt_pk_bf16_f32 v196, v168, v169\n\t" \
    "v_exp_f32 v173, v173\n\t" \
    "v_cvt_pk_bf16_f32 v197, v170, v171\n\t" \
    "v_exp_f32 v174, v174\n\t" \
    "v_exp_f32 v175, v175\n\t" \
    "v_cvt_pk_bf16_f32 v198, v172, v173\n\t" \
    "v_cvt_pk_bf16_f32 v199, v174, v175\n\t" \
    "v_mfma_f32_32x32x8_bf16 v[160:175], v[216:217], %[qr2], 0\n\t" \
    "v_exp_f32 v176, v176\n\t" \
    "v_exp_f32 v177, v177\n\t" \
    "v_exp_f32 v178, v178\n\t" \
    "v_exp_f32 v179, v179\n\t" \
    "v_mfma_f32_32x32x16_bf16 %[o0], v[228:231], v[196:199], %[o0]\n\t" \
    "v_exp_f32 v180, v180\n\t" \
    "v_cvt_pk_bf16_f32 v192, v176, v177\n\t" \
    "v_exp_f32 v181, v181\n\t" \
    "v_cvt_pk_bf16_f32 v193, v178, v179\n\t" \
    "v_exp_f32 v182, v182\n\t" \
    "v_exp_f32 v183, v183\n\t" \
    "v_mfma_f32_32x32x16_bf16 v[160:175], v[200:203], %[qm2], v[160:175]\n\t" \
    "s_mul_i32 s48, s47, 1280\n\t" \
    "s_add_u32 s44, s49, s48\n\t" \
    "s_addc_u32 s45, s50, 0\n\t" \
    "s_mul_i32 s48, s47, 160\n\t" \
    "s_add_u32 m0, %[lw], s48\n\t" \
    "s_mov_b64 exec, s[52:53]\n\t" \
    "global_load_lds_dwordx4 %[ov], s[44:45]\n\t" \
    "s_mov_b64 exec, -1\n\t" \
    "s_add_u32 s47, s47, 6\n\t" \
    "s_min_u32 s47, s47, 634\n\t" \
    "v_exp_f32 v184, v184\n\t" \
    "v_cvt_pk_bf16_f32 v194, v180, v181\n\t" \
    "v_exp_f32 v185, v185\n\t" \
    "v_cvt_pk_bf16_f32 v195, v182, v183\n\t" \
    "v_exp_f32 v186, v186\n\t" \
    "v_exp_f32 v187, v187\n\t" \
    "v_mfma_f32_32x32x16_bf16 %[o1], v[224:227], v[192:195], %[o1]\n\t" \
    "v_exp_f32 v188, v188\n\t" \
    "v_cvt_pk_bf16_f32 v196, v184, v185\n\t" \
    "v_exp_f32 v189, v189\n\t" \
    "v_cvt_pk_bf16_f32 v197, v186, v187\n\t" \
    "v_exp_f32 v190, v190\n\t" \
    "v_exp_f32 v191, v191\n\t" \
    "v_cvt_pk_bf16_f32 v198, v188, v189\n\t" \
    "v_cvt_pk_bf16_f32 v199, v190, v191\n\t" \
    "v_mfma_f32_32x32x8_bf16 v[176:191], v[216:217], %[qr3], 0\n\t" \
    "v_exp_f32 v160, v160\n\t" \
    "v_exp_f32 v161, v161\n\t" \
    "v_exp_f32 v162, v162\n\t" \
    "v_exp_f32 v163, v163\n\t" \
    "v_mfma_f32_32x32x16_bf16 %[o1], v[228:231], v[196:199], %[o1]\n\t" \
    "v_exp_f32 v164, v164\n\t" \
    "v_cvt_pk_bf16_f32 v192, v160, v161\n\t" \
    "v_exp_f32 v165, v165\n\t" \
    "v_cvt_pk_bf16_f32 v193, v162, v163\n\t" \
    "v_exp_f32 v166, v166\n\t" \
    "v_exp_f32 v167, v167\n\t" \
    "v_mfma_f32_32x32x16_bf16 v[176:191], v[200:203], %[qm3], v[176:191]\n\t" \
    "s_mov_b32 m0, s46\n\t" \
    "s_add_u32 s46, s46, 1024\n\t" \
    "global_load_lds_dwordx4 %[rv], s[58:59]\n\t" \
    "s_add_u32 s58, s58, %[rl]\n\t" \
    "s_addc_u32 s59, s59, 0\n\t" \
    "v_exp_f32 v168, v168\n\t" \
    "v_cvt_pk_bf16_f32 v194, v164, v165\n\t" \
    "v_exp_f32 v169, v169\n\t" \
    "v_cvt_pk_bf16_f32 v195, v166, v167\n\t" \
    "v_exp_f32 v170, v170\n\t" \
    "v_exp_f32 v171, v171\n\t" \
    "v_mfma_f32_32x32x16_bf16 %[o2], v[224:227], v[192:195], %[o2]\n\t" \
    "v_exp_f32 v172, v172\n\t" \
    "v_cvt_pk_bf16_f32 v196, v168, v169\n\t" \
    "v_exp_f32 v173, v173\n\t" \
    "v_cvt_pk_bf16_f32 v197, v170, v171\n\t" \
    "v_exp_f32 v174, v174\n\t" \
    "v_exp_f32 v175, v175\n\t" \
    "v_cvt_pk_bf16_f32 v198, v172, v173\n\t" \
    "v_cvt_pk_bf16_f32 v199, v174, v175\n\t" \
    "s_waitcnt vmcnt(16)\n\t" \
    "v_mfma_f32_32x32x8_bf16 v[160:175], v[218:219], %[qr0], 0\n\t" \
    "v_exp_f32 v176, v176\n\t" \
    "v_exp_f32 v177, v177\n\t" \
    "v_exp_f32 v178, v178\n\t" \
    "v_exp_f32 v179, v179\n\t" \
    "v_mfma_f32_32x32x16_bf16 %[o2], v[228:231], v[196:199], %[o2]\n\t" \
    "v_exp_f32 v180, v180\n\t" \
    "v_cvt_pk_bf16_f32 v192, v176, v177\n\t" \
    "v_exp_f32 v181, v181\n\t" \
    "v_cvt_pk_bf16_f32 v193, v178, v179\n\t" \
    "v_exp_f32 v182, v182\n\t" \
    "v_exp_f32 v183, v183\n\t" \
    "v_mfma_f32_32x32x16_bf16 v[160:175], v[204:207], %[qm0], v[160:175]\n\t" \
    "global_load_dwordx4 v[200:203], %[o16], s[40:41] sc1\n\t" \
    "global_load_dwordx2 v[216:217], %[o8], s[40:41] offset:1024 sc1\n\t" \
    "s_add_u32 s40, s40, 1536\n\t" \
    "s_addc_u32 s41, s41, 0\n\t" \
    "global_store_dwordx4 %[o16], v[152:155], s[56:57] offset:0 sc1\n\t" \
    "v_exp_f32 v184, v184\n\t" \
    "v_cvt_pk_bf16_f32 v194, v180, v181\n\t" \
    "v_exp_f32 v185, v185\n\t" \
    "v_cvt_pk_bf16_f32 v195, v182, v183\n\t" \
    "v_exp_f32 v186, v186\n\t" \
    "v_exp_f32 v187, v187\n\t" \
    "v_mfma_f32_32x32x16_bf16 %[o3], v[224:227], v[192:195], %[o3]\n\t" \
    "v_exp_f32 v188, v188\n\t" \
    "v_cvt_pk_bf16_f32 v196, v184, v185\n\t" \
    "v_exp_f32 v189, v189\n\t" \
    "v_cvt_pk_bf16_f32 v197, v186, v187\n\t" \
    "v_exp_f32 v190, v190\n\t" \
    "v_exp_f32 v191, v191\n\t" \
    "v_cvt_pk_bf16_f32 v198, v188, v189\n\t" \
    "v_cvt_pk_bf16_f32 v199, v190, v191\n\t" \
    "v_mfma_f32_32x32x8_bf16 v[176:191], v[218:219], %[qr1], 0\n\t" \
    "v_exp_f32 v160, v160\n\t" \
    "v_exp_f32 v161, v161\n\t" \
    "v_exp_f32 v162, v162\n\t" \
    "v_exp_f32 v163, v163\n\t" \
    "v_mfma_f32_32x32x16_bf16 %[o3], v[228:231], v[196:199], %[o3]\n\t" \
    "v_exp_f32 v164, v164\n\t" \
    "v_cvt_pk_bf16_f32 v192, v160, v161\n\t" \
    "v_exp_f32 v165, v165\n\t" \
    "v_cvt_pk_bf16_f32 v193, v162, v163\n\t" \
    "v_exp_f32 v166, v166\n\t" \
    "v_exp_f32 v167, v167\n\t" \
    "v_mfma_f32_32x32x16_bf16 v[176:191], v[204:207], %[qm1], v[176:191]\n\t" \
    "global_load_dwordx4 v[224:227], %[o16], s[42:43] sc1\n\t" \
    "global_load_dwordx4 v[228:231], %[o16], s[42:43] offset:1024 sc1\n\t" \
    "s_add_u32 s42, s42, 2048\n\t" \
    "s_addc_u32 s43, s43, 0\n\t" \
    "s_mov_b32 m0, s46\n\t" \
    "s_add_u32 s46, s46, 1024\n\t" \
    "global_load_lds_dwordx4 %[rv], s[58:59]\n\t" \
    "s_add_u32 s58, s58, %[rl]\n\t" \
    "s_addc_u32 s59, s59, 0\n\t" \
    "v_exp_f32 v168, v168\n\t" \
    "v_cvt_pk_bf16_f32 v194, v164, v165\n\t" \
    "v_exp_f32 v169, v169\n\t" \
    "v_cvt_pk_bf16_f32 v195, v166, v167\n\t" \
    "v_exp_f32 v170, v170\n\t" \
    "v_exp_f32 v171, v171\n\t" \
    "s_waitcnt vmcnt(19)\n\t" \
    "v_mfma_f32_32x32x16_bf16 %[o0], v[232:235], v[192:195], %[o0]\n\t" \
    "v_exp_f32 v172, v172\n\t" \
    "v_cvt_pk_bf16_f32 v196, v168, v169\n\t" \
    "v_exp_f32 v173, v173\n\t" \
    "v_cvt_pk_bf16_f32 v197, v170, v171\n\t" \
    "v_exp_f32 v174, v174\n\t" \
    "v_exp_f32 v175, v175\n\t" \
    "v_cvt_pk_bf16_f32 v198, v172, v173\n\t" \
    "v_cvt_pk_bf16_f32 v199, v174, v175\n\t" \
    "v_mfma_f32_32x32x8_bf16 v[160:175], v[218:219], %[qr2], 0\n\t" \
    "v_exp_f32 v176, v176\n\t" \
    "v_exp_f32 v177, v177\n\t" \
    "v_exp_f32 v178, v178\n\t" \
    "v_exp_f32 v179, v179\n\t" \
    "v_mfma_f32_32x32x16_bf16 %[o0], v[236:239], v[196:199], %[o0]\n\t" \
    "v_exp_f32 v180, v180\n\t" \
    "v_cvt_pk_bf16_f32 v192, v176, v177\n\t" \
    "v_exp_f32 v181, v181\n\t" \
    "v_cvt_pk_bf16_f32 v193, v178, v179\n\t" \
    "v_exp_f32 v182, v182\n\t" \
    "v_exp_f32 v183, v183\n\t" \
    "v_mfma_f32_32x32x16_bf16 v[160:175], v[204:207], %[qm2], v[160:175]\n\t" \
    "s_mul_i32 s48, s47, 1280\n\t" \
    "s_add_u32 s44, s49, s48\n\t" \
    "s_addc_u32 s45, s50, 0\n\t" \
    "s_mul_i32 s48, s47, 160\n\t" \
    "s_add_u32 m0, %[lw], s48\n\t" \
    "s_mov_b64 exec, s[52:53]\n\t" \
    "global_load_lds_dwordx4 %[ov], s[44:45]\n\t" \
    "s_mov_b64 exec, -1\n\t" \
    "s_add_u32 s47, s47, 6\n\t" \
    "s_min_u32 s47, s47, 634\n\t" \
    "v_exp_f32 v184, v184\n\t" \
    "v_cvt_pk_bf16_f32 v194, v180, v181\n\t" \
    "v_exp_f32 v185, v185\n\t" \
    "v_cvt_pk_bf16_f32 v195, v182, v183\n\t" \
    "v_exp_f32 v186, v186\n\t" \
    "v_exp_f32 v187, v187\n\t" \
    "v_mfma_f32_32x32x16_bf16 %[o1], v[232:235], v[192:195], %[o1]\n\t" \
    "v_exp_f32 v188, v188\n\t" \
    "v_cvt_pk_bf16_f32 v196, v184, v185\n\t" \
    "v_exp_f32 v189, v189\n\t" \
    "v_cvt_pk_bf16_f32 v197, v186, v187\n\t" \
    "v_exp_f32 v190, v190\n\t" \
    "v_exp_f32 v191, v191\n\t" \
    "v_cvt_pk_bf16_f32 v198, v188, v189\n\t" \
    "v_cvt_pk_bf16_f32 v199, v190, v191\n\t" \
    "v_mfma_f32_32x32x8_bf16 v[176:191], v[218:219], %[qr3], 0\n\t" \
    "v_exp_f32 v160, v160\n\t" \
    "v_exp_f32 v161, v161\n\t" \
    "v_exp_f32 v162, v162\n\t" \
    "v_exp_f32 v163, v163\n\t" \
    "v_mfma_f32_32x32x16_bf16 %[o1], v[236:239], v[196:199], %[o1]\n\t" \
    "v_exp_f32 v164, v164\n\t" \
    "v_cvt_pk_bf16_f32 v192, v160, v161\n\t" \
    "v_exp_f32 v165, v165\n\t" \
    "v_cvt_pk_bf16_f32 v193, v162, v163\n\t" \
    "v_exp_f32 v166, v166\n\t" \
    "v_exp_f32 v167, v167\n\t" \
    "v_mfma_f32_32x32x16_bf16 v[176:191], v[204:207], %[qm3], v[176:191]\n\t" \
    "s_mov_b32 m0, s46\n\t" \
    "s_add_u32 s46, s46, 1024\n\t" \
    "global_load_lds_dwordx4 %[rv], s[58:59]\n\t" \
    "s_add_u32 s58, s58, %[rl]\n\t" \
    "s_addc_u32 s59, s59, 0\n\t" \
    "v_exp_f32 v168, v168\n\t" \
    "v_cvt_pk_bf16_f32 v194, v164, v165\n\t" \
    "v_exp_f32 v169, v169\n\t" \
    "v_cvt_pk_bf16_f32 v195, v166, v167\n\t" \
    "v_exp_f32 v170, v170\n\t" \
    "v_exp_f32 v171, v171\n\t" \
    "v_mfma_f32_32x32x16_bf16 %[o2], v[232:235], v[192:195], %[o2]\n\t" \
    "v_exp_f32 v172, v172\n\t" \
    "v_cvt_pk_bf16_f32 v196, v168, v169\n\t" \
    "v_exp_f32 v173, v173\n\t" \
    "v_cvt_pk_bf16_f32 v197, v170, v171\n\t" \
    "v_exp_f32 v174, v174\n\t" \
    "v_exp_f32 v175, v175\n\t" \
    "v_cvt_pk_bf16_f32 v198, v172, v173\n\t" \
    "v_cvt_pk_bf16_f32 v199, v174, v175\n\t" \
    "s_waitcnt vmcnt(19)\n\t" \
    "v_mfma_f32_32x32x8_bf16 v[160:175], v[220:221], %[qr0], 0\n\t" \
    "v_exp_f32 v176, v176\n\t" \
    "v_exp_f32 v177, v177\n\t" \
    "v_exp_f32 v178, v178\n\t" \
    "v_exp_f32 v179, v179\n\t" \
    "v_mfma_f32_32x32x16_bf16 %[o2], v[236:239], v[196:199], %[o2]\n\t" \
    "v_exp_f32 v180, v180\n\t" \
    "v_cvt_pk_bf16_f32 v192, v176, v177\n\t" \
    "v_exp_f32 v181, v181\n\t" \
    "v_cvt_pk_bf16_f32 v193, v178, v179\n\t" \
    "v_exp_f32 v182, v182\n\t" \
    "v_exp_f32 v183, v183\n\t" \
    "v_mfma_f32_32x32x16_bf16 v[160:175], v[208:211], %[qm0], v[160:175]\n\t" \
    "global_load_dwordx4 v[204:207], %[o16], s[40:41] sc1\n\t" \
    "global_load_dwordx2 v[218:219], %[o8], s[40:41] offset:1024 sc1\n\t" \
    "s_add_u32 s40, s40, 1536\n\t" \
    "s_addc_u32 s41, s41, 0\n\t" \
    "global_store_dwordx4 %[o16], v[152:155], s[56:57] offset:1024 sc1\n\t" \
    "v_exp_f32 v184, v184\n\t" \
    "v_cvt_pk_bf16_f32 v194, v180, v181\n\t" \
    "v_exp_f32 v185, v185\n\t" \
    "v_cvt_pk_bf16_f32 v195, v182, v183\n\t" \
    "v_exp_f32 v186, v186\n\t" \
    "v_exp_f32 v187, v187\n\t" \
    "v_mfma_f32_32x32x16_bf16 %[o3], v[232:235], v[192:195], %[o3]\n\t" \
    "v_exp_f32 v188, v188\n\t" \
    "v_cvt_pk_bf16_f32 v196, v184, v185\n\t" \
    "v_exp_f32 v189, v189\n\t" \
    "v_cvt_pk_bf16_f32 v197, v186, v187\n\t" \
    "v_exp_f32 v190, v190\n\t" \
    "v_exp_f32 v191, v191\n\t" \
    "v_cvt_pk_bf16_f32 v198, v188, v189\n\t" \
    "v_cvt_pk_bf16_f32 v199, v190, v191\n\t" \
    "v_mfma_f32_32x32x8_bf16 v[176:191], v[220:221], %[qr1], 0\n\t" \
    "v_exp_f32 v160, v160\n\t" \
    "v_exp_f32 v161, v161\n\t" \
    "v_exp_f32 v162, v162\n\t" \
    "v_exp_f32 v163, v163\n\t" \
    "v_mfma_f32_32x32x16_bf16 %[o3], v[236:239], v[196:199], %[o3]\n\t" \
    "v_exp_f32 v164, v164\n\t" \
    "v_cvt_pk_bf16_f32 v192, v160, v161\n\t" \
    "v_exp_f32 v165, v165\n\t" \
    "v_cvt_pk_bf16_f32 v193, v162, v163\n\t" \
    "v_exp_f32 v166, v166\n\t" \
    "v_exp_f32 v167, v167\n\t" \
    "v_mfma_f32_32x32x16_bf16 v[176:191], v[208:211], %[qm1], v[176:191]\n\t" \
    "global_load_dwordx4 v[232:235], %[o16], s[42:43] sc1\n\t" \
    "global_load_dwordx4 v[236:239], %[o16], s[42:43] offset:1024 sc1\n\t" \
    "s_add_u32 s42, s42, 2048\n\t" \
    "s_addc_u32 s43, s43, 0\n\t" \
    "v_exp_f32 v168, v168\n\t" \
    "v_cvt_pk_bf16_f32 v194, v164, v165\n\t" \
    "v_exp_f32 v169, v169\n\t" \
    "v_cvt_pk_bf16_f32 v195, v166, v167\n\t" \
    "v_exp_f32 v170, v170\n\t" \
    "v_exp_f32 v171, v171\n\t" \
    "s_waitcnt vmcnt(21)\n\t" \
    "v_mfma_f32_32x32x16_bf16 %[o0], v[240:243], v[192:195], %[o0]\n\t" \
    "v_exp_f32 v172, v172\n\t" \
    "v_cvt_pk_bf16_f32 v196, v168, v169\n\t" \
    "v_exp_f32 v173, v173\n\t" \
    "v_cvt_pk_bf16_f32 v197, v170, v171\n\t" \
    "v_exp_f32 v174, v174\n\t" \
    "v_exp_f32 v175, v175\n\t" \
    "v_cvt_pk_bf16_f32 v198, v172, v173\n\t" \
    "v_cvt_pk_bf16_f32 v199, v174, v175\n\t" \
    "v_mfma_f32_32x32x8_bf16 v[160:175], v[220:221], %[qr2], 0\n\t" \
    "v_exp_f32 v176, v176\n\t" \
    "v_exp_f32 v177, v177\n\t" \
    "v_exp_f32 v178, v178\n\t" \
    "v_exp_f32 v179, v179\n\t" \
    "v_mfma_f32_32x32x16_bf16 %[o0], v[244:247], v[196:199], %[o0]\n\t" \
    "v_exp_f32 v180, v180\n\t" \
    "v_cvt_pk_bf16_f32 v192, v176, v177\n\t" \
    "v_exp_f32 v181, v181\n\t" \
    "v_cvt_pk_bf16_f32 v193, v178, v179\n\t" \
    "v_exp_f32 v182, v182\n\t" \
    "v_exp_f32 v183, v183\n\t" \
    "v_mfma_f32_32x32x16_bf16 v[160:175], v[208:211], %[qm2], v[160:175]\n\t" \
    "s_mul_i32 s48, s47, 1280\n\t" \
    "s_add_u32 s44, s49, s48\n\t" \
    "s_addc_u32 s45, s50, 0\n\t" \
    "s_mul_i32 s48, s47, 160\n\t" \
    "s_add_u32 m0, %[lw], s48\n\t" \
    "s_mov_b64 exec, s[52:53]\n\t" \
    "global_load_lds_dwordx4 %[ov], s[44:45]\n\t" \
    "s_mov_b64 exec, -1\n\t" \
    "s_add_u32 s47, s47, 6\n\t" \
    "s_min_u32 s47, s47, 634\n\t" \
    "v_exp_f32 v184, v184\n\t" \
    "v_cvt_pk_bf16_f32 v194, v180, v181\n\t" \
    "v_exp_f32 v185, v185\n\t" \
    "v_cvt_pk_bf16_f32 v195, v182, v183\n\t" \
    "v_exp_f32 v186, v186\n\t" \
    "v_exp_f32 v187, v187\n\t" \
    "v_mfma_f32_32x32x16_bf16 %[o1], v[240:243], v[192:195], %[o1]\n\t" \
    "v_exp_f32 v188, v188\n\t" \
    "v_cvt_pk_bf16_f32 v196, v184, v185\n\t" \
    "v_exp_f32 v189, v189\n\t" \
    "v_cvt_pk_bf16_f32 v197, v186, v187\n\t" \
    "v_exp_f32 v190, v190\n\t" \
    "v_exp_f32 v191, v191\n\t" \
    "v_cvt_pk_bf16_f32 v198, v188, v189\n\t" \
    "v_cvt_pk_bf16_f32 v199, v190, v191\n\t" \
    "v_mfma_f32_32x32x8_bf16 v[176:191], v[220:221], %[qr3], 0\n\t" \
    "v_exp_f32 v160, v160\n\t" \
    "v_exp_f32 v161, v161\n\t" \
    "v_exp_f32 v162, v162\n\t" \
    "v_exp_f32 v163, v163\n\t" \
    "v_mfma_f32_32x32x16_bf16 %[o1], v[244:247], v[196:199], %[o1]\n\t" \
    "v_exp_f32 v164, v164\n\t" \
    "v_cvt_pk_bf16_f32 v192, v160, v161\n\t" \
    "v_exp_f32 v165, v165\n\t" \
    "v_cvt_pk_bf16_f32 v193, v162, v163\n\t" \
    "v_exp_f32 v166, v166\n\t" \
    "v_exp_f32 v167, v167\n\t" \
    "v_mfma_f32_32x32x16_bf16 v[176:191], v[208:211], %[qm3], v[176:191]\n\t" \
    "s_mul_i32 s48, s47, 1280\n\t" \
    "s_add_u32 s44, s49, s48\n\t" \
    "s_addc_u32 s45, s50, 0\n\t" \
    "s_mul_i32 s48, s47, 160\n\t" \
    "s_add_u32 m0, %[lw], s48\n\t" \
    "s_mov_b64 exec, s[52:53]\n\t" \
    "global_load_lds_dwordx4 %[ov], s[44:45]\n\t" \
    "s_mov_b64 exec, -1\n\t" \
    "s_add_u32 s47, s47, 6\n\t" \
    "s_min_u32 s47, s47, 634\n\t" \
    "v_exp_f32 v168, v168\n\t" \
    "v_cvt_pk_bf16_f32 v194, v164, v165\n\t" \
    "v_exp_f32 v169, v169\n\t" \
    "v_cvt_pk_bf16_f32 v195, v166, v167\n\t" \
    "v_exp_f32 v170, v170\n\t" \
    "v_exp_f32 v171, v171\n\t" \
    "v_mfma_f32_32x32x16_bf16 %[o2], v[240:243], v[192:195], %[o2]\n\t" \
    "v_exp_f32 v172, v172\n\t" \
    "v_cvt_pk_bf16_f32 v196, v168, v169\n\t" \
    "v_exp_f32 v173, v173\n\t" \
    "v_cvt_pk_bf16_f32 v197, v170, v171\n\t" \
    "v_exp_f32 v174, v174\n\t" \
    "v_exp_f32 v175, v175\n\t" \
    "v_cvt_pk_bf16_f32 v198, v172, v173\n\t" \
    "v_cvt_pk_bf16_f32 v199, v174, v175\n\t" \
    "s_waitcnt vmcnt(21)\n\t" \
    "v_mfma_f32_32x32x8_bf16 v[160:175], v[222:223], %[qr0], 0\n\t" \
    "v_exp_f32 v176, v176\n\t" \
    "v_exp_f32 v177, v177\n\t" \
    "v_exp_f32 v178, v178\n\t" \
    "v_exp_f32 v179, v179\n\t" \
    "v_mfma_f32_32x32x16_bf16 %[o2], v[244:247], v[196:199], %[o2]\n\t" \
    "v_exp_f32 v180, v180\n\t" \
    "v_cvt_pk_bf16_f32 v192, v176, v177\n\t" \
    "v_exp_f32 v181, v181\n\t" \
    "v_cvt_pk_bf16_f32 v193, v178, v179\n\t" \
    "v_exp_f32 v182, v182\n\t" \
    "v_exp_f32 v183, v183\n\t" \
    "v_mfma_f32_32x32x16_bf16 v[160:175], v[212:215], %[qm0], v[160:175]\n\t" \
    "global_load_dwordx4 v[208:211], %[o16], s[40:41] sc1\n\t" \
    "global_load_dwordx2 v[220:221], %[o8], s[40:41] offset:1024 sc1\n\t" \
    "s_add_u32 s40, s40, 1536\n\t" \
    "s_addc_u32 s41, s41, 0\n\t" \
    "global_store_dwordx4 %[o16], v[152:155], s[56:57] offset:2048 sc1\n\t" \
    "v_exp_f32 v184, v184\n\t" \
    "v_cvt_pk_bf16_f32 v194, v180, v181\n\t" \
    "v_exp_f32 v185, v185\n\t" \
    "v_cvt_pk_bf16_f32 v195, v182, v183\n\t" \
    "v_exp_f32 v186, v186\n\t" \
    "v_exp_f32 v187, v187\n\t" \
    "v_mfma_f32_32x32x16_bf16 %[o3], v[240:243], v[192:195], %[o3]\n\t" \
    "v_exp_f32 v188, v188\n\t" \
    "v_cvt_pk_bf16_f32 v196, v184, v185\n\t" \
    "v_exp_f32 v189, v189\n\t" \
    "v_cvt_pk_bf16_f32 v197, v186, v187\n\t" \
    "v_exp_f32 v190, v190\n\t" \
    "v_exp_f32 v191, v191\n\t" \
    "v_cvt_pk_bf16_f32 v198, v188, v189\n\t" \
    "v_cvt_pk_bf16_f32 v199, v190, v191\n\t" \
    "v_mfma_f32_32x32x8_bf16 v[176:191], v[222:223], %[qr1], 0\n\t" \
    "v_exp_f32 v160, v160\n\t" \
    "v_exp_f32 v161, v161\n\t" \
    "v_exp_f32 v162, v162\n\t" \
    "v_exp_f32 v163, v163\n\t" \
    "v_mfma_f32_32x32x16_bf16 %[o3], v[244:247], v[196:199], %[o3]\n\t" \
    "v_exp_f32 v164, v164\n\t" \
    "v_cvt_pk_bf16_f32 v192, v160, v161\n\t" \
    "v_exp_f32 v165, v165\n\t" \
    "v_cvt_pk_bf16_f32 v193, v162, v163\n\t" \
    "v_exp_f32 v166, v166\n\t" \
    "v_exp_f32 v167, v167\n\t" \
    "v_mfma_f32_32x32x16_bf16 v[176:191], v[212:215], %[qm1], v[176:191]\n\t" \
    "global_load_dwordx4 v[240:243], %[o16], s[42:43] sc1\n\t" \
    "global_load_dwordx4 v[244:247], %[o16], s[42:43] offset:1024 sc1\n\t" \
    "s_add_u32 s42, s42, 2048\n\t" \
    "s_addc_u32 s43, s43, 0\n\t" \
    "s_mul_i32 s48, s47, 1280\n\t" \
    "s_add_u32 s44, s49, s48\n\t" \
    "s_addc_u32 s45, s50, 0\n\t" \
    "s_mul_i32 s48, s47, 160\n\t" \
    "s_add_u32 m0, %[lw], s48\n\t" \
    "s_mov_b64 exec, s[52:53]\n\t" \
    "global_load_lds_dwordx4 %[ov], s[44:45]\n\t" \
    "s_mov_b64 exec, -1\n\t" \
    "s_add_u32 s47, s47, 6\n\t" \
    "s_min_u32 s47, s47, 634\n\t" \
    "v_exp_f32 v168, v168\n\t" \
    "v_cvt_pk_bf16_f32 v194, v164, v165\n\t" \
    "v_exp_f32 v169, v169\n\t" \
    "v_cvt_pk_bf16_f32 v195, v166, v167\n\t" \
    "v_exp_f32 v170, v170\n\t" \
    "v_exp_f32 v171, v171\n\t" \
    "s_waitcnt vmcnt(24)\n\t" \
    "v_mfma_f32_32x32x16_bf16 %[o0], v[248:251], v[192:195], %[o0]\n\t" \
    "v_exp_f32 v172, v172\n\t" \
    "v_cvt_pk_bf16_f32 v196, v168, v169\n\t" \
    "v_exp_f32 v173, v173\n\t" \
    "v_cvt_pk_bf16_f32 v197, v170, v171\n\t" \
    "v_exp_f32 v174, v174\n\t" \
    "v_exp_f32 v175, v175\n\t" \
    "v_cvt_pk_bf16_f32 v198, v172, v173\n\t" \
    "v_cvt_pk_bf16_f32 v199, v174, v175\n\t" \
    "v_mfma_f32_32x32x8_bf16 v[160:175], v[222:223], %[qr2], 0\n\t" \
    "v_exp_f32 v176, v176\n\t" \
    "v_exp_f32 v177, v177\n\t" \
    "v_exp_f32 v178, v178\n\t" \
    "v_exp_f32 v179, v179\n\t" \
    "v_mfma_f32_32x32x16_bf16 %[o0], v[252:255], v[196:199], %[o0]\n\t" \
    "v_exp_f32 v180, v180\n\t" \
    "v_cvt_pk_bf16_f32 v192, v176, v177\n\t" \
    "v_exp_f32 v181, v181\n\t" \
    "v_cvt_pk_bf16_f32 v193, v178, v179\n\t" \
    "v_exp_f32 v182, v182\n\t" \
    "v_exp_f32 v183, v183\n\t" \
    "v_mfma_f32_32x32x16_bf16 v[160:175], v[212:215], %[qm2], v[160:175]\n\t" \
    "s_mul_i32 s48, s47, 1280\n\t" \
    "s_add_u32 s44, s49, s48\n\t" \
    "s_addc_u32 s45, s50, 0\n\t" \
    "s_mul_i32 s48, s47, 160\n\t" \
    "s_add_u32 m0, %[lw], s48\n\t" \
    "s_mov_b64 exec, s[52:53]\n\t" \
    "global_load_lds_dwordx4 %[ov], s[44:45]\n\t" \
    "s_mov_b64 exec, -1\n\t" \
    "s_add_u32 s47, s47, 6\n\t" \
    "s_min_u32 s47, s47, 634\n\t" \
    "v_exp_f32 v184, v184\n\t" \
    "v_cvt_pk_bf16_f32 v194, v180, v181\n\t" \
    "v_exp_f32 v185, v185\n\t" \
    "v_cvt_pk_bf16_f32 v195, v182, v183\n\t" \
    "v_exp_f32 v186, v186\n\t" \
    "v_exp_f32 v187, v187\n\t" \
    "v_mfma_f32_32x32x16_bf16 %[o1], v[248:251], v[192:195], %[o1]\n\t" \
    "v_exp_f32 v188, v188\n\t" \
    "v_cvt_pk_bf16_f32 v196, v184, v185\n\t" \
    "v_exp_f32 v189, v189\n\t" \
    "v_cvt_pk_bf16_f32 v197, v186, v187\n\t" \
    "v_exp_f32 v190, v190\n\t" \
    "v_exp_f32 v191, v191\n\t" \
    "v_cvt_pk_bf16_f32 v198, v188, v189\n\t" \
    "v_cvt_pk_bf16_f32 v199, v190, v191\n\t" \
    "v_mfma_f32_32x32x8_bf16 v[176:191], v[222:223], %[qr3], 0\n\t" \
    "v_exp_f32 v160, v160\n\t" \
    "v_exp_f32 v161, v161\n\t" \
    "v_exp_f32 v162, v162\n\t" \
    "v_exp_f32 v163, v163\n\t" \
    "v_mfma_f32_32x32x16_bf16 %[o1], v[252:255], v[196:199], %[o1]\n\t" \
    "v_exp_f32 v164, v164\n\t" \
    "v_cvt_pk_bf16_f32 v192, v160, v161\n\t" \
    "v_exp_f32 v165, v165\n\t" \
    "v_cvt_pk_bf16_f32 v193, v162, v163\n\t" \
    "v_exp_f32 v166, v166\n\t" \
    "v_exp_f32 v167, v167\n\t" \
    "v_mfma_f32_32x32x16_bf16 v[176:191], v[212:215], %[qm3], v[176:191]\n\t" \
    "s_mul_i32 s48, s47, 1280\n\t" \
    "s_add_u32 s44, s49, s48\n\t" \
    "s_addc_u32 s45, s50, 0\n\t" \
    "s_mul_i32 s48, s47, 160\n\t" \
    "s_add_u32 m0, %[lw], s48\n\t" \
    "s_mov_b64 exec, s[52:53]\n\t" \
    "global_load_lds_dwordx4 %[ov], s[44:45]\n\t" \
    "s_mov_b64 exec, -1\n\t" \
    "s_add_u32 s47, s47, 6\n\t" \
    "s_min_u32 s47, s47, 634\n\t" \
    "v_exp_f32 v168, v168\n\t" \
    "v_cvt_pk_bf16_f32 v194, v164, v165\n\t" \
    "v_exp_f32 v169, v169\n\t" \
    "v_cvt_pk_bf16_f32 v195, v166, v167\n\t" \
    "v_exp_f32 v170, v170\n\t" \
    "v_exp_f32 v171, v171\n\t" \
    "v_mfma_f32_32x32x16_bf16 %[o2], v[248:251], v[192:195], %[o2]\n\t" \
    "v_exp_f32 v172, v172\n\t" \
    "v_cvt_pk_bf16_f32 v196, v168, v169\n\t" \
    "v_exp_f32 v173, v173\n\t" \
    "v_cvt_pk_bf16_f32 v197, v170, v171\n\t" \
    "v_exp_f32 v174, v174\n\t" \
    "v_exp_f32 v175, v175\n\t" \
    "v_cvt_pk_bf16_f32 v198, v172, v173\n\t" \
    "v_cvt_pk_bf16_f32 v199, v174, v175\n\t" \
    "s_waitcnt vmcnt(21)\n\t" \
    "v_mfma_f32_32x32x8_bf16 v[160:175], v[216:217], %[qr0], 0\n\t" \
    "v_exp_f32 v176, v176\n\t" \
    "v_exp_f32 v177, v177\n\t" \
    "v_exp_f32 v178, v178\n\t" \
    "v_exp_f32 v179, v179\n\t" \
    "v_mfma_f32_32x32x16_bf16 %[o2], v[252:255], v[196:199], %[o2]\n\t" \
    "v_exp_f32 v180, v180\n\t" \
    "v_cvt_pk_bf16_f32 v192, v176, v177\n\t" \
    "v_exp_f32 v181, v181\n\t" \
    "v_cvt_pk_bf16_f32 v193, v178, v179\n\t" \
    "v_exp_f32 v182, v182\n\t" \
    "v_exp_f32 v183, v183\n\t" \
    "v_mfma_f32_32x32x16_bf16 v[160:175], v[200:203], %[qm0], v[160:175]\n\t" \
    "global_load_dwordx4 v[212:215], %[o16], s[40:41] sc1\n\t" \
    "global_load_dwordx2 v[222:223], %[o8], s[40:41] offset:1024 sc1\n\t" \
    "s_add_u32 s40, s40, 1536\n\t" \
    "s_addc_u32 s41, s41, 0\n\t" \
    "v_exp_f32 v184, v184\n\t" \
    "v_cvt_pk_bf16_f32 v194, v180, v181\n\t" \
    "v_exp_f32 v185, v185\n\t" \
    "v_cvt_pk_bf16_f32 v195, v182, v183\n\t" \
    "v_exp_f32 v186, v186\n\t" \
    "v_exp_f32 v187, v187\n\t" \
    "v_mfma_f32_32x32x16_bf16 %[o3], v[248:251], v[192:195], %[o3]\n\t" \
    "v_exp_f32 v188, v188\n\t" \
    "v_cvt_pk_bf16_f32 v196, v184, v185\n\t" \
    "v_exp_f32 v189, v189\n\t" \
    "v_cvt_pk_bf16_f32 v197, v186, v187\n\t" \
    "v_exp_f32 v190, v190\n\t" \
    "v_exp_f32 v191, v191\n\t" \
    "s_mov_b32 m0, s54\n\t" \
    "s_mov_b64 %[kb], s[40:41]\n\t" \
    "s_mov_b64 %[vb], s[42:43]\n\t" \
    "s_mov_b32 %[wr], s47\n\t"
#define ATTN_SEG2_ASM \
    "s_mov_b64 s[40:41], %[kb]\n\t" \
    "s_mov_b64 s[42:43], %[vb]\n\t" \
    "s_mov_b32 s54, m0\n\t" \
    "s_mov_b32 s47, %[wr]\n\t" \
    "s_mov_b64 s[50:51], %[wo]\n\t" \
    "s_mov_b32 s49, s50\n\t" \
    "s_mov_b32 s50, s51\n\t" \
    "s_mov_b32 s52, -1\n\t" \
    "s_mov_b32 s53, 0x0fffffff\n\t" \
    "v_cvt_pk_bf16_f32 v198, v188, v189\n\t" \
    "v_cvt_pk_bf16_f32 v199, v190, v191\n\t" \
    "v_mfma_f32_32x32x8_bf16 v[176:191], v[216:217], %[qr1], 0\n\t" \
    "v_exp_f32 v160, v160\n\t" \
    "v_exp_f32 v161, v161\n\t" \
    "v_exp_f32 v162, v162\n\t" \
    "v_exp_f32 v163, v163\n\t" \
    "v_mfma_f32_32x32x16_bf16 %[o3], v[252:255], v[196:199], %[o3]\n\t" \
    "v_exp_f32 v164, v164\n\t" \
    "v_cvt_pk_bf16_f32 v192, v160, v161\n\t" \
    "v_exp_f32 v165, v165\n\t" \
    "v_cvt_pk_bf16_f32 v193, v162, v163\n\t" \
    "v_exp_f32 v166, v166\n\t" \
    "v_exp_f32 v167, v167\n\t" \
    "v_mfma_f32_32x32x16_bf16 v[176:191], v[200:203], %[qm1], v[176:191]\n\t" \
    "global_load_dwordx4 v[248:251], %[o16], s[42:43] sc1\n\t" \
    "global_load_dwordx4 v[252:255], %[o16], s[42:43] offset:1024 sc1\n\t" \
    "s_add_u32 s42, s42, 2048\n\t" \
    "s_addc_u32 s43, s43, 0\n\t" \
    "v_exp_f32 v168, v168\n\t" \
    "v_cvt_pk_bf16_f32 v194, v164, v165\n\t" \
    "v_exp_f32 v169, v169\n\t" \
    "v_cvt_pk_bf16_f32 v195, v166, v167\n\t" \
    "v_exp_f32 v170, v170\n\t" \
    "v_exp_f32 v171, v171\n\t" \
    "s_waitcnt vmcnt(30)\n\t" \
    "v_mfma_f32_32x32x16_bf16 %[o0], v[224:227], v[192:195], %[o0]\n\t" \
    "v_exp_f32 v172, v172\n\t" \
    "v_cvt_pk_bf16_f32 v196, v168, v169\n\t" \
    "v_exp_f32 v173, v173\n\t" \
    "v_cvt_pk_bf16_f32 v197, v170, v171\n\t" \
    "v_exp_f32 v174, v174\n\t" \
    "v_exp_f32 v175, v175\n\t" \
    "v_cvt_pk_bf16_f32 v198, v172, v173\n\t" \
    "v_cvt_pk_bf16_f32 v199, v174, v175\n\t" \
    "v_mfma_f32_32x32x8_bf16 v[160:175], v[216:217], %[qr2], 0\n\t" \
    "v_exp_f32 v176, v176\n\t" \
    "v_exp_f32 v177, v177\n\t" \
    "v_exp_f32 v178, v178\n\t" \
    "v_exp_f32 v179, v179\n\t" \
    "v_mfma_f32_32x32x16_bf16 %[o0], v[228:231], v[196:199], %[o0]\n\t" \
    "v_exp_f32 v180, v180\n\t" \
    "v_cvt_pk_bf16_f32 v192, v176, v177\n\t" \
    "v_exp_f32 v181, v181\n\t" \
    "v_cvt_pk_bf16_f32 v193, v178, v179\n\t" \
    "v_exp_f32 v182, v182\n\t" \
    "v_exp_f32 v183, v183\n\t" \
    "v_mfma_f32_32x32x16_bf16 v[160:175], v[200:203], %[qm2], v[160:175]\n\t" \
    "s_mul_i32 s48, s47, 1280\n\t" \
    "s_add_u32 s44, s49, s48\n\t" \
    "s_addc_u32 s45, s50, 0\n\t" \
    "s_mul_i32 s48, s47, 160\n\t" \
    "s_add_u32 m0, %[lw], s48\n\t" \
    "s_mov_b64 exec, s[52:53]\n\t" \
    "global_load_lds_dwordx4 %[ov], s[44:45]\n\t" \
    "s_mov_b64 exec, -1\n\t" \
    "s_add_u32 s47, s47, 6\n\t" \
    "s_min_u32 s47, s47, 634\n\t" \
    "v_exp_f32 v184, v184\n\t" \
    "v_cvt_pk_bf16_f32 v194, v180, v181\n\t" \
    "v_exp_f32 v185, v185\n\t" \
    "v_cvt_pk_bf16_f32 v195, v182, v183\n\t" \
    "v_exp_f32 v186, v186\n\t" \
    "v_exp_f32 v187, v187\n\t" \
    "v_mfma_f32_32x32x16_bf16 %[o1], v[224:227], v[192:195], %[o1]\n\t" \
    "v_exp_f32 v188, v188\n\t" \
    "v_cvt_pk_bf16_f32 v196, v184, v185\n\t" \
    "v_exp_f32 v189, v189\n\t" \
    "v_cvt_pk_bf16_f32 v197, v186, v187\n\t" \
    "v_exp_f32 v190, v190\n\t" \
    "v_exp_f32 v191, v191\n\t" \
    "v_cvt_pk_bf16_f32 v198, v188, v189\n\t" \
    "v_cvt_pk_bf16_f32 v199, v190, v191\n\t" \
    "v_mfma_f32_32x32x8_bf16 v[176:191], v[216:217], %[qr3], 0\n\t" \
    "v_exp_f32 v160, v160\n\t" \
    "v_exp_f32 v161, v161\n\t" \
    "v_exp_f32 v162, v162\n\t" \
    "v_exp_f32 v163, v163\n\t" \
    "v_mfma_f32_32x32x16_bf16 %[o1], v[228:231], v[196:199], %[o1]\n\t" \
    "v_exp_f32 v164, v164\n\t" \
    "v_cvt_pk_bf16_f32 v192, v160, v161\n\t" \
    "v_exp_f32 v165, v165\n\t" \
    "v_cvt_pk_bf16_f32 v193, v162, v163\n\t" \
    "v_exp_f32 v166, v166\n\t" \
    "v_exp_f32 v167, v167\n\t" \
    "v_mfma_f32_32x32x16_bf16 v[176:191], v[200:203], %[qm3], v[176:191]\n\t" \
    "s_mul_i32 s48, s47, 1280\n\t" \
    "s_add_u32 s44, s49, s48\n\t" \
    "s_addc_u32 s45, s50, 0\n\t" \
    "s_mul_i32 s48, s47, 160\n\t" \
    "s_add_u32 m0, %[lw], s48\n\t" \
    "s_mov_b64 exec, s[52:53]\n\t" \
    "global_load_lds_dwordx4 %[ov], s[44:45]\n\t" \
    "s_mov_b64 exec, -1\n\t" \
    "s_add_u32 s47, s47, 6\n\t" \
    "s_min_u32 s47, s47, 634\n\t" \
    "v_exp_f32 v168, v168\n\t" \
    "v_cvt_pk_bf16_f32 v194, v164, v165\n\t" \
    "v_exp_f32 v169, v169\n\t" \
    "v_cvt_pk_bf16_f32 v195, v166, v167\n\t" \
    "v_exp_f32 v170, v170\n\t" \
    "v_exp_f32 v171, v171\n\t" \
    "v_mfma_f32_32x32x16_bf16 %[o2], v[224:227], v[192:195], %[o2]\n\t" \
    "v_exp_f32 v172, v172\n\t" \
    "v_cvt_pk_bf16_f32 v196, v168, v169\n\t" \
    "v_exp_f32 v173, v173\n\t" \
    "v_cvt_pk_bf16_f32 v197, v170, v171\n\t" \
    "v_exp_f32 v174, v174\n\t" \
    "v_exp_f32 v175, v175\n\t" \
    "v_cvt_pk_bf16_f32 v198, v172, v173\n\t" \
    "v_cvt_pk_bf16_f32 v199, v174, v175\n\t" \
    "s_waitcnt vmcnt(27)\n\t" \
    "v_mfma_f32_32x32x8_bf16 v[160:175], v[218:219], %[qr0], 0\n\t" \
    "v_exp_f32 v176, v176\n\t" \
    "v_exp_f32 v177, v177\n\t" \
    "v_exp_f32 v178, v178\n\t" \
    "v_exp_f32 v179, v179\n\t" \
    "v_mfma_f32_32x32x16_bf16 %[o2], v[228:231], v[196:199], %[o2]\n\t" \
    "v_exp_f32 v180, v180\n\t" \
    "v_cvt_pk_bf16_f32 v192, v176, v177\n\t" \
    "v_exp_f32 v181, v181\n\t" \
    "v_cvt_pk_bf16_f32 v193, v178, v179\n\t" \
    "v_exp_f32 v182, v182\n\t" \
    "v_exp_f32 v183, v183\n\t" \
    "v_mfma_f32_32x32x16_bf16 v[160:175], v[204:207], %[qm0], v[160:175]\n\t" \
    "global_load_dwordx4 v[200:203], %[o16], s[40:41] sc1\n\t" \
    "global_load_dwordx2 v[216:217], %[o8], s[40:41] offset:1024 sc1\n\t" \
    "s_add_u32 s40, s40, 1536\n\t" \
    "s_addc_u32 s41, s41, 0\n\t" \
    "v_exp_f32 v184, v184\n\t" \
    "v_cvt_pk_bf16_f32 v194, v180, v181\n\t" \
    "v_exp_f32 v185, v185\n\t" \
    "v_cvt_pk_bf16_f32 v195, v182, v183\n\t" \
    "v_exp_f32 v186, v186\n\t" \
    "v_exp_f32 v187, v187\n\t" \
    "v_mfma_f32_32x32x16_bf16 %[o3], v[224:227], v[192:195], %[o3]\n\t" \
    "v_exp_f32 v188, v188\n\t" \
    "v_cvt_pk_bf16_f32 v196, v184, v185\n\t" \
    "v_exp_f32 v189, v189\n\t" \
    "v_cvt_pk_bf16_f32 v197, v186, v187\n\t" \
    "v_exp_f32 v190, v190\n\t" \
    "v_exp_f32 v191, v191\n\t" \
    "v_cvt_pk_bf16_f32 v198, v188, v189\n\t" \
    "v_cvt_pk_bf16_f32 v199, v190, v191\n\t" \
    "v_mfma_f32_32x32x8_bf16 v[176:191], v[218:219], %[qr1], 0\n\t" \
    "v_exp_f32 v160, v160\n\t" \
    "v_exp_f32 v161, v161\n\t" \
    "v_exp_f32 v162, v162\n\t" \
    "v_exp_f32 v163, v163\n\t" \
    "v_mfma_f32_32x32x16_bf16 %[o3], v[228:231], v[196:199], %[o3]\n\t" \
    "v_exp_f32 v164, v164\n\t" \
    "v_cvt_pk_bf16_f32 v192, v160, v161\n\t" \
    "v_exp_f32 v165, v165\n\t" \
    "v_cvt_pk_bf16_f32 v193, v162, v163\n\t" \
    "v_exp_f32 v166, v166\n\t" \
    "v_exp_f32 v167, v167\n\t" \
    "v_mfma_f32_32x32x16_bf16 v[176:191], v[204:207], %[qm1], v[176:191]\n\t" \
    "global_load_dwordx4 v[224:227], %[o16], s[42:43] sc1\n\t" \
    "global_load_dwordx4 v[228:231], %[o16], s[42:43] offset:1024 sc1\n\t" \
    "s_add_u32 s42, s42, 2048\n\t" \
    "s_addc_u32 s43, s43, 0\n\t" \
    "v_exp_f32 v168, v168\n\t" \
    "v_cvt_pk_bf16_f32 v194, v164, v165\n\t" \
    "v_exp_f32 v169, v169\n\t" \
    "v_cvt_pk_bf16_f32 v195, v166, v167\n\t" \
    "v_exp_f32 v170, v170\n\t" \
    "v_exp_f32 v171, v171\n\t" \
    "s_waitcnt vmcnt(28)\n\t" \
    "v_mfma_f32_32x32x16_bf16 %[o0], v[232:235], v[192:195], %[o0]\n\t" \
    "v_exp_f32 v172, v172\n\t" \
    "v_cvt_pk_bf16_f32 v196, v168, v169\n\t" \
    "v_exp_f32 v173, v173\n\t" \
    "v_cvt_pk_bf16_f32 v197, v170, v171\n\t" \
    "v_exp_f32 v174, v174\n\t" \
    "v_exp_f32 v175, v175\n\t" \
    "v_cvt_pk_bf16_f32 v198, v172, v173\n\t" \
    "v_cvt_pk_bf16_f32 v199, v174, v175\n\t" \
    "v_mfma_f32_32x32x8_bf16 v[160:175], v[218:219], %[qr2], 0\n\t" \
    "v_exp_f32 v176, v176\n\t" \
    "v_exp_f32 v177, v177\n\t" \
    "v_exp_f32 v178, v178\n\t" \
    "v_exp_f32 v179, v179\n\t" \
    "v_mfma_f32_32x32x16_bf16 %[o0], v[236:239], v[196:199], %[o0]\n\t" \
    "v_exp_f32 v180, v180\n\t" \
    "v_cvt_pk_bf16_f32 v192, v176, v177\n\t" \
    "v_exp_f32 v181, v181\n\t" \
    "v_cvt_pk_bf16_f32 v193, v178, v179\n\t" \
    "v_exp_f32 v182, v182\n\t" \
    "v_exp_f32 v183, v183\n\t" \
    "v_mfma_f32_32x32x16_bf16 v[160:175], v[204:207], %[qm2], v[160:175]\n\t" \
    "s_mul_i32 s48, s47, 1280\n\t" \
    "s_add_u32 s44, s49, s48\n\t" \
    "s_addc_u32 s45, s50, 0\n\t" \
    "s_mul_i32 s48, s47, 160\n\t" \
    "s_add_u32 m0, %[lw], s48\n\t" \
    "s_mov_b64 exec, s[52:53]\n\t" \
    "global_load_lds_dwordx4 %[ov], s[44:45]\n\t" \
    "s_mov_b64 exec, -1\n\t" \
    "s_add_u32 s47, s47, 6\n\t" \
    "s_min_u32 s47, s47, 634\n\t" \
    "v_exp_f32 v184, v184\n\t" \
    "v_cvt_pk_bf16_f32 v194, v180, v181\n\t" \
    "v_exp_f32 v185, v185\n\t" \
    "v_cvt_pk_bf16_f32 v195, v182, v183\n\t" \
    "v_exp_f32 v186, v186\n\t" \
    "v_exp_f32 v187, v187\n\t" \
    "v_mfma_f32_32x32x16_bf16 %[o1], v[232:235], v[192:195], %[o1]\n\t" \
    "v_exp_f32 v188, v188\n\t" \
    "v_cvt_pk_bf16_f32 v196, v184, v185\n\t" \
    "v_exp_f32 v189, v189\n\t" \
    "v_cvt_pk_bf16_f32 v197, v186, v187\n\t" \
    "v_exp_f32 v190, v190\n\t" \
    "v_exp_f32 v191, v191\n\t" \
    "v_cvt_pk_bf16_f32 v198, v188, v189\n\t" \
    "v_cvt_pk_bf16_f32 v199, v190, v191\n\t" \
    "v_mfma_f32_32x32x8_bf16 v[176:191], v[218:219], %[qr3], 0\n\t" \
    "v_exp_f32 v160, v160\n\t" \
    "v_exp_f32 v161, v161\n\t" \
    "v_exp_f32 v162, v162\n\t" \
    "v_exp_f32 v163, v163\n\t" \
    "v_mfma_f32_32x32x16_bf16 %[o1], v[236:239], v[196:199], %[o1]\n\t" \
    "v_exp_f32 v164, v164\n\t" \
    "v_cvt_pk_bf16_f32 v192, v160, v161\n\t" \
    "v_exp_f32 v165, v165\n\t" \
    "v_cvt_pk_bf16_f32 v193, v162, v163\n\t" \
    "v_exp_f32 v166, v166\n\t" \
    "v_exp_f32 v167, v167\n\t" \
    "v_mfma_f32_32x32x16_bf16 v[176:191], v[204:207], %[qm3], v[176:191]\n\t" \
    "s_mul_i32 s48, s47, 1280\n\t" \
    "s_add_u32 s44, s49, s48\n\t" \
    "s_addc_u32 s45, s50, 0\n\t" \
    "s_mul_i32 s48, s47, 160\n\t" \
    "s_add_u32 m0, %[lw], s48\n\t" \
    "s_mov_b64 exec, s[52:53]\n\t" \
    "global_load_lds_dwordx4 %[ov], s[44:45]\n\t" \
    "s_mov_b64 exec, -1\n\t" \
    "s_add_u32 s47, s47, 6\n\t" \
    "s_min_u32 s47, s47, 634\n\t" \
    "v_exp_f32 v168, v168\n\t" \
    "v_cvt_pk_bf16_f32 v194, v164, v165\n\t" \
    "v_exp_f32 v169, v169\n\t" \
    "v_cvt_pk_bf16_f32 v195, v166, v167\n\t" \
    "v_exp_f32 v170, v170\n\t" \
    "v_exp_f32 v171, v171\n\t" \
    "v_mfma_f32_32x32x16_bf16 %[o2], v[232:235], v[192:195], %[o2]\n\t" \
    "v_exp_f32 v172, v172\n\t" \
    "v_cvt_pk_bf16_f32 v196, v168, v169\n\t" \
    "v_exp_f32 v173, v173\n\t" \
    "v_cvt_pk_bf16_f32 v197, v170, v171\n\t" \
    "v_exp_f32 v174, v174\n\t" \
    "v_exp_f32 v175, v175\n\t" \
    "v_cvt_pk_bf16_f32 v198, v172, v173\n\t" \
    "v_cvt_pk_bf16_f32 v199, v174, v175\n\t" \
    "s_waitcnt vmcnt(26)\n\t" \
    "v_mfma_f32_32x32x8_bf16 v[160:175], v[220:221], %[qr0], 0\n\t" \
    "v_exp_f32 v176, v176\n\t" \
    "v_exp_f32 v177, v177\n\t" \
    "v_exp_f32 v178, v178\n\t" \
    "v_exp_f32 v179, v179\n\t" \
    "v_mfma_f32_32x32x16_bf16 %[o2], v[236:239], v[196:199], %[o2]\n\t" \
    "v_exp_f32 v180, v180\n\t" \
    "v_cvt_pk_bf16_f32 v192, v176, v177\n\t" \
    "v_exp_f32 v181, v181\n\t" \
    "v_cvt_pk_bf16_f32 v193, v178, v179\n\t" \
    "v_exp_f32 v182, v182\n\t" \
    "v_exp_f32 v183, v183\n\t" \
    "v_mfma_f32_32x32x16_bf16 v[160:175], v[208:211], %[qm0], v[160:175]\n\t" \
    "global_load_dwordx4 v[204:207], %[o16], s[40:41] sc1\n\t" \
    "global_load_dwordx2 v[218:219], %[o8], s[40:41] offset:1024 sc1\n\t" \
    "s_add_u32 s40, s40, 1536\n\t" \
    "s_addc_u32 s41, s41, 0\n\t" \
    "v_exp_f32 v184, v184\n\t" \
    "v_cvt_pk_bf16_f32 v194, v180, v181\n\t" \
    "v_exp_f32 v185, v185\n\t" \
    "v_cvt_pk_bf16_f32 v195, v182, v183\n\t" \
    "v_exp_f32 v186, v186\n\t" \
    "v_exp_f32 v187, v187\n\t" \
    "v_mfma_f32_32x32x16_bf16 %[o3], v[232:235], v[192:195], %[o3]\n\t" \
    "v_exp_f32 v188, v188\n\t" \
    "v_cvt_pk_bf16_f32 v196, v184, v185\n\t" \
    "v_exp_f32 v189, v189\n\t" \
    "v_cvt_pk_bf16_f32 v197, v186, v187\n\t" \
    "v_exp_f32 v190, v190\n\t" \
    "v_exp_f32 v191, v191\n\t" \
    "v_cvt_pk_bf16_f32 v198, v188, v189\n\t" \
    "v_cvt_pk_bf16_f32 v199, v190, v191\n\t" \
    "v_mfma_f32_32x32x8_bf16 v[176:191], v[220:221], %[qr1], 0\n\t" \
    "v_exp_f32 v160, v160\n\t" \
    "v_exp_f32 v161, v161\n\t" \
    "v_exp_f32 v162, v162\n\t" \
    "v_exp_f32 v163, v163\n\t" \
    "v_mfma_f32_32x32x16_bf16 %[o3], v[236:239], v[196:199], %[o3]\n\t" \
    "v_exp_f32 v164, v164\n\t" \
    "v_cvt_pk_bf16_f32 v192, v160, v161\n\t" \
    "v_exp_f32 v165, v165\n\t" \
    "v_cvt_pk_bf16_f32 v193, v162, v163\n\t" \
    "v_exp_f32 v166, v166\n\t" \
    "v_exp_f32 v167, v167\n\t" \
    "v_mfma_f32_32x32x16_bf16 v[176:191], v[208:211], %[qm1], v[176:191]\n\t" \
    "global_load_dwordx4 v[232:235], %[o16], s[42:43] sc1\n\t" \
    "global_load_dwordx4 v[236:239], %[o16], s[42:43] offset:1024 sc1\n\t" \
    "s_add_u32 s42, s42, 2048\n\t" \
    "s_addc_u32 s43, s43, 0\n\t" \
    "v_exp_f32 v168, v168\n\t" \
    "v_cvt_pk_bf16_f32 v194, v164, v165\n\t" \
    "v_exp_f32 v169, v169\n\t" \
    "v_cvt_pk_bf16_f32 v195, v166, v167\n\t" \
    "v_exp_f32 v170, v170\n\t" \
    "v_exp_f32 v171, v171\n\t" \
    "s_waitcnt vmcnt(27)\n\t" \
    "v_mfma_f32_32x32x16_bf16 %[o0], v[240:243], v[192:195], %[o0]\n\t" \
    "v_exp_f32 v172, v172\n\t" \
    "v_cvt_pk_bf16_f32 v196, v168, v169\n\t" \
    "v_exp_f32 v173, v173\n\t" \
    "v_cvt_pk_bf16_f32 v197, v170, v171\n\t" \
    "v_exp_f32 v174, v174\n\t" \
    "v_exp_f32 v175, v175\n\t" \
    "v_cvt_pk_bf16_f32 v198, v172, v173\n\t" \
    "v_cvt_pk_bf16_f32 v199, v174, v175\n\t" \
    "v_mfma_f32_32x32x8_bf16 v[160:175], v[220:221], %[qr2], 0\n\t" \
    "v_exp_f32 v176, v176\n\t" \
    "v_exp_f32 v177, v177\n\t" \
    "v_exp_f32 v178, v178\n\t" \
    "v_exp_f32 v179, v179\n\t" \
    "v_mfma_f32_32x32x16_bf16 %[o0], v[244:247], v[196:199], %[o0]\n\t" \
    "v_exp_f32 v180, v180\n\t" \
    "v_cvt_pk_bf16_f32 v192, v176, v177\n\t" \
    "v_exp_f32 v181, v181\n\t" \
    "v_cvt_pk_bf16_f32 v193, v178, v179\n\t" \
    "v_exp_f32 v182, v182\n\t" \
    "v_exp_f32 v183, v183\n\t" \
    "v_mfma_f32_32x32x16_bf16 v[160:175], v[208:211], %[qm2], v[160:175]\n\t" \
    "s_mul_i32 s48, s47, 1280\n\t" \
    "s_add_u32 s44, s49, s48\n\t" \
    "s_addc_u32 s45, s50, 0\n\t" \
    "s_mul_i32 s48, s47, 160\n\t" \
    "s_add_u32 m0, %[lw], s48\n\t" \
    "s_mov_b64 exec, s[52:53]\n\t" \
    "global_load_lds_dwordx4 %[ov], s[44:45]\n\t" \
    "s_mov_b64 exec, -1\n\t" \
    "s_add_u32 s47, s47, 6\n\t" \
    "s_min_u32 s47, s47, 634\n\t" \
    "v_exp_f32 v184, v184\n\t" \
    "v_cvt_pk_bf16_f32 v194, v180, v181\n\t" \
    "v_exp_f32 v185, v185\n\t" \
    "v_cvt_pk_bf16_f32 v195, v182, v183\n\t" \
    "v_exp_f32 v186, v186\n\t" \
    "v_exp_f32 v187, v187\n\t" \
    "v_mfma_f32_32x32x16_bf16 %[o1], v[240:243], v[192:195], %[o1]\n\t" \
    "v_exp_f32 v188, v188\n\t" \
    "v_cvt_pk_bf16_f32 v196, v184, v185\n\t" \
    "v_exp_f32 v189, v189\n\t" \
    "v_cvt_pk_bf16_f32 v197, v186, v187\n\t" \
    "v_exp_f32 v190, v190\n\t" \
    "v_exp_f32 v191, v191\n\t" \
    "v_cvt_pk_bf16_f32 v198, v188, v189\n\t" \
    "v_cvt_pk_bf16_f32 v199, v190, v191\n\t" \
    "v_mfma_f32_32x32x8_bf16 v[176:191], v[220:221], %[qr3], 0\n\t" \
    "v_exp_f32 v160, v160\n\t" \
    "v_exp_f32 v161, v161\n\t" \
    "v_exp_f32 v162, v162\n\t" \
    "v_exp_f32 v163, v163\n\t" \
    "v_mfma_f32_32x32x16_bf16 %[o1], v[244:247], v[196:199], %[o1]\n\t" \
    "v_exp_f32 v164, v164\n\t" \
    "v_cvt_pk_bf16_f32 v192, v160, v161\n\t" \
    "v_exp_f32 v165, v165\n\t" \
    "v_cvt_pk_bf16_f32 v193, v162, v163\n\t" \
    "v_exp_f32 v166, v166\n\t" \
    "v_exp_f32 v167, v167\n\t" \
    "v_mfma_f32_32x32x16_bf16 v[176:191], v[208:211], %[qm3], v[176:191]\n\t" \
    "s_mul_i32 s48, s47, 1280\n\t" \
    "s_add_u32 s44, s49, s48\n\t" \
    "s_addc_u32 s45, s50, 0\n\t" \
    "s_mul_i32 s48, s47, 160\n\t" \
    "s_add_u32 m0, %[lw], s48\n\t" \
    "s_mov_b64 exec, s[52:53]\n\t" \
    "global_load_lds_dwordx4 %[ov], s[44:45]\n\t" \
    "s_mov_b64 exec, -1\n\t" \
    "s_add_u32 s47, s47, 6\n\t" \
    "s_min_u32 s47, s47, 634\n\t" \
    "v_exp_f32 v168, v168\n\t" \
    "v_cvt_pk_bf16_f32 v194, v164, v165\n\t" \
    "v_exp_f32 v169, v169\n\t" \
    "v_cvt_pk_bf16_f32 v195, v166, v167\n\t" \
    "v_exp_f32 v170, v170\n\t" \
    "v_exp_f32 v171, v171\n\t" \
    "v_mfma_f32_32x32x16_bf16 %[o2], v[240:243], v[192:195], %[o2]\n\t" \
    "v_exp_f32 v172, v172\n\t" \
    "v_cvt_pk_bf16_f32 v196, v168, v169\n\t" \
    "v_exp_f32 v173, v173\n\t" \
    "v_cvt_pk_bf16_f32 v197, v170, v171\n\t" \
    "v_exp_f32 v174, v174\n\t" \
    "v_exp_f32 v175, v175\n\t" \
    "v_cvt_pk_bf16_f32 v198, v172, v173\n\t" \
    "v_cvt_pk_bf16_f32 v199, v174, v175\n\t" \
    "s_waitcnt vmcnt(24)\n\t" \
    "v_mfma_f32_32x32x8_bf16 v[160:175], v[222:223], %[qr0], 0\n\t" \
    "v_exp_f32 v176, v176\n\t" \
    "v_exp_f32 v177, v177\n\t" \
    "v_exp_f32 v178, v178\n\t" \
    "v_exp_f32 v179, v179\n\t" \
    "v_mfma_f32_32x32x16_bf16 %[o2], v[244:247], v[196:199], %[o2]\n\t" \
    "v_exp_f32 v180, v180\n\t" \
    "v_cvt_pk_bf16_f32 v192, v176, v177\n\t" \
    "v_exp_f32 v181, v181\n\t" \
    "v_cvt_pk_bf16_f32 v193, v178, v179\n\t" \
    "v_exp_f32 v182, v182\n\t" \
    "v_exp_f32 v183, v183\n\t" \
    "v_mfma_f32_32x32x16_bf16 v[160:175], v[212:215], %[qm0], v[160:175]\n\t" \
    "global_load_dwordx4 v[208:211], %[o16], s[40:41] sc1\n\t" \
    "global_load_dwordx2 v[220:221], %[o8], s[40:41] offset:1024 sc1\n\t" \
    "s_add_u32 s40, s40, 1536\n\t" \
    "s_addc_u32 s41, s41, 0\n\t" \
    "v_exp_f32 v184, v184\n\t" \
    "v_cvt_pk_bf16_f32 v194, v180, v181\n\t" \
    "v_exp_f32 v185, v185\n\t" \
    "v_cvt_pk_bf16_f32 v195, v182, v183\n\t" \
    "v_exp_f32 v186, v186\n\t" \
    "v_exp_f32 v187, v187\n\t" \
    "v_mfma_f32_32x32x16_bf16 %[o3], v[240:243], v[192:195], %[o3]\n\t" \
    "v_exp_f32 v188, v188\n\t" \
    "v_cvt_pk_bf16_f32 v196, v184, v185\n\t" \
    "v_exp_f32 v189, v189\n\t" \
    "v_cvt_pk_bf16_f32 v197, v186, v187\n\t" \
    "v_exp_f32 v190, v190\n\t" \
    "v_exp_f32 v191, v191\n\t" \
    "v_cvt_pk_bf16_f32 v198, v188, v189\n\t" \
    "v_cvt_pk_bf16_f32 v199, v190, v191\n\t" \
    "v_mfma_f32_32x32x8_bf16 v[176:191], v[222:223], %[qr1], 0\n\t" \
    "v_exp_f32 v160, v160\n\t" \
    "v_exp_f32 v161, v161\n\t" \
    "v_exp_f32 v162, v162\n\t" \
    "v_exp_f32 v163, v163\n\t" \
    "v_mfma_f32_32x32x16_bf16 %[o3], v[244:247], v[196:199], %[o3]\n\t" \
    "v_exp_f32 v164, v164\n\t" \
    "v_cvt_pk_bf16_f32 v192, v160, v161\n\t" \
    "v_exp_f32 v165, v165\n\t" \
    "v_cvt_pk_bf16_f32 v193, v162, v163\n\t" \
    "v_exp_f32 v166, v166\n\t" \
    "v_exp_f32 v167, v167\n\t" \
    "v_mfma_f32_32x32x16_bf16 v[176:191], v[212:215], %[qm1], v[176:191]\n\t" \
    "global_load_dwordx4 v[240:243], %[o16], s[42:43] sc1\n\t" \
    "global_load_dwordx4 v[244:247], %[o16], s[42:43] offset:1024 sc1\n\t" \
    "s_add_u32 s42, s42, 2048\n\t" \
    "s_addc_u32 s43, s43, 0\n\t" \
    "v_exp_f32 v168, v168\n\t" \
    "v_cvt_pk_bf16_f32 v194, v164, v165\n\t" \
    "v_exp_f32 v169, v169\n\t" \
    "v_cvt_pk_bf16_f32 v195, v166, v167\n\t" \
    "v_exp_f32 v170, v170\n\t" \
    "v_exp_f32 v171, v171\n\t" \
    "s_waitcnt vmcnt(18)\n\t" \
    "v_mfma_f32_32x32x16_bf16 %[o0], v[248:251], v[192:195], %[o0]\n\t" \
    "v_exp_f32 v172, v172\n\t" \
    "v_cvt_pk_bf16_f32 v196, v168, v169\n\t" \
    "v_exp_f32 v173, v173\n\t" \
    "v_cvt_pk_bf16_f32 v197, v170, v171\n\t" \
    "v_exp_f32 v174, v174\n\t" \
    "v_exp_f32 v175, v175\n\t" \
    "v_cvt_pk_bf16_f32 v198, v172, v173\n\t" \
    "v_cvt_pk_bf16_f32 v199, v174, v175\n\t" \
    "v_mfma_f32_32x32x8_bf16 v[160:175], v[222:223], %[qr2], 0\n\t" \
    "v_exp_f32 v176, v176\n\t" \
    "v_exp_f32 v177, v177\n\t" \
    "v_exp_f32 v178, v178\n\t" \
    "v_exp_f32 v179, v179\n\t" \
    "v_mfma_f32_32x32x16_bf16 %[o0], v[252:255], v[196:199], %[o0]\n\t" \
    "v_exp_f32 v180, v180\n\t" \
    "v_cvt_pk_bf16_f32 v192, v176, v177\n\t" \
    "v_exp_f32 v181, v181\n\t" \
    "v_cvt_pk_bf16_f32 v193, v178, v179\n\t" \
    "v_exp_f32 v182, v182\n\t" \
    "v_exp_f32 v183, v183\n\t" \
    "v_mfma_f32_32x32x16_bf16 v[160:175], v[212:215], %[qm2], v[160:175]\n\t" \
    "s_mul_i32 s48, s47, 1280\n\t" \
    "s_add_u32 s44, s49, s48\n\t" \
    "s_addc_u32 s45, s50, 0\n\t" \
    "s_mul_i32 s48, s47, 160\n\t" \
    "s_add_u32 m0, %[lw], s48\n\t" \
    "s_mov_b64 exec, s[52:53]\n\t" \
    "global_load_lds_dwordx4 %[ov], s[44:45]\n\t" \
    "s_mov_b64 exec, -1\n\t" \
    "s_add_u32 s47, s47, 6\n\t" \
    "s_min_u32 s47, s47, 634\n\t" \
    "v_exp_f32 v184, v184\n\t" \
    "v_cvt_pk_bf16_f32 v194, v180, v181\n\t" \
    "v_exp_f32 v185, v185\n\t" \
    "v_cvt_pk_bf16_f32 v195, v182, v183\n\t" \
    "v_exp_f32 v186, v186\n\t" \
    "v_exp_f32 v187, v187\n\t" \
    "v_mfma_f32_32x32x16_bf16 %[o1], v[248:251], v[192:195], %[o1]\n\t" \
    "v_exp_f32 v188, v188\n\t" \
    "v_cvt_pk_bf16_f32 v196, v184, v185\n\t" \
    "v_exp_f32 v189, v189\n\t" \
    "v_cvt_pk_bf16_f32 v197, v186, v187\n\t" \
    "v_exp_f32 v190, v190\n\t" \
    "v_exp_f32 v191, v191\n\t" \
    "v_cvt_pk_bf16_f32 v198, v188, v189\n\t" \
    "v_cvt_pk_bf16_f32 v199, v190, v191\n\t" \
    "v_mfma_f32_32x32x8_bf16 v[176:191], v[222:223], %[qr3], 0\n\t" \
    "v_exp_f32 v160, v160\n\t" \
    "v_exp_f32 v161, v161\n\t" \
    "v_exp_f32 v162, v162\n\t" \
    "v_exp_f32 v163, v163\n\t" \
    "v_mfma_f32_32x32x16_bf16 %[o1], v[252:255], v[196:199], %[o1]\n\t" \
    "v_exp_f32 v164, v164\n\t" \
    "v_cvt_pk_bf16_f32 v192, v160, v161\n\t" \
    "v_exp_f32 v165, v165\n\t" \
    "v_cvt_pk_bf16_f32 v193, v162, v163\n\t" \
    "v_exp_f32 v166, v166\n\t" \
    "v_exp_f32 v167, v167\n\t" \
    "v_mfma_f32_32x32x16_bf16 v[176:191], v[212:215], %[qm3], v[176:191]\n\t" \
    "v_exp_f32 v168, v168\n\t" \
    "v_cvt_pk_bf16_f32 v194, v164, v165\n\t" \
    "v_exp_f32 v169, v169\n\t" \
    "v_cvt_pk_bf16_f32 v195, v166, v167\n\t" \
    "v_exp_f32 v170, v170\n\t" \
    "v_exp_f32 v171, v171\n\t" \
    "v_mfma_f32_32x32x16_bf16 %[o2], v[248:251], v[192:195], %[o2]\n\t" \
    "v_exp_f32 v172, v172\n\t" \
    "v_cvt_pk_bf16_f32 v196, v168, v169\n\t" \
    "v_exp_f32 v173, v173\n\t" \
    "v_cvt_pk_bf16_f32 v197, v170, v171\n\t" \
    "v_exp_f32 v174, v174\n\t" \
    "v_exp_f32 v175, v175\n\t" \
    "v_cvt_pk_bf16_f32 v198, v172, v173\n\t" \
    "v_cvt_pk_bf16_f32 v199, v174, v175\n\t" \
    "s_waitcnt vmcnt(15)\n\t" \
    "v_mfma_f32_32x32x8_bf16 v[160:175], v[216:217], %[qr0], 0\n\t" \
    "v_exp_f32 v176, v176\n\t" \
    "v_exp_f32 v177, v177\n\t" \
    "v_exp_f32 v178, v178\n\t" \
    "v_exp_f32 v179, v179\n\t" \
    "v_mfma_f32_32x32x16_bf16 %[o2], v[252:255], v[196:199], %[o2]\n\t" \
    "v_exp_f32 v180, v180\n\t" \
    "v_cvt_pk_bf16_f32 v192, v176, v177\n\t" \
    "v_exp_f32 v181, v181\n\t" \
    "v_cvt_pk_bf16_f32 v193, v178, v179\n\t" \
    "v_exp_f32 v182, v182\n\t" \
    "v_exp_f32 v183, v183\n\t" \
    "v_mfma_f32_32x32x16_bf16 v[160:175], v[200:203], %[qm0], v[160:175]\n\t" \
    "global_load_dwordx4 v[212:215], %[o16], s[40:41] sc1\n\t" \
    "global_load_dwordx2 v[222:223], %[o8], s[40:41] offset:1024 sc1\n\t" \
    "s_add_u32 s40, s40, 1536\n\t" \
    "s_addc_u32 s41, s41, 0\n\t" \
    "v_exp_f32 v184, v184\n\t" \
    "v_cvt_pk_bf16_f32 v194, v180, v181\n\t" \
    "v_exp_f32 v185, v185\n\t" \
    "v_cvt_pk_bf16_f32 v195, v182, v183\n\t" \
    "v_exp_f32 v186, v186\n\t" \
    "v_exp_f32 v187, v187\n\t" \
    "v_mfma_f32_32x32x16_bf16 %[o3], v[248:251], v[192:195], %[o3]\n\t" \
    "v_exp_f32 v188, v188\n\t" \
    "v_cvt_pk_bf16_f32 v196, v184, v185\n\t" \
    "v_exp_f32 v189, v189\n\t" \
    "v_cvt_pk_bf16_f32 v197, v186, v187\n\t" \
    "v_exp_f32 v190, v190\n\t" \
    "v_exp_f32 v191, v191\n\t" \
    "s_mov_b32 m0, s54\n\t" \
    "s_mov_b64 %[kb], s[40:41]\n\t" \
    "s_mov_b64 %[vb], s[42:43]\n\t" \
    "s_mov_b32 %[wr], s47\n\t"
#define ATTN_SEG3_ASM \
    "s_mov_b64 s[40:41], %[kb]\n\t" \
    "s_mov_b64 s[42:43], %[vb]\n\t" \
    "s_mov_b32 s54, m0\n\t" \
    "v_cvt_pk_bf16_f32 v198, v188, v189\n\t" \
    "v_cvt_pk_bf16_f32 v199, v190, v191\n\t" \
    "v_mfma_f32_32x32x8_bf16 v[176:191], v[216:217], %[qr1], 0\n\t" \
    "v_exp_f32 v160, v160\n\t" \
    "v_exp_f32 v161, v161\n\t" \
    "v_exp_f32 v162, v162\n\t" \
    "v_exp_f32 v163, v163\n\t" \
    "v_mfma_f32_32x32x16_bf16 %[o3], v[252:255], v[196:199], %[o3]\n\t" \
    "v_exp_f32 v164, v164\n\t" \
    "v_cvt_pk_bf16_f32 v192, v160, v161\n\t" \
    "v_exp_f32 v165, v165\n\t" \
    "v_cvt_pk_bf16_f32 v193, v162, v163\n\t" \
    "v_exp_f32 v166, v166\n\t" \
    "v_exp_f32 v167, v167\n\t" \
    "v_mfma_f32_32x32x16_bf16 v[176:191], v[200:203], %[qm1], v[176:191]\n\t" \
    "global_load_dwordx4 v[248:251], %[o16], s[42:43] sc1\n\t" \
    "global_load_dwordx4 v[252:255], %[o16], s[42:43] offset:1024 sc1\n\t" \
    "s_add_u32 s42, s42, 2048\n\t" \
    "s_addc_u32 s43, s43, 0\n\t" \
    "v_exp_f32 v168, v168\n\t" \
    "v_cvt_pk_bf16_f32 v194, v164, v165\n\t" \
    "v_exp_f32 v169, v169\n\t" \
    "v_cvt_pk_bf16_f32 v195, v166, v167\n\t" \
    "v_exp_f32 v170, v170\n\t" \
    "v_exp_f32 v171, v171\n\t" \
    "s_waitcnt vmcnt(17)\n\t" \
    "v_mfma_f32_32x32x16_bf16 %[o0], v[224:227], v[192:195], %[o0]\n\t" \
    "v_exp_f32 v172, v172\n\t" \
    "v_cvt_pk_bf16_f32 v196, v168, v169\n\t" \
    "v_exp_f32 v173, v173\n\t" \
    "v_cvt_pk_bf16_f32 v197, v170, v171\n\t" \
    "v_exp_f32 v174, v174\n\t" \
    "v_exp_f32 v175, v175\n\t" \
    "v_cvt_pk_bf16_f32 v198, v172, v173\n\t" \
    "v_cvt_pk_bf16_f32 v199, v174, v175\n\t" \
    "v_mfma_f32_32x32x8_bf16 v[160:175], v[216:217], %[qr2], 0\n\t" \
    "v_exp_f32 v176, v176\n\t" \
    "v_exp_f32 v177, v177\n\t" \
    "v_exp_f32 v178, v178\n\t" \
    "v_exp_f32 v179, v179\n\t" \
    "v_mfma_f32_32x32x16_bf16 %[o0], v[228:231], v[196:199], %[o0]\n\t" \
    "v_exp_f32 v180, v180\n\t" \
    "v_cvt_pk_bf16_f32 v192, v176, v177\n\t" \
    "v_exp_f32 v181, v181\n\t" \
    "v_cvt_pk_bf16_f32 v193, v178, v179\n\t" \
    "v_exp_f32 v182, v182\n\t" \
    "v_exp_f32 v183, v183\n\t" \
    "v_mfma_f32_32x32x16_bf16 v[160:175], v[200:203], %[qm2], v[160:175]\n\t" \
    "v_exp_f32 v184, v184\n\t" \
    "v_cvt_pk_bf16_f32 v194, v180, v181\n\t" \
    "v_exp_f32 v185, v185\n\t" \
    "v_cvt_pk_bf16_f32 v195, v182, v183\n\t" \
    "v_exp_f32 v186, v186\n\t" \
    "v_exp_f32 v187, v187\n\t" \
    "v_mfma_f32_32x32x16_bf16 %[o1], v[224:227], v[192:195], %[o1]\n\t" \
    "v_exp_f32 v188, v188\n\t" \
    "v_cvt_pk_bf16_f32 v196, v184, v185\n\t" \
    "v_exp_f32 v189, v189\n\t" \
    "v_cvt_pk_bf16_f32 v197, v186, v187\n\t" \
    "v_exp_f32 v190, v190\n\t" \
    "v_exp_f32 v191, v191\n\t" \
    "v_cvt_pk_bf16_f32 v198, v188, v189\n\t" \
    "v_cvt_pk_bf16_f32 v199, v190, v191\n\t" \
    "v_mfma_f32_32x32x8_bf16 v[176:191], v[216:217], %[qr3], 0\n\t" \
    "v_exp_f32 v160, v160\n\t" \
    "v_exp_f32 v161, v161\n\t" \
    "v_exp_f32 v162, v162\n\t" \
    "v_exp_f32 v163, v163\n\t" \
    "v_mfma_f32_32x32x16_bf16 %[o1], v[228:231], v[196:199], %[o1]\n\t" \
    "v_exp_f32 v164, v164\n\t" \
    "v_cvt_pk_bf16_f32 v192, v160, v161\n\t" \
    "v_exp_f32 v165, v165\n\t" \
    "v_cvt_pk_bf16_f32 v193, v162, v163\n\t" \
    "v_exp_f32 v166, v166\n\t" \
    "v_exp_f32 v167, v167\n\t" \
    "v_mfma_f32_32x32x16_bf16 v[176:191], v[200:203], %[qm3], v[176:191]\n\t" \
    "v_exp_f32 v168, v168\n\t" \
    "v_cvt_pk_bf16_f32 v194, v164, v165\n\t" \
    "v_exp_f32 v169, v169\n\t" \
    "v_cvt_pk_bf16_f32 v195, v166, v167\n\t" \
    "v_exp_f32 v170, v170\n\t" \
    "v_exp_f32 v171, v171\n\t" \
    "v_mfma_f32_32x32x16_bf16 %[o2], v[224:227], v[192:195], %[o2]\n\t" \
    "v_exp_f32 v172, v172\n\t" \
    "v_cvt_pk_bf16_f32 v196, v168, v169\n\t" \
    "v_exp_f32 v173, v173\n\t" \
    "v_cvt_pk_bf16_f32 v197, v170, v171\n\t" \
    "v_exp_f32 v174, v174\n\t" \
    "v_exp_f32 v175, v175\n\t" \
    "v_cvt_pk_bf16_f32 v198, v172, v173\n\t" \
    "v_cvt_pk_bf16_f32 v199, v174, v175\n\t" \
    "s_waitcnt vmcnt(13)\n\t" \
    "v_mfma_f32_32x32x8_bf16 v[160:175], v[218:219], %[qr0], 0\n\t" \
    "v_exp_f32 v176, v176\n\t" \
    "v_exp_f32 v177, v177\n\t" \
    "v_exp_f32 v178, v178\n\t" \
    "v_exp_f32 v179, v179\n\t" \
    "v_mfma_f32_32x32x16_bf16 %[o2], v[228:231], v[196:199], %[o2]\n\t" \
    "v_exp_f32 v180, v180\n\t" \
    "v_cvt_pk_bf16_f32 v192, v176, v177\n\t" \
    "v_exp_f32 v181, v181\n\t" \
    "v_cvt_pk_bf16_f32 v193, v178, v179\n\t" \
    "v_exp_f32 v182, v182\n\t" \
    "v_exp_f32 v183, v183\n\t" \
    "v_mfma_f32_32x32x16_bf16 v[160:175], v[204:207], %[qm0], v[160:175]\n\t" \
    "global_load_dwordx4 v[200:203], %[o16], s[40:41] sc1\n\t" \
    "global_load_dwordx2 v[216:217], %[o8], s[40:41] offset:1024 sc1\n\t" \
    "s_add_u32 s40, s40, 1536\n\t" \
    "s_addc_u32 s41, s41, 0\n\t" \
    "v_exp_f32 v184, v184\n\t" \
    "v_cvt_pk_bf16_f32 v194, v180, v181\n\t" \
    "v_exp_f32 v185, v185\n\t" \
    "v_cvt_pk_bf16_f32 v195, v182, v183\n\t" \
    "v_exp_f32 v186, v186\n\t" \
    "v_exp_f32 v187, v187\n\t" \
    "v_mfma_f32_32x32x16_bf16 %[o3], v[224:227], v[192:195], %[o3]\n\t" \
    "v_exp_f32 v188, v188\n\t" \
    "v_cvt_pk_bf16_f32 v196, v184, v185\n\t" \
    "v_exp_f32 v189, v189\n\t" \
    "v_cvt_pk_bf16_f32 v197, v186, v187\n\t" \
    "v_exp_f32 v190, v190\n\t" \
    "v_exp_f32 v191, v191\n\t" \
    "v_cvt_pk_bf16_f32 v198, v188, v189\n\t" \
    "v_cvt_pk_bf16_f32 v199, v190, v191\n\t" \
    "v_mfma_f32_32x32x8_bf16 v[176:191], v[218:219], %[qr1], 0\n\t" \
    "v_exp_f32 v160, v160\n\t" \
    "v_exp_f32 v161, v161\n\t" \
    "v_exp_f32 v162, v162\n\t" \
    "v_exp_f32 v163, v163\n\t" \
    "v_mfma_f32_32x32x16_bf16 %[o3], v[228:231], v[196:199], %[o3]\n\t" \
    "v_exp_f32 v164, v164\n\t" \
    "v_cvt_pk_bf16_f32 v192, v160, v161\n\t" \
    "v_exp_f32 v165, v165\n\t" \
    "v_cvt_pk_bf16_f32 v193, v162, v163\n\t" \
    "v_exp_f32 v166, v166\n\t" \
    "v_exp_f32 v167, v167\n\t" \
    "v_mfma_f32_32x32x16_bf16 v[176:191], v[204:207], %[qm1], v[176:191]\n\t" \
    "global_load_dwordx4 v[224:227], %[o16], s[42:43] sc1\n\t" \
    "global_load_dwordx4 v[228:231], %[o16], s[42:43] offset:1024 sc1\n\t" \
    "s_add_u32 s42, s42, 2048\n\t" \
    "s_addc_u32 s43, s43, 0\n\t" \
    "v_exp_f32 v168, v168\n\t" \
    "v_cvt_pk_bf16_f32 v194, v164, v165\n\t" \
    "v_exp_f32 v169, v169\n\t" \
    "v_cvt_pk_bf16_f32 v195, v166, v167\n\t" \
    "v_exp_f32 v170, v170\n\t" \
    "v_exp_f32 v171, v171\n\t" \
    "s_waitcnt vmcnt(15)\n\t" \
    "v_mfma_f32_32x32x16_bf16 %[o0], v[232:235], v[192:195], %[o0]\n\t" \
    "v_exp_f32 v172, v172\n\t" \
    "v_cvt_pk_bf16_f32 v196, v168, v169\n\t" \
    "v_exp_f32 v173, v173\n\t" \
    "v_cvt_pk_bf16_f32 v197, v170, v171\n\t" \
    "v_exp_f32 v174, v174\n\t" \
    "v_exp_f32 v175, v175\n\t" \
    "v_cvt_pk_bf16_f32 v198, v172, v173\n\t" \
    "v_cvt_pk_bf16_f32 v199, v174, v175\n\t" \
    "v_mfma_f32_32x32x8_bf16 v[160:175], v[218:219], %[qr2], 0\n\t" \
    "v_exp_f32 v176, v176\n\t" \
    "v_exp_f32 v177, v177\n\t" \
    "v_exp_f32 v178, v178\n\t" \
    "v_exp_f32 v179, v179\n\t" \
    "v_mfma_f32_32x32x16_bf16 %[o0], v[236:239], v[196:199], %[o0]\n\t" \
    "v_exp_f32 v180, v180\n\t" \
    "v_cvt_pk_bf16_f32 v192, v176, v177\n\t" \
    "v_exp_f32 v181, v181\n\t" \
    "v_cvt_pk_bf16_f32 v193, v178, v179\n\t" \
    "v_exp_f32 v182, v182\n\t" \
    "v_exp_f32 v183, v183\n\t" \
    "v_mfma_f32_32x32x16_bf16 v[160:175], v[204:207], %[qm2], v[160:175]\n\t" \
    "v_exp_f32 v184, v184\n\t" \
    "v_cvt_pk_bf16_f32 v194, v180, v181\n\t" \
    "v_exp_f32 v185, v185\n\t" \
    "v_cvt_pk_bf16_f32 v195, v182, v183\n\t" \
    "v_exp_f32 v186, v186\n\t" \
    "v_exp_f32 v187, v187\n\t" \
    "v_mfma_f32_32x32x16_bf16 %[o1], v[232:235], v[192:195], %[o1]\n\t" \
    "v_exp_f32 v188, v188\n\t" \
    "v_cvt_pk_bf16_f32 v196, v184, v185\n\t" \
    "v_exp_f32 v189, v189\n\t" \
    "v_cvt_pk_bf16_f32 v197, v186, v187\n\t" \
    "v_exp_f32 v190, v190\n\t" \
    "v_exp_f32 v191, v191\n\t" \
    "v_cvt_pk_bf16_f32 v198, v188, v189\n\t" \
    "v_cvt_pk_bf16_f32 v199, v190, v191\n\t" \
    "v_mfma_f32_32x32x8_bf16 v[176:191], v[218:219], %[qr3], 0\n\t" \
    "v_exp_f32 v160, v160\n\t" \
    "v_exp_f32 v161, v161\n\t" \
    "v_exp_f32 v162, v162\n\t" \
    "v_exp_f32 v163, v163\n\t" \
    "v_mfma_f32_32x32x16_bf16 %[o1], v[236:239], v[196:199], %[o1]\n\t" \
    "v_exp_f32 v164, v164\n\t" \
    "v_cvt_pk_bf16_f32 v192, v160, v161\n\t" \
    "v_exp_f32 v165, v165\n\t" \
    "v_cvt_pk_bf16_f32 v193, v162, v163\n\t" \
    "v_exp_f32 v166, v166\n\t" \
    "v_exp_f32 v167, v167\n\t" \
    "v_mfma_f32_32x32x16_bf16 v[176:191], v[204:207], %[qm3], v[176:191]\n\t" \
    "v_exp_f32 v168, v168\n\t" \
    "v_cvt_pk_bf16_f32 v194, v164, v165\n\t" \
    "v_exp_f32 v169, v169\n\t" \
    "v_cvt_pk_bf16_f32 v195, v166, v167\n\t" \
    "v_exp_f32 v170, v170\n\t" \
    "v_exp_f32 v171, v171\n\t" \
    "v_mfma_f32_32x32x16_bf16 %[o2], v[232:235], v[192:195], %[o2]\n\t" \
    "v_exp_f32 v172, v172\n\t" \
    "v_cvt_pk_bf16_f32 v196, v168, v169\n\t" \
    "v_exp_f32 v173, v173\n\t" \
    "v_cvt_pk_bf16_f32 v197, v170, v171\n\t" \
    "v_exp_f32 v174, v174\n\t" \
    "v_exp_f32 v175, v175\n\t" \
    "v_cvt_pk_bf16_f32 v198, v172, v173\n\t" \
    "v_cvt_pk_bf16_f32 v199, v174, v175\n\t" \
    "s_waitcnt vmcnt(11)\n\t" \
    "v_mfma_f32_32x32x8_bf16 v[160:175], v[220:221], %[qr0], 0\n\t" \
    "v_exp_f32 v176, v176\n\t" \
    "v_exp_f32 v177, v177\n\t" \
    "v_exp_f32 v178, v178\n\t" \
    "v_exp_f32 v179, v179\n\t" \
    "v_mfma_f32_32x32x16_bf16 %[o2], v[236:239], v[196:199], %[o2]\n\t" \
    "v_exp_f32 v180, v180\n\t" \
    "v_cvt_pk_bf16_f32 v192, v176, v177\n\t" \
    "v_exp_f32 v181, v181\n\t" \
    "v_cvt_pk_bf16_f32 v193, v178, v179\n\t" \
    "v_exp_f32 v182, v182\n\t" \
    "v_exp_f32 v183, v183\n\t" \
    "v_mfma_f32_32x32x16_bf16 v[160:175], v[208:211], %[qm0], v[160:175]\n\t" \
    "global_load_dwordx4 v[204:207], %[o16], s[40:41] sc1\n\t" \
    "global_load_dwordx2 v[218:219], %[o8], s[40:41] offset:1024 sc1\n\t" \
    "s_add_u32 s40, s40, 1536\n\t" \
    "s_addc_u32 s41, s41, 0\n\t" \
    "v_exp_f32 v184, v184\n\t" \
    "v_cvt_pk_bf16_f32 v194, v180, v181\n\t" \
    "v_exp_f32 v185, v185\n\t" \
    "v_cvt_pk_bf16_f32 v195, v182, v183\n\t" \
    "v_exp_f32 v186, v186\n\t" \
    "v_exp_f32 v187, v187\n\t" \
    "v_mfma_f32_32x32x16_bf16 %[o3], v[232:235], v[192:195], %[o3]\n\t" \
    "v_exp_f32 v188, v188\n\t" \
    "v_cvt_pk_bf16_f32 v196, v184, v185\n\t" \
    "v_exp_f32 v189, v189\n\t" \
    "v_cvt_pk_bf16_f32 v197, v186, v187\n\t" \
    "v_exp_f32 v190, v190\n\t" \
    "v_exp_f32 v191, v191\n\t" \
    "v_cvt_pk_bf16_f32 v198, v188, v189\n\t" \
    "v_cvt_pk_bf16_f32 v199, v190, v191\n\t" \
    "v_mfma_f32_32x32x8_bf16 v[176:191], v[220:221], %[qr1], 0\n\t" \
    "v_exp_f32 v160, v160\n\t" \
    "v_exp_f32 v161, v161\n\t" \
    "v_exp_f32 v162, v162\n\t" \
    "v_exp_f32 v163, v163\n\t" \
    "v_mfma_f32_32x32x16_bf16 %[o3], v[236:239], v[196:199], %[o3]\n\t" \
    "v_exp_f32 v164, v164\n\t" \
    "v_cvt_pk_bf16_f32 v192, v160, v161\n\t" \
    "v_exp_f32 v165, v165\n\t" \
    "v_cvt_pk_bf16_f32 v193, v162, v163\n\t" \
    "v_exp_f32 v166, v166\n\t" \
    "v_exp_f32 v167, v167\n\t" \
    "v_mfma_f32_32x32x16_bf16 v[176:191], v[208:211], %[qm1], v[176:191]\n\t" \
    "global_load_dwordx4 v[232:235], %[o16], s[42:43] sc1\n\t" \
    "global_load_dwordx4 v[236:239], %[o16], s[42:43] offset:1024 sc1\n\t" \
    "s_add_u32 s42, s42, 2048\n\t" \
    "s_addc_u32 s43, s43, 0\n\t" \
    "v_exp_f32 v168, v168\n\t" \
    "v_cvt_pk_bf16_f32 v194, v164, v165\n\t" \
    "v_exp_f32 v169, v169\n\t" \
    "v_cvt_pk_bf16_f32 v195, v166, v167\n\t" \
    "v_exp_f32 v170, v170\n\t" \
    "v_exp_f32 v171, v171\n\t" \
    "s_waitcnt vmcnt(13)\n\t" \
    "v_mfma_f32_32x32x16_bf16 %[o0], v[240:243], v[192:195], %[o0]\n\t" \
    "v_exp_f32 v172, v172\n\t" \
    "v_cvt_pk_bf16_f32 v196, v168, v169\n\t" \
    "v_exp_f32 v173, v173\n\t" \
    "v_cvt_pk_bf16_f32 v197, v170, v171\n\t" \
    "v_exp_f32 v174, v174\n\t" \
    "v_exp_f32 v175, v175\n\t" \
    "v_cvt_pk_bf16_f32 v198, v172, v173\n\t" \
    "v_cvt_pk_bf16_f32 v199, v174, v175\n\t" \
    "v_mfma_f32_32x32x8_bf16 v[160:175], v[220:221], %[qr2], 0\n\t" \
    "v_exp_f32 v176, v176\n\t" \
    "v_exp_f32 v177, v177\n\t" \
    "v_exp_f32 v178, v178\n\t" \
    "v_exp_f32 v179, v179\n\t" \
    "v_mfma_f32_32x32x16_bf16 %[o0], v[244:247], v[196:199], %[o0]\n\t" \
    "v_exp_f32 v180, v180\n\t" \
    "v_cvt_pk_bf16_f32 v192, v176, v177\n\t" \
    "v_exp_f32 v181, v181\n\t" \
    "v_cvt_pk_bf16_f32 v193, v178, v179\n\t" \
    "v_exp_f32 v182, v182\n\t" \
    "v_exp_f32 v183, v183\n\t" \
    "v_mfma_f32_32x32x16_bf16 v[160:175], v[208:211], %[qm2], v[160:175]\n\t" \
    "v_exp_f32 v184, v184\n\t" \
    "v_cvt_pk_bf16_f32 v194, v180, v181\n\t" \
    "v_exp_f32 v185, v185\n\t" \
    "v_cvt_pk_bf16_f32 v195, v182, v183\n\t" \
    "v_exp_f32 v186, v186\n\t" \
    "v_exp_f32 v187, v187\n\t" \
    "v_mfma_f32_32x32x16_bf16 %[o1], v[240:243], v[192:195], %[o1]\n\t" \
    "v_exp_f32 v188, v188\n\t" \
    "v_cvt_pk_bf16_f32 v196, v184, v185\n\t" \
    "v_exp_f32 v189, v189\n\t" \
    "v_cvt_pk_bf16_f32 v197, v186, v187\n\t" \
    "v_exp_f32 v190, v190\n\t" \
    "v_exp_f32 v191, v191\n\t" \
    "v_cvt_pk_bf16_f32 v198, v188, v189\n\t" \
    "v_cvt_pk_bf16_f32 v199, v190, v191\n\t" \
    "v_mfma_f32_32x32x8_bf16 v[176:191], v[220:221], %[qr3], 0\n\t" \
    "v_exp_f32 v160, v160\n\t" \
    "v_exp_f32 v161, v161\n\t" \
    "v_exp_f32 v162, v162\n\t" \
    "v_exp_f32 v163, v163\n\t" \
    "v_mfma_f32_32x32x16_bf16 %[o1], v[244:247], v[196:199], %[o1]\n\t" \
    "v_exp_f32 v164, v164\n\t" \
    "v_cvt_pk_bf16_f32 v192, v160, v161\n\t" \
    "v_exp_f32 v165, v165\n\t" \
    "v_cvt_pk_bf16_f32 v193, v162, v163\n\t" \
    "v_exp_f32 v166, v166\n\t" \
    "v_exp_f32 v167, v167\n\t" \
    "v_mfma_f32_32x32x16_bf16 v[176:191], v[208:211], %[qm3], v[176:191]\n\t" \
    "v_exp_f32 v168, v168\n\t" \
    "v_cvt_pk_bf16_f32 v194, v164, v165\n\t" \
    "v_exp_f32 v169, v169\n\t" \
    "v_cvt_pk_bf16_f32 v195, v166, v167\n\t" \
    "v_exp_f32 v170, v170\n\t" \
    "v_exp_f32 v171, v171\n\t" \
    "v_mfma_f32_32x32x16_bf16 %[o2], v[240:243], v[192:195], %[o2]\n\t" \
    "v_exp_f32 v172, v172\n\t" \
    "v_cvt_pk_bf16_f32 v196, v168, v169\n\t" \
    "v_exp_f32 v173, v173\n\t" \
    "v_cvt_pk_bf16_f32 v197, v170, v171\n\t" \
    "v_exp_f32 v174, v174\n\t" \
    "v_exp_f32 v175, v175\n\t" \
    "v_cvt_pk_bf16_f32 v198, v172, v173\n\t" \
    "v_cvt_pk_bf16_f32 v199, v174, v175\n\t" \
    "s_waitcnt vmcnt(10)\n\t" \
    "v_mfma_f32_32x32x8_bf16 v[160:175], v[222:223], %[qr0], 0\n\t" \
    "v_exp_f32 v176, v176\n\t" \
    "v_exp_f32 v177, v177\n\t" \
    "v_exp_f32 v178, v178\n\t" \
    "v_exp_f32 v179, v179\n\t" \
    "v_mfma_f32_32x32x16_bf16 %[o2], v[244:247], v[196:199], %[o2]\n\t" \
    "v_exp_f32 v180, v180\n\t" \
    "v_cvt_pk_bf16_f32 v192, v176, v177\n\t" \
    "v_exp_f32 v181, v181\n\t" \
    "v_cvt_pk_bf16_f32 v193, v178, v179\n\t" \
    "v_exp_f32 v182, v182\n\t" \
    "v_exp_f32 v183, v183\n\t" \
    "v_mfma_f32_32x32x16_bf16 v[160:175], v[212:215], %[qm0], v[160:175]\n\t" \
    "global_load_dwordx4 v[208:211], %[o16], s[40:41] sc1\n\t" \
    "global_load_dwordx2 v[220:221], %[o8], s[40:41] offset:1024 sc1\n\t" \
    "s_add_u32 s40, s40, 1536\n\t" \
    "s_addc_u32 s41, s41, 0\n\t" \
    "v_exp_f32 v184, v184\n\t" \
    "v_cvt_pk_bf16_f32 v194, v180, v181\n\t" \
    "v_exp_f32 v185, v185\n\t" \
    "v_cvt_pk_bf16_f32 v195, v182, v183\n\t" \
    "v_exp_f32 v186, v186\n\t" \
    "v_exp_f32 v187, v187\n\t" \
    "v_mfma_f32_32x32x16_bf16 %[o3], v[240:243], v[192:195], %[o3]\n\t" \
    "v_exp_f32 v188, v188\n\t" \
    "v_cvt_pk_bf16_f32 v196, v184, v185\n\t" \
    "v_exp_f32 v189, v189\n\t" \
    "v_cvt_pk_bf16_f32 v197, v186, v187\n\t" \
    "v_exp_f32 v190, v190\n\t" \
    "v_exp_f32 v191, v191\n\t" \
    "v_cvt_pk_bf16_f32 v198, v188, v189\n\t" \
    "v_cvt_pk_bf16_f32 v199, v190, v191\n\t" \
    "v_mfma_f32_32x32x8_bf16 v[176:191], v[222:223], %[qr1], 0\n\t" \
    "v_exp_f32 v160, v160\n\t" \
    "v_exp_f32 v161, v161\n\t" \
    "v_exp_f32 v162, v162\n\t" \
    "v_exp_f32 v163, v163\n\t" \
    "v_mfma_f32_32x32x16_bf16 %[o3], v[244:247], v[196:199], %[o3]\n\t" \
    "v_exp_f32 v164, v164\n\t" \
    "v_cvt_pk_bf16_f32 v192, v160, v161\n\t" \
    "v_exp_f32 v165, v165\n\t" \
    "v_cvt_pk_bf16_f32 v193, v162, v163\n\t" \
    "v_exp_f32 v166, v166\n\t" \
    "v_exp_f32 v167, v167\n\t" \
    "v_mfma_f32_32x32x16_bf16 v[176:191], v[212:215], %[qm1], v[176:191]\n\t" \
    "global_load_dwordx4 v[240:243], %[o16], s[42:43] sc1\n\t" \
    "global_load_dwordx4 v[244:247], %[o16], s[42:43] offset:1024 sc1\n\t" \
    "s_add_u32 s42, s42, 2048\n\t" \
    "s_addc_u32 s43, s43, 0\n\t" \
    "v_exp_f32 v168, v168\n\t" \
    "v_cvt_pk_bf16_f32 v194, v164, v165\n\t" \
    "v_exp_f32 v169, v169\n\t" \
    "v_cvt_pk_bf16_f32 v195, v166, v167\n\t" \
    "v_exp_f32 v170, v170\n\t" \
    "v_exp_f32 v171, v171\n\t" \
    "s_waitcnt vmcnt(12)\n\t" \
    "v_mfma_f32_32x32x16_bf16 %[o0], v[248:251], v[192:195], %[o0]\n\t" \
    "v_exp_f32 v172, v172\n\t" \
    "v_cvt_pk_bf16_f32 v196, v168, v169\n\t" \
    "v_exp_f32 v173, v173\n\t" \
    "v_cvt_pk_bf16_f32 v197, v170, v171\n\t" \
    "v_exp_f32 v174, v174\n\t" \
    "v_exp_f32 v175, v175\n\t" \
    "v_cvt_pk_bf16_f32 v198, v172, v173\n\t" \
    "v_cvt_pk_bf16_f32 v199, v174, v175\n\t" \
    "v_mfma_f32_32x32x8_bf16 v[160:175], v[222:223], %[qr2], 0\n\t" \
    "v_exp_f32 v176, v176\n\t" \
    "v_exp_f32 v177, v177\n\t" \
    "v_exp_f32 v178, v178\n\t" \
    "v_exp_f32 v179, v179\n\t" \
    "v_mfma_f32_32x32x16_bf16 %[o0], v[252:255], v[196:199], %[o0]\n\t" \
    "v_exp_f32 v180, v180\n\t" \
    "v_cvt_pk_bf16_f32 v192, v176, v177\n\t" \
    "v_exp_f32 v181, v181\n\t" \
    "v_cvt_pk_bf16_f32 v193, v178, v179\n\t" \
    "v_exp_f32 v182, v182\n\t" \
    "v_exp_f32 v183, v183\n\t" \
    "v_mfma_f32_32x32x16_bf16 v[160:175], v[212:215], %[qm2], v[160:175]\n\t" \
    "global_load_dwordx2 v[144:145], %[o8], %[fl] offset:0 sc1\n\t" \
    "global_load_dwordx2 v[146:147], %[o8], %[fl] offset:512 sc1\n\t" \
    "global_load_dwordx2 v[148:149], %[o8], %[fl] offset:1024 sc1\n\t" \
    "global_load_dwordx2 v[150:151], %[o8], %[fl] offset:1536 sc1\n\t" \
    "v_exp_f32 v184, v184\n\t" \
    "v_cvt_pk_bf16_f32 v194, v180, v181\n\t" \
    "v_exp_f32 v185, v185\n\t" \
    "v_cvt_pk_bf16_f32 v195, v182, v183\n\t" \
    "v_exp_f32 v186, v186\n\t" \
    "v_exp_f32 v187, v187\n\t" \
    "v_mfma_f32_32x32x16_bf16 %[o1], v[248:251], v[192:195], %[o1]\n\t" \
    "v_exp_f32 v188, v188\n\t" \
    "v_cvt_pk_bf16_f32 v196, v184, v185\n\t" \
    "v_exp_f32 v189, v189\n\t" \
    "v_cvt_pk_bf16_f32 v197, v186, v187\n\t" \
    "v_exp_f32 v190, v190\n\t" \
    "v_exp_f32 v191, v191\n\t" \
    "v_cvt_pk_bf16_f32 v198, v188, v189\n\t" \
    "v_cvt_pk_bf16_f32 v199, v190, v191\n\t" \
    "v_mfma_f32_32x32x8_bf16 v[176:191], v[222:223], %[qr3], 0\n\t" \
    "v_exp_f32 v160, v160\n\t" \
    "v_exp_f32 v161, v161\n\t" \
    "v_exp_f32 v162, v162\n\t" \
    "v_exp_f32 v163, v163\n\t" \
    "v_mfma_f32_32x32x16_bf16 %[o1], v[252:255], v[196:199], %[o1]\n\t" \
    "v_exp_f32 v164, v164\n\t" \
    "v_cvt_pk_bf16_f32 v192, v160, v161\n\t" \
    "v_exp_f32 v165, v165\n\t" \
    "v_cvt_pk_bf16_f32 v193, v162, v163\n\t" \
    "v_exp_f32 v166, v166\n\t" \
    "v_exp_f32 v167, v167\n\t" \
    "v_mfma_f32_32x32x16_bf16 v[176:191], v[212:215], %[qm3], v[176:191]\n\t" \
    "v_exp_f32 v168, v168\n\t" \
    "v_cvt_pk_bf16_f32 v194, v164, v165\n\t" \
    "v_exp_f32 v169, v169\n\t" \
    "v_cvt_pk_bf16_f32 v195, v166, v167\n\t" \
    "v_exp_f32 v170, v170\n\t" \
    "v_exp_f32 v171, v171\n\t" \
    "v_mfma_f32_32x32x16_bf16 %[o2], v[248:251], v[192:195], %[o2]\n\t" \
    "v_exp_f32 v172, v172\n\t" \
    "v_cvt_pk_bf16_f32 v196, v168, v169\n\t" \
    "v_exp_f32 v173, v173\n\t" \
    "v_cvt_pk_bf16_f32 v197, v170, v171\n\t" \
    "v_exp_f32 v174, v174\n\t" \
    "v_exp_f32 v175, v175\n\t" \
    "v_cvt_pk_bf16_f32 v198, v172, v173\n\t" \
    "v_cvt_pk_bf16_f32 v199, v174, v175\n\t" \
    "s_waitcnt vmcnt(14)\n\t" \
    "v_mfma_f32_32x32x8_bf16 v[160:175], v[216:217], %[qr0], 0\n\t" \
    "v_exp_f32 v176, v176\n\t" \
    "v_exp_f32 v177, v177\n\t" \
    "v_exp_f32 v178, v178\n\t" \
    "v_exp_f32 v179, v179\n\t" \
    "v_mfma_f32_32x32x16_bf16 %[o2], v[252:255], v[196:199], %[o2]\n\t" \
    "v_exp_f32 v180, v180\n\t" \
    "v_cvt_pk_bf16_f32 v192, v176, v177\n\t" \
    "v_exp_f32 v181, v181\n\t" \
    "v_cvt_pk_bf16_f32 v193, v178, v179\n\t" \
    "v_exp_f32 v182, v182\n\t" \
    "v_exp_f32 v183, v183\n\t" \
    "v_mfma_f32_32x32x16_bf16 v[160:175], v[200:203], %[qm0], v[160:175]\n\t" \
    "global_load_dwordx4 v[212:215], %[o16], s[40:41] sc1\n\t" \
    "global_load_dwordx2 v[222:223], %[o8], s[40:41] offset:1024 sc1\n\t" \
    "s_add_u32 s40, s40, 1536\n\t" \
    "s_addc_u32 s41, s41, 0\n\t" \
    "v_exp_f32 v184, v184\n\t" \
    "v_cvt_pk_bf16_f32 v194, v180, v181\n\t" \
    "v_exp_f32 v185, v185\n\t" \
    "v_cvt_pk_bf16_f32 v195, v182, v183\n\t" \
    "v_exp_f32 v186, v186\n\t" \
    "v_exp_f32 v187, v187\n\t" \
    "v_mfma_f32_32x32x16_bf16 %[o3], v[248:251], v[192:195], %[o3]\n\t" \
    "v_exp_f32 v188, v188\n\t" \
    "v_cvt_pk_bf16_f32 v196, v184, v185\n\t" \
    "v_exp_f32 v189, v189\n\t" \
    "v_cvt_pk_bf16_f32 v197, v186, v187\n\t" \
    "v_exp_f32 v190, v190\n\t" \
    "v_exp_f32 v191, v191\n\t" \
    "v_cvt_pk_bf16_f32 v198, v188, v189\n\t" \
    "v_cvt_pk_bf16_f32 v199, v190, v191\n\t" \
    "s_nop 1\n\t" \
    "v_mfma_f32_32x32x16_bf16 %[o3], v[252:255], v[196:199], %[o3]\n\t" \
    "s_waitcnt vmcnt(0)\n\t" \
    "s_nop 15\n\t" \
    "s_mov_b32 m0, s54\n\t" \
    "s_mov_b64 %[kb], s[40:41]\n\t" \
    "s_mov_b64 %[vb], s[42:43]\n\t"
#define ATTN_WAIT_RT "s_waitcnt vmcnt(17)"
#define ATTN_WAIT_ST "s_waitcnt vmcnt(23)"
#define ATTN_SEG_CLOBBERS "v152", "v153", "v154", "v155", "v159", "s40", "s41", "s42", "s43", "s44", "s45", "s46", "s47", "s48", "s49", "s50", "s51", "s52", "s53", "s54", "s55", "s56", "s57", "s58", "s59", "scc", "memory"
#define QA_ASM \
    "v_mov_b32 v16, 0\n\t" \
    "v_mov_b32 v17, 0\n\t" \
    "v_mov_b32 v18, 0\n\t" \
    "v_mov_b32 v19, 0\n\t" \
    "v_mov_b32 v20, 0\n\t" \
    "v_mov_b32 v21, 0\n\t" \
    "v_mov_b32 v22, 0\n\t" \
    "v_mov_b32 v23, 0\n\t" \
    "v_mov_b32 v24, 0\n\t" \
    "v_mov_b32 v25, 0\n\t" \
    "v_mov_b32 v26, 0\n\t" \
    "v_mov_b32 v27, 0\n\t" \
    "v_mov_b32 v28, 0\n\t" \
    "v_mov_b32 v29, 0\n\t" \
    "v_mov_b32 v30, 0\n\t" \
    "v_mov_b32 v31, 0\n\t" \
    "ds_read_b64_tr_b16 v[64:65], %[ab] offset:0\n\t" \
    "ds_read_b64_tr_b16 v[66:67], %[ab] offset:768\n\t" \
    "ds_read_b128 v[40:43], %[xb] offset:0\n\t" \
    "ds_read_b64_tr_b16 v[68:69], %[ab] offset:3072\n\t" \
    "ds_read_b64_tr_b16 v[70:71], %[ab] offset:3840\n\t" \
    "ds_read_b128 v[44:47], %[xb] offset:32\n\t" \
    "ds_read_b64_tr_b16 v[72:73], %[ab] offset:6144\n\t" \
    "ds_read_b64_tr_b16 v[74:75], %[ab] offset:6912\n\t" \
    "ds_read_b128 v[48:51], %[xb] offset:64\n\t" \
    "s_waitcnt lgkmcnt(6)\n\t" \
    "v_mfma_f32_32x32x16_bf16 v[16:31], v[64:67], v[40:43], v[16:31]\n\t" \
    "ds_read_b64_tr_b16 v[76:77], %[ab] offset:9216\n\t" \
    "ds_read_b64_tr_b16 v[78:79], %[ab] offset:9984\n\t" \
    "ds_read_b128 v[52:55], %[xb] offset:96\n\t" \
    "s_waitcnt lgkmcnt(6)\n\t" \
    "v_mfma_f32_32x32x16_bf16 v[16:31], v[68:71], v[44:47], v[16:31]\n\t" \
    "ds_read_b64_tr_b16 v[80:81], %[ab] offset:12288\n\t" \
    "ds_read_b64_tr_b16 v[82:83], %[ab] offset:13056\n\t" \
    "ds_read_b128 v[40:43], %[xb] offset:128\n\t" \
    "s_waitcnt lgkmcnt(6)\n\t" \
    "v_mfma_f32_32x32x16_bf16 v[16:31], v[72:75], v[48:51], v[16:31]\n\t" \
    "ds_read_b64_tr_b16 v[84:85], %[ab] offset:15360\n\t" \
    "ds_read_b64_tr_b16 v[86:87], %[ab] offset:16128\n\t" \
    "ds_read_b128 v[44:47], %[xb] offset:160\n\t" \
    "s_waitcnt lgkmcnt(6)\n\t" \
    "v_mfma_f32_32x32x16_bf16 v[16:31], v[76:79], v[52:55], v[16:31]\n\t" \
    "ds_read_b64_tr_b16 v[88:89], %[ab] offset:18432\n\t" \
    "ds_read_b64_tr_b16 v[90:91], %[ab] offset:19200\n\t" \
    "ds_read_b128 v[48:51], %[xb] offset:192\n\t" \
    "s_waitcnt lgkmcnt(6)\n\t" \
    "v_mfma_f32_32x32x16_bf16 v[16:31], v[80:83], v[40:43], v[16:31]\n\t" \
    "ds_read_b64_tr_b16 v[92:93], %[ab] offset:21504\n\t" \
    "ds_read_b64_tr_b16 v[94:95], %[ab] offset:22272\n\t" \
    "ds_read_b128 v[52:55], %[xb] offset:224\n\t" \
    "s_waitcnt lgkmcnt(6)\n\t" \
    "v_mfma_f32_32x32x16_bf16 v[16:31], v[84:87], v[44:47], v[16:31]\n\t" \
    "ds_read_b64_tr_b16 v[96:97], %[ab] offset:24576\n\t" \
    "ds_read_b64_tr_b16 v[98:99], %[ab] offset:25344\n\t" \
    "ds_read_b128 v[40:43], %[xb] offset:256\n\t" \
    "s_waitcnt lgkmcnt(6)\n\t" \
    "v_mfma_f32_32x32x16_bf16 v[16:31], v[88:91], v[48:51], v[16:31]\n\t" \
    "ds_read_b64_tr_b16 v[100:101], %[ab] offset:27648\n\t" \
    "ds_read_b64_tr_b16 v[102:103], %[ab] offset:28416\n\t" \
    "ds_read_b128 v[44:47], %[xb] offset:288\n\t" \
    "s_waitcnt lgkmcnt(6)\n\t" \
    "v_mfma_f32_32x32x16_bf16 v[16:31], v[92:95], v[52:55], v[16:31]\n\t" \
    "ds_read_b64_tr_b16 v[104:105], %[ab] offset:30720\n\t" \
    "ds_read_b64_tr_b16 v[106:107], %[ab] offset:31488\n\t" \
    "ds_read_b128 v[48:51], %[xb] offset:320\n\t" \
    "s_waitcnt lgkmcnt(6)\n\t" \
    "v_mfma_f32_32x32x16_bf16 v[16:31], v[96:99], v[40:43], v[16:31]\n\t" \
    "ds_read_b64_tr_b16 v[108:109], %[ab] offset:33792\n\t" \
    "ds_read_b64_tr_b16 v[110:111], %[ab] offset:34560\n\t" \
    "ds_read_b128 v[52:55], %[xb] offset:352\n\t" \
    "s_waitcnt lgkmcnt(6)\n\t" \
    "v_mfma_f32_32x32x16_bf16 v[16:31], v[100:103], v[44:47], v[16:31]\n\t" \
    "ds_read_b64_tr_b16 v[112:113], %[ab] offset:36864\n\t" \
    "ds_read_b64_tr_b16 v[114:115], %[ab] offset:37632\n\t" \
    "ds_read_b128 v[40:43], %[xb] offset:384\n\t" \
    "s_waitcnt lgkmcnt(6)\n\t" \
    "v_mfma_f32_32x32x16_bf16 v[16:31], v[104:107], v[48:51], v[16:31]\n\t" \
    "ds_read_b64_tr_b16 v[116:117], %[ab] offset:39936\n\t" \
    "ds_read_b64_tr_b16 v[118:119], %[ab] offset:40704\n\t" \
    "ds_read_b128 v[44:47], %[xb] offset:416\n\t" \
    "s_waitcnt lgkmcnt(6)\n\t" \
    "v_mfma_f32_32x32x16_bf16 v[16:31], v[108:111], v[52:55], v[16:31]\n\t" \
    "ds_read_b64_tr_b16 v[120:121], %[ab] offset:43008\n\t" \
    "ds_read_b64_tr_b16 v[122:123], %[ab] offset:43776\n\t" \
    "ds_read_b128 v[48:51], %[xb] offset:448\n\t" \
    "s_waitcnt lgkmcnt(6)\n\t" \
    "v_mfma_f32_32x32x16_bf16 v[16:31], v[112:115], v[40:43], v[16:31]\n\t" \
    "ds_read_b64_tr_b16 v[124:125], %[ab] offset:46080\n\t" \
    "ds_read_b64_tr_b16 v[126:127], %[ab] offset:46848\n\t" \
    "ds_read_b128 v[52:55], %[xb] offset:480\n\t" \
    "s_waitcnt lgkmcnt(6)\n\t" \
    "v_mfma_f32_32x32x16_bf16 v[16:31], v[116:119], v[44:47], v[16:31]\n\t" \
    "s_waitcnt lgkmcnt(3)\n\t" \
    "v_mfma_f32_32x32x16_bf16 v[16:31], v[120:123], v[48:51], v[16:31]\n\t" \
    "s_waitcnt lgkmcnt(0)\n\t" \
    "v_mfma_f32_32x32x16_bf16 v[16:31], v[124:127], v[52:55], v[16:31]\n\t" \
    "s_waitcnt lgkmcnt(0)\n\t" \
    "s_nop 15\n\t"
#define QA_CLOBBERS "v40", "v41", "v42", "v43", "v44", "v45", "v46", "v47", "v48", "v49", "v50", "v51", "v52", "v53", "v54", "v55", "memory"
#define QB_ASM \
    "v_mov_b32 v32, 0\n\t" \
    "v_mov_b32 v33, 0\n\t" \
    "v_mov_b32 v34, 0\n\t" \
    "v_mov_b32 v35, 0\n\t" \
    "ds_read_b64_tr_b16 v[64:65], %[ab] offset:0\n\t" \
    "ds_read_b64_tr_b16 v[66:67], %[ab] offset:768\n\t" \
    "ds_read_b128 v[40:43], %[xb] offset:512\n\t" \
    "ds_read_b64_tr_b16 v[68:69], %[ab] offset:3072\n\t" \
    "ds_read_b64_tr_b16 v[70:71], %[ab] offset:3840\n\t" \
    "ds_read_b128 v[44:47], %[xb] offset:544\n\t" \
    "ds_read_b64_tr_b16 v[72:73], %[ab] offset:6144\n\t" \
    "ds_read_b64_tr_b16 v[74:75], %[ab] offset:6912\n\t" \
    "ds_read_b128 v[48:51], %[xb] offset:576\n\t" \
    "s_waitcnt lgkmcnt(6)\n\t" \
    "v_mfma_f32_32x32x16_bf16 v[16:31], v[64:67], v[40:43], v[16:31]\n\t" \
    "v_mfma_f32_16x16x32_bf16 v[32:35], v[64:67], %[sel], v[32:35]\n\t" \
    "ds_read_b64_tr_b16 v[76:77], %[ab] offset:9216\n\t" \
    "ds_read_b64_tr_b16 v[78:79], %[ab] offset:9984\n\t" \
    "ds_read_b128 v[52:55], %[xb] offset:608\n\t" \
    "s_waitcnt lgkmcnt(6)\n\t" \
    "v_mfma_f32_32x32x16_bf16 v[16:31], v[68:71], v[44:47], v[16:31]\n\t" \
    "v_mfma_f32_16x16x32_bf16 v[32:35], v[68:71], %[sel], v[32:35]\n\t" \
    "ds_read_b64_tr_b16 v[64:65], %[ab] offset:12288\n\t" \
    "ds_read_b64_tr_b16 v[66:67], %[ab] offset:13056\n\t" \
    "ds_read_b128 v[40:43], %[xb] offset:640\n\t" \
    "s_waitcnt lgkmcnt(6)\n\t" \
    "v_mfma_f32_32x32x16_bf16 v[16:31], v[72:75], v[48:51], v[16:31]\n\t" \
    "v_mfma_f32_16x16x32_bf16 v[32:35], v[72:75], %[sel], v[32:35]\n\t" \
    "ds_read_b64_tr_b16 v[68:69], %[ab] offset:15360\n\t" \
    "ds_read_b64_tr_b16 v[70:71], %[ab] offset:16128\n\t" \
    "ds_read_b128 v[44:47], %[xb] offset:672\n\t" \
    "s_waitcnt lgkmcnt(6)\n\t" \
    "v_mfma_f32_32x32x16_bf16 v[16:31], v[76:79], v[52:55], v[16:31]\n\t" \
    "v_mfma_f32_16x16x32_bf16 v[32:35], v[76:79], %[sel], v[32:35]\n\t" \
    "ds_read_b64_tr_b16 v[72:73], %[ab] offset:18432\n\t" \
    "ds_read_b64_tr_b16 v[74:75], %[ab] offset:19200\n\t" \
    "ds_read_b128 v[48:51], %[xb] offset:704\n\t" \
    "s_waitcnt lgkmcnt(6)\n\t" \
    "v_mfma_f32_32x32x16_bf16 v[16:31], v[64:67], v[40:43], v[16:31]\n\t" \
    "v_mfma_f32_16x16x32_bf16 v[32:35], v[64:67], %[sel], v[32:35]\n\t" \
    "ds_read_b64_tr_b16 v[76:77], %[ab] offset:21504\n\t" \
    "ds_read_b64_tr_b16 v[78:79], %[ab] offset:22272\n\t" \
    "ds_read_b128 v[52:55], %[xb] offset:736\n\t" \
    "s_waitcnt lgkmcnt(6)\n\t" \
    "v_mfma_f32_32x32x16_bf16 v[16:31], v[68:71], v[44:47], v[16:31]\n\t" \
    "v_mfma_f32_16x16x32_bf16 v[32:35], v[68:71], %[sel], v[32:35]\n\t" \
    "ds_read_b64_tr_b16 v[64:65], %[ab] offset:24576\n\t" \
    "ds_read_b64_tr_b16 v[66:67], %[ab] offset:25344\n\t" \
    "ds_read_b128 v[40:43], %[xb] offset:768\n\t" \
    "s_waitcnt lgkmcnt(6)\n\t" \
    "v_mfma_f32_32x32x16_bf16 v[16:31], v[72:75], v[48:51], v[16:31]\n\t" \
    "v_mfma_f32_16x16x32_bf16 v[32:35], v[72:75], %[sel], v[32:35]\n\t" \
    "ds_read_b64_tr_b16 v[68:69], %[ab] offset:27648\n\t" \
    "ds_read_b64_tr_b16 v[70:71], %[ab] offset:28416\n\t" \
    "ds_read_b128 v[44:47], %[xb] offset:800\n\t" \
    "s_waitcnt lgkmcnt(6)\n\t" \
    "v_mfma_f32_32x32x16_bf16 v[16:31], v[76:79], v[52:55], v[16:31]\n\t" \
    "v_mfma_f32_16x16x32_bf16 v[32:35], v[76:79], %[sel], v[32:35]\n\t" \
    "ds_read_b64_tr_b16 v[72:73], %[ab] offset:30720\n\t" \
    "ds_read_b64_tr_b16 v[74:75], %[ab] offset:31488\n\t" \
    "ds_read_b128 v[48:51], %[xb] offset:832\n\t" \
    "s_waitcnt lgkmcnt(6)\n\t" \
    "v_mfma_f32_32x32x16_bf16 v[16:31], v[64:67], v[40:43], v[16:31]\n\t" \
    "v_mfma_f32_16x16x32_bf16 v[32:35], v[64:67], %[sel], v[32:35]\n\t" \
    "ds_read_b64_tr_b16 v[76:77], %[ab] offset:33792\n\t" \
    "ds_read_b64_tr_b16 v[78:79], %[ab] offset:34560\n\t" \
    "ds_read_b128 v[52:55], %[xb] offset:864\n\t" \
    "s_waitcnt lgkmcnt(6)\n\t" \
    "v_mfma_f32_32x32x16_bf16 v[16:31], v[68:71], v[44:47], v[16:31]\n\t" \
    "v_mfma_f32_16x16x32_bf16 v[32:35], v[68:71], %[sel], v[32:35]\n\t" \
    "ds_read_b64_tr_b16 v[64:65], %[ab] offset:36864\n\t" \
    "ds_read_b64_tr_b16 v[66:67], %[ab] offset:37632\n\t" \
    "ds_read_b128 v[40:43], %[xb] offset:896\n\t" \
    "s_waitcnt lgkmcnt(6)\n\t" \
    "v_mfma_f32_32x32x16_bf16 v[16:31], v[72:75], v[48:51], v[16:31]\n\t" \
    "v_mfma_f32_16x16x32_bf16 v[32:35], v[72:75], %[sel], v[32:35]\n\t" \
    "ds_read_b64_tr_b16 v[68:69], %[ab] offset:39936\n\t" \
    "ds_read_b64_tr_b16 v[70:71], %[ab] offset:40704\n\t" \
    "ds_read_b128 v[44:47], %[xb] offset:928\n\t" \
    "s_barrier\n\t" \
    "s_waitcnt lgkmcnt(6)\n\t" \
    "v_mfma_f32_32x32x16_bf16 v[16:31], v[76:79], v[52:55], v[16:31]\n\t" \
    "v_mfma_f32_16x16x32_bf16 v[32:35], v[76:79], %[sel], v[32:35]\n\t" \
    "ds_read_b64_tr_b16 v[72:73], %[ab] offset:43008\n\t" \
    "ds_read_b64_tr_b16 v[74:75], %[ab] offset:43776\n\t" \
    "ds_read_b128 v[48:51], %[xb] offset:960\n\t" \
    "s_waitcnt lgkmcnt(6)\n\t" \
    "v_mfma_f32_32x32x16_bf16 v[16:31], v[64:67], v[40:43], v[16:31]\n\t" \
    "v_mfma_f32_16x16x32_bf16 v[32:35], v[64:67], %[sel], v[32:35]\n\t" \
    "ds_read_b64_tr_b16 v[76:77], %[ab] offset:46080\n\t" \
    "ds_read_b64_tr_b16 v[78:79], %[ab] offset:46848\n\t" \
    "ds_read_b128 v[52:55], %[xb] offset:992\n\t" \
    "s_waitcnt lgkmcnt(6)\n\t" \
    "v_mfma_f32_32x32x16_bf16 v[16:31], v[68:71], v[44:47], v[16:31]\n\t" \
    "v_mfma_f32_16x16x32_bf16 v[32:35], v[68:71], %[sel], v[32:35]\n\t" \
    "ds_read_b64_tr_b16 v[64:65], %[a4] offset:0\n\t" \
    "ds_read_b64_tr_b16 v[66:67], %[a4] offset:768\n\t" \
    "ds_read_b128 v[40:43], %[xb] offset:1024\n\t" \
    "s_waitcnt lgkmcnt(6)\n\t" \
    "v_mfma_f32_32x32x16_bf16 v[16:31], v[72:75], v[48:51], v[16:31]\n\t" \
    "v_mfma_f32_16x16x32_bf16 v[32:35], v[72:75], %[sel], v[32:35]\n\t" \
    "ds_read_b64_tr_b16 v[68:69], %[a4] offset:3072\n\t" \
    "ds_read_b64_tr_b16 v[70:71], %[a4] offset:3840\n\t" \
    "ds_read_b128 v[44:47], %[xb] offset:1056\n\t" \
    "s_waitcnt lgkmcnt(6)\n\t" \
    "v_mfma_f32_32x32x16_bf16 v[16:31], v[76:79], v[52:55], v[16:31]\n\t" \
    "v_mfma_f32_16x16x32_bf16 v[32:35], v[76:79], %[sel], v[32:35]\n\t" \
    "ds_read_b64_tr_b16 v[72:73], %[a4] offset:6144\n\t" \
    "ds_read_b64_tr_b16 v[74:75], %[a4] offset:6912\n\t" \
    "ds_read_b128 v[48:51], %[xb] offset:1088\n\t" \
    "s_waitcnt lgkmcnt(6)\n\t" \
    "v_mfma_f32_32x32x16_bf16 v[16:31], v[64:67], v[40:43], v[16:31]\n\t" \
    "v_mfma_f32_16x16x32_bf16 v[32:35], v[64:67], %[sel], v[32:35]\n\t" \
    "ds_read_b64_tr_b16 v[76:77], %[a4] offset:9216\n\t" \
    "ds_read_b64_tr_b16 v[78:79], %[a4] offset:9984\n\t" \
    "ds_read_b128 v[52:55], %[xb] offset:1120\n\t" \
    "s_waitcnt lgkmcnt(6)\n\t" \
    "v_mfma_f32_32x32x16_bf16 v[16:31], v[68:71], v[44:47], v[16:31]\n\t" \
    "v_mfma_f32_16x16x32_bf16 v[32:35], v[68:71], %[sel], v[32:35]\n\t" \
    "ds_read_b64_tr_b16 v[64:65], %[a4] offset:12288\n\t" \
    "ds_read_b64_tr_b16 v[66:67], %[a4] offset:13056\n\t" \
    "ds_read_b128 v[40:43], %[xb] offset:1152\n\t" \
    "s_waitcnt lgkmcnt(6)\n\t" \
    "v_mfma_f32_32x32x16_bf16 v[16:31], v[72:75], v[48:51], v[16:31]\n\t" \
    "v_mfma_f32_16x16x32_bf16 v[32:35], v[72:75], %[sel], v[32:35]\n\t" \
    "ds_read_b64_tr_b16 v[68:69], %[a4] offset:15360\n\t" \
    "ds_read_b64_tr_b16 v[70:71], %[a4] offset:16128\n\t" \
    "ds_read_b128 v[44:47], %[xb] offset:1184\n\t" \
    "s_waitcnt lgkmcnt(6)\n\t" \
    "v_mfma_f32_32x32x16_bf16 v[16:31], v[76:79], v[52:55], v[16:31]\n\t" \
    "v_mfma_f32_16x16x32_bf16 v[32:35], v[76:79], %[sel], v[32:35]\n\t" \
    "ds_read_b64_tr_b16 v[72:73], %[a4] offset:18432\n\t" \
    "ds_read_b64_tr_b16 v[74:75], %[a4] offset:19200\n\t" \
    "ds_read_b128 v[48:51], %[xb] offset:1216\n\t" \
    "s_waitcnt lgkmcnt(6)\n\t" \
    "v_mfma_f32_32x32x16_bf16 v[16:31], v[64:67], v[40:43], v[16:31]\n\t" \
    "v_mfma_f32_16x16x32_bf16 v[32:35], v[64:67], %[sel], v[32:35]\n\t" \
    "ds_read_b64_tr_b16 v[76:77], %[a4] offset:21504\n\t" \
    "ds_read_b64_tr_b16 v[78:79], %[a4] offset:22272\n\t" \
    "ds_read_b128 v[52:55], %[xb] offset:1248\n\t" \
    "s_waitcnt lgkmcnt(6)\n\t" \
    "v_mfma_f32_32x32x16_bf16 v[16:31], v[68:71], v[44:47], v[16:31]\n\t" \
    "v_mfma_f32_16x16x32_bf16 v[32:35], v[68:71], %[sel], v[32:35]\n\t" \
    "s_waitcnt lgkmcnt(3)\n\t" \
    "v_mfma_f32_32x32x16_bf16 v[16:31], v[72:75], v[48:51], v[16:31]\n\t" \
    "v_mfma_f32_16x16x32_bf16 v[32:35], v[72:75], %[sel], v[32:35]\n\t" \
    "s_waitcnt lgkmcnt(0)\n\t" \
    "v_mfma_f32_32x32x16_bf16 v[16:31], v[76:79], v[52:55], v[16:31]\n\t" \
    "v_mfma_f32_16x16x32_bf16 v[32:35], v[76:79], %[sel], v[32:35]\n\t" \
    "s_waitcnt lgkmcnt(0)\n\t" \
    "s_nop 15\n\t"
#define QB_CLOBBERS "v40", "v41", "v42", "v43", "v44", "v45", "v46", "v47", "v48", "v49", "v50", "v51", "v52", "v53", "v54", "v55", "v64", "v65", "v66", "v67", "v68", "v69", "v70", "v71", "v72", "v73", "v74", "v75", "v76", "v77", "v78", "v79", "memory"
#define QP_ASM \
    "ds_read_b64_tr_b16 v[64:65], %[ab] offset:0\n\t" \
    "ds_read_b64_tr_b16 v[66:67], %[ab] offset:768\n\t" \
    "ds_read_b128 v[40:43], %[xb] offset:512\n\t" \
    "ds_read_b64_tr_b16 v[68:69], %[ab] offset:3072\n\t" \
    "ds_read_b64_tr_b16 v[70:71], %[ab] offset:3840\n\t" \
    "ds_read_b128 v[44:47], %[xb] offset:544\n\t" \
    "ds_read_b64_tr_b16 v[72:73], %[ab] offset:6144\n\t" \
    "ds_read_b64_tr_b16 v[74:75], %[ab] offset:6912\n\t" \
    "ds_read_b128 v[48:51], %[xb] offset:576\n\t" \
    "s_waitcnt lgkmcnt(6)\n\t" \
    "v_mfma_f32_32x32x16_bf16 v[16:31], v[64:67], v[40:43], v[16:31]\n\t" \
    "ds_read_b64_tr_b16 v[76:77], %[ab] offset:9216\n\t" \
    "ds_read_b64_tr_b16 v[78:79], %[ab] offset:9984\n\t" \
    "ds_read_b128 v[52:55], %[xb] offset:608\n\t" \
    "s_waitcnt lgkmcnt(6)\n\t" \
    "v_mfma_f32_32x32x16_bf16 v[16:31], v[68:71], v[44:47], v[16:31]\n\t" \
    "ds_read_b64_tr_b16 v[64:65], %[ab] offset:12288\n\t" \
    "ds_read_b64_tr_b16 v[66:67], %[ab] offset:13056\n\t" \
    "ds_read_b128 v[40:43], %[xb] offset:640\n\t" \
    "s_waitcnt lgkmcnt(6)\n\t" \
    "v_mfma_f32_32x32x16_bf16 v[16:31], v[72:75], v[48:51], v[16:31]\n\t" \
    "ds_read_b64_tr_b16 v[68:69], %[ab] offset:15360\n\t" \
    "ds_read_b64_tr_b16 v[70:71], %[ab] offset:16128\n\t" \
    "ds_read_b128 v[44:47], %[xb] offset:672\n\t" \
    "s_waitcnt lgkmcnt(6)\n\t" \
    "v_mfma_f32_32x32x16_bf16 v[16:31], v[76:79], v[52:55], v[16:31]\n\t" \
    "ds_read_b64_tr_b16 v[72:73], %[ab] offset:18432\n\t" \
    "ds_read_b64_tr_b16 v[74:75], %[ab] offset:19200\n\t" \
    "ds_read_b128 v[48:51], %[xb] offset:704\n\t" \
    "s_waitcnt lgkmcnt(6)\n\t" \
    "v_mfma_f32_32x32x16_bf16 v[16:31], v[64:67], v[40:43], v[16:31]\n\t" \
    "ds_read_b64_tr_b16 v[76:77], %[ab] offset:21504\n\t" \
    "ds_read_b64_tr_b16 v[78:79], %[ab] offset:22272\n\t" \
    "ds_read_b128 v[52:55], %[xb] offset:736\n\t" \
    "s_waitcnt lgkmcnt(6)\n\t" \
    "v_mfma_f32_32x32x16_bf16 v[16:31], v[68:71], v[44:47], v[16:31]\n\t" \
    "ds_read_b64_tr_b16 v[64:65], %[ab] offset:24576\n\t" \
    "ds_read_b64_tr_b16 v[66:67], %[ab] offset:25344\n\t" \
    "ds_read_b128 v[40:43], %[xb] offset:768\n\t" \
    "s_waitcnt lgkmcnt(6)\n\t" \
    "v_mfma_f32_32x32x16_bf16 v[16:31], v[72:75], v[48:51], v[16:31]\n\t" \
    "ds_read_b64_tr_b16 v[68:69], %[ab] offset:27648\n\t" \
    "ds_read_b64_tr_b16 v[70:71], %[ab] offset:28416\n\t" \
    "ds_read_b128 v[44:47], %[xb] offset:800\n\t" \
    "s_waitcnt lgkmcnt(6)\n\t" \
    "v_mfma_f32_32x32x16_bf16 v[16:31], v[76:79], v[52:55], v[16:31]\n\t" \
    "ds_read_b64_tr_b16 v[72:73], %[ab] offset:30720\n\t" \
    "ds_read_b64_tr_b16 v[74:75], %[ab] offset:31488\n\t" \
    "ds_read_b128 v[48:51], %[xb] offset:832\n\t" \
    "s_waitcnt lgkmcnt(6)\n\t" \
    "v_mfma_f32_32x32x16_bf16 v[16:31], v[64:67], v[40:43], v[16:31]\n\t" \
    "ds_read_b64_tr_b16 v[76:77], %[ab] offset:33792\n\t" \
    "ds_read_b64_tr_b16 v[78:79], %[ab] offset:34560\n\t" \
    "ds_read_b128 v[52:55], %[xb] offset:864\n\t" \
    "s_waitcnt lgkmcnt(6)\n\t" \
    "v_mfma_f32_32x32x16_bf16 v[16:31], v[68:71], v[44:47], v[16:31]\n\t" \
    "ds_read_b64_tr_b16 v[64:65], %[ab] offset:36864\n\t" \
    "ds_read_b64_tr_b16 v[66:67], %[ab] offset:37632\n\t" \
    "ds_read_b128 v[40:43], %[xb] offset:896\n\t" \
    "s_waitcnt lgkmcnt(6)\n\t" \
    "v_mfma_f32_32x32x16_bf16 v[16:31], v[72:75], v[48:51], v[16:31]\n\t" \
    "ds_read_b64_tr_b16 v[68:69], %[ab] offset:39936\n\t" \
    "ds_read_b64_tr_b16 v[70:71], %[ab] offset:40704\n\t" \
    "ds_read_b128 v[44:47], %[xb] offset:928\n\t" \
    "s_barrier\n\t" \
    "s_waitcnt lgkmcnt(6)\n\t" \
    "v_mfma_f32_32x32x16_bf16 v[16:31], v[76:79], v[52:55], v[16:31]\n\t" \
    "ds_read_b64_tr_b16 v[72:73], %[ab] offset:43008\n\t" \
    "ds_read_b64_tr_b16 v[74:75], %[ab] offset:43776\n\t" \
    "ds_read_b128 v[48:51], %[xb] offset:960\n\t" \
    "s_waitcnt lgkmcnt(6)\n\t" \
    "v_mfma_f32_32x32x16_bf16 v[16:31], v[64:67], v[40:43], v[16:31]\n\t" \
    "ds_read_b64_tr_b16 v[76:77], %[ab] offset:46080\n\t" \
    "ds_read_b64_tr_b16 v[78:79], %[ab] offset:46848\n\t" \
    "ds_read_b128 v[52:55], %[xb] offset:992\n\t" \
    "s_waitcnt lgkmcnt(6)\n\t" \
    "v_mfma_f32_32x32x16_bf16 v[16:31], v[68:71], v[44:47], v[16:31]\n\t" \
    "ds_read_b64_tr_b16 v[64:65], %[a4] offset:0\n\t" \
    "ds_read_b64_tr_b16 v[66:67], %[a4] offset:768\n\t" \
    "ds_read_b128 v[40:43], %[xb] offset:1024\n\t" \
    "s_waitcnt lgkmcnt(6)\n\t" \
    "v_mfma_f32_32x32x16_bf16 v[16:31], v[72:75], v[48:51], v[16:31]\n\t" \
    "ds_read_b64_tr_b16 v[68:69], %[a4] offset:3072\n\t" \
    "ds_read_b64_tr_b16 v[70:71], %[a4] offset:3840\n\t" \
    "ds_read_b128 v[44:47], %[xb] offset:1056\n\t" \
    "s_waitcnt lgkmcnt(6)\n\t" \
    "v_mfma_f32_32x32x16_bf16 v[16:31], v[76:79], v[52:55], v[16:31]\n\t" \
    "ds_read_b64_tr_b16 v[72:73], %[a4] offset:6144\n\t" \
    "ds_read_b64_tr_b16 v[74:75], %[a4] offset:6912\n\t" \
    "ds_read_b128 v[48:51], %[xb] offset:1088\n\t" \
    "s_waitcnt lgkmcnt(6)\n\t" \
    "v_mfma_f32_32x32x16_bf16 v[16:31], v[64:67], v[40:43], v[16:31]\n\t" \
    "ds_read_b64_tr_b16 v[76:77], %[a4] offset:9216\n\t" \
    "ds_read_b64_tr_b16 v[78:79], %[a4] offset:9984\n\t" \
    "ds_read_b128 v[52:55], %[xb] offset:1120\n\t" \
    "s_waitcnt lgkmcnt(6)\n\t" \
    "v_mfma_f32_32x32x16_bf16 v[16:31], v[68:71], v[44:47], v[16:31]\n\t" \
    "ds_read_b64_tr_b16 v[64:65], %[a4] offset:12288\n\t" \
    "ds_read_b64_tr_b16 v[66:67], %[a4] offset:13056\n\t" \
    "ds_read_b128 v[40:43], %[xb] offset:1152\n\t" \
    "s_waitcnt lgkmcnt(6)\n\t" \
    "v_mfma_f32_32x32x16_bf16 v[16:31], v[72:75], v[48:51], v[16:31]\n\t" \
    "ds_read_b64_tr_b16 v[68:69], %[a4] offset:15360\n\t" \
    "ds_read_b64_tr_b16 v[70:71], %[a4] offset:16128\n\t" \
    "ds_read_b128 v[44:47], %[xb] offset:1184\n\t" \
    "s_waitcnt lgkmcnt(6)\n\t" \
    "v_mfma_f32_32x32x16_bf16 v[16:31], v[76:79], v[52:55], v[16:31]\n\t" \
    "ds_read_b64_tr_b16 v[72:73], %[a4] offset:18432\n\t" \
    "ds_read_b64_tr_b16 v[74:75], %[a4] offset:19200\n\t" \
    "ds_read_b128 v[48:51], %[xb] offset:1216\n\t" \
    "s_waitcnt lgkmcnt(6)\n\t" \
    "v_mfma_f32_32x32x16_bf16 v[16:31], v[64:67], v[40:43], v[16:31]\n\t" \
    "ds_read_b64_tr_b16 v[76:77], %[a4] offset:21504\n\t" \
    "ds_read_b64_tr_b16 v[78:79], %[a4] offset:22272\n\t" \
    "ds_read_b128 v[52:55], %[xb] offset:1248\n\t" \
    "s_waitcnt lgkmcnt(6)\n\t" \
    "v_mfma_f32_32x32x16_bf16 v[16:31], v[68:71], v[44:47], v[16:31]\n\t" \
    "s_waitcnt lgkmcnt(3)\n\t" \
    "v_mfma_f32_32x32x16_bf16 v[16:31], v[72:75], v[48:51], v[16:31]\n\t" \
    "s_waitcnt lgkmcnt(0)\n\t" \
    "v_mfma_f32_32x32x16_bf16 v[16:31], v[76:79], v[52:55], v[16:31]\n\t" \
    "s_waitcnt lgkmcnt(0)\n\t" \
    "s_nop 15\n\t"
#define OP_MAIN_ASM \
    "s_mov_b64 s[62:63], %[ob]\n\t" \
    "s_mov_b32 s70, 30000\n\t" \
    "global_load_dwordx4 v[112:115], %[off], s[62:63] offset:0 sc1\n\t" \
    "global_load_dwordx4 v[116:119], %[off], s[62:63] offset:1024 sc1\n\t" \
    "global_load_dwordx4 v[120:123], %[off], s[62:63] offset:2048 sc1\n\t" \
    "s_barrier\n\t" \
    "global_load_dwordx4 v[124:127], %[off], s[62:63] offset:3072 sc1\n\t" \
    "s_add_u32 s62, s62, 4096\n\t" \
    "s_addc_u32 s63, s63, 0\n\t" \
    "global_load_dwordx4 v[128:131], %[off], s[62:63] offset:0 sc1\n\t" \
    "global_load_dwordx4 v[132:135], %[off], s[62:63] offset:1024 sc1\n\t" \
    "s_barrier\n\t" \
    "global_load_dwordx4 v[136:139], %[off], s[62:63] offset:2048 sc1\n\t" \
    "global_load_dwordx4 v[140:143], %[off], s[62:63] offset:3072 sc1\n\t" \
    "s_add_u32 s62, s62, 4096\n\t" \
    "s_addc_u32 s63, s63, 0\n\t" \
    "global_load_dwordx4 v[144:147], %[off], s[62:63] offset:0 sc1\n\t" \
    "global_load_dwordx4 v[148:151], %[off], s[62:63] offset:1024 sc1\n\t" \
    "v_mov_b32 v16, 0\n\t" \
    "v_mov_b32 v17, 0\n\t" \
    "v_mov_b32 v18, 0\n\t" \
    "v_mov_b32 v19, 0\n\t" \
    "v_mov_b32 v20, 0\n\t" \
    "v_mov_b32 v21, 0\n\t" \
    "v_mov_b32 v22, 0\n\t" \
    "v_mov_b32 v23, 0\n\t" \
    "v_mov_b32 v24, 0\n\t" \
    "v_mov_b32 v25, 0\n\t" \
    "v_mov_b32 v26, 0\n\t" \
    "v_mov_b32 v27, 0\n\t" \
    "v_mov_b32 v28, 0\n\t" \
    "v_mov_b32 v29, 0\n\t" \
    "v_mov_b32 v30, 0\n\t" \
    "v_mov_b32 v31, 0\n\t" \
    "v_mov_b32 v32, 0\n\t" \
    "v_mov_b32 v33, 0\n\t" \
    "v_mov_b32 v34, 0\n\t" \
    "v_mov_b32 v35, 0\n\t" \
    "ds_read_b64_tr_b16 v[40:41], %[lr] offset:0\n\t" \
    "ds_read_b64_tr_b16 v[42:43], %[lr] offset:1280\n\t" \
    "ds_read_b64_tr_b16 v[44:45], %[lr] offset:32\n\t" \
    "ds_read_b64_tr_b16 v[46:47], %[lr] offset:1312\n\t" \
    "ds_read_b64_tr_b16 v[48:49], %[lr] offset:64\n\t" \
    "ds_read_b64_tr_b16 v[50:51], %[lr] offset:1344\n\t" \
    "ds_read_b64_tr_b16 v[52:53], %[lr] offset:96\n\t" \
    "ds_read_b64_tr_b16 v[54:55], %[lr] offset:1376\n\t" \
    "ds_read_b64_tr_b16 v[56:57], %[lr] offset:128\n\t" \
    "ds_read_b64_tr_b16 v[58:59], %[lr] offset:1408\n\t" \
    "s_mov_b64 s[64:65], %[wb]\n\t" \
    "s_waitcnt vmcnt(5)\n\t" \
    "CK0A_%=:\n\t" \
    "v_max3_u32 v6, v112, v113, v114\n\t" \
    "v_max3_u32 v6, v6, v115, v116\n\t" \
    "v_max3_u32 v6, v6, v117, v118\n\t" \
    "v_max3_u32 v6, v6, v119, v120\n\t" \
    "v_max3_u32 v6, v6, v121, v122\n\t" \
    "v_max3_u32 v6, v6, v123, v124\n\t" \
    "v_max3_u32 v6, v6, v125, v126\n\t" \
    "v_max3_u32 v6, v6, v127, v128\n\t" \
    "v_max3_u32 v6, v6, v129, v130\n\t" \
    "v_max_u32 v6, v6, v131\n\t" \
    "v_cmp_eq_u32 vcc, -1, v6\n\t" \
    "s_cbranch_vccz CK0B_%=\n\t" \
    "s_cmp_eq_u32 s70, 0\n\t" \
    "s_cbranch_scc1 CK0B_%=\n\t" \
    "s_sub_u32 s70, s70, 1\n\t" \
    "s_mov_b64 s[62:63], %[ob]\n\t" \
    "global_load_dwordx4 v[112:115], %[off], s[62:63] offset:0 sc1\n\t" \
    "global_load_dwordx4 v[116:119], %[off], s[62:63] offset:1024 sc1\n\t" \
    "global_load_dwordx4 v[120:123], %[off], s[62:63] offset:2048 sc1\n\t" \
    "global_load_dwordx4 v[124:127], %[off], s[62:63] offset:3072 sc1\n\t" \
    "s_add_u32 s62, s62, 4096\n\t" \
    "s_addc_u32 s63, s63, 0\n\t" \
    "global_load_dwordx4 v[128:131], %[off], s[62:63] offset:0 sc1\n\t" \
    "global_load_dwordx4 v[132:135], %[off], s[62:63] offset:1024 sc1\n\t" \
    "global_load_dwordx4 v[136:139], %[off], s[62:63] offset:2048 sc1\n\t" \
    "global_load_dwordx4 v[140:143], %[off], s[62:63] offset:3072 sc1\n\t" \
    "s_add_u32 s62, s62, 4096\n\t" \
    "s_addc_u32 s63, s63, 0\n\t" \
    "global_load_dwordx4 v[144:147], %[off], s[62:63] offset:0 sc1\n\t" \
    "global_load_dwordx4 v[148:151], %[off], s[62:63] offset:1024 sc1\n\t" \
    "s_waitcnt vmcnt(0)\n\t" \
    "s_branch CK0A_%=\n\t" \
    "CK0B_%=:\n\t" \
    "ds_read_b64_tr_b16 v[60:61], %[lr] offset:6400\n\t" \
    "ds_read_b64_tr_b16 v[62:63], %[lr] offset:7680\n\t" \
    "s_waitcnt lgkmcnt(10)\n\t" \
    "v_mfma_f32_16x16x32_bf16 v[16:19], v[40:43], v[112:115], v[16:19]\n\t" \
    "global_load_dwordx4 v[192:195], %[off], s[64:65] offset:0 sc1\n\t" \
    "ds_read_b64_tr_b16 v[64:65], %[lr] offset:6432\n\t" \
    "ds_read_b64_tr_b16 v[66:67], %[lr] offset:7712\n\t" \
    "s_waitcnt lgkmcnt(10)\n\t" \
    "v_mfma_f32_16x16x32_bf16 v[20:23], v[44:47], v[112:115], v[20:23]\n\t" \
    "ds_read_b64_tr_b16 v[68:69], %[lr] offset:6464\n\t" \
    "ds_read_b64_tr_b16 v[70:71], %[lr] offset:7744\n\t" \
    "s_waitcnt lgkmcnt(10)\n\t" \
    "v_mfma_f32_16x16x32_bf16 v[24:27], v[48:51], v[112:115], v[24:27]\n\t" \
    "global_load_dwordx4 v[196:199], %[off], s[64:65] offset:1024 sc1\n\t" \
    "ds_read_b64_tr_b16 v[72:73], %[lr] offset:6496\n\t" \
    "ds_read_b64_tr_b16 v[74:75], %[lr] offset:7776\n\t" \
    "s_waitcnt lgkmcnt(10)\n\t" \
    "v_mfma_f32_16x16x32_bf16 v[28:31], v[52:55], v[112:115], v[28:31]\n\t" \
    "ds_read_b64_tr_b16 v[76:77], %[lr] offset:6528\n\t" \
    "ds_read_b64_tr_b16 v[78:79], %[lr] offset:7808\n\t" \
    "s_waitcnt lgkmcnt(10)\n\t" \
    "v_mfma_f32_16x16x32_bf16 v[32:35], v[56:59], v[112:115], v[32:35]\n\t" \
    "ds_read_b64_tr_b16 v[40:41], %[lz] offset:0\n\t" \
    "ds_read_b64_tr_b16 v[42:43], %[lz] offset:3200\n\t" \
    "s_waitcnt lgkmcnt(10)\n\t" \
    "v_mfma_f32_16x16x32_bf16 v[16:19], v[60:63], v[116:119], v[16:19]\n\t" \
    "global_load_dwordx4 v[200:203], %[off], s[64:65] offset:2048 sc1\n\t" \
    "ds_read_b64_tr_b16 v[44:45], %[lz] offset:32\n\t" \
    "ds_read_b64_tr_b16 v[46:47], %[lz] offset:3232\n\t" \
    "s_waitcnt lgkmcnt(10)\n\t" \
    "v_mfma_f32_16x16x32_bf16 v[20:23], v[64:67], v[116:119], v[20:23]\n\t" \
    "ds_read_b64_tr_b16 v[48:49], %[lz] offset:64\n\t" \
    "ds_read_b64_tr_b16 v[50:51], %[lz] offset:3264\n\t" \
    "s_waitcnt lgkmcnt(10)\n\t" \
    "v_mfma_f32_16x16x32_bf16 v[24:27], v[68:71], v[116:119], v[24:27]\n\t" \
    "global_load_dwordx4 v[204:207], %[off], s[64:65] offset:3072 sc1\n\t" \
    "s_add_u32 s64, s64, 4096\n\t" \
    "s_addc_u32 s65, s65, 0\n\t" \
    "ds_read_b64_tr_b16 v[52:53], %[lz] offset:96\n\t" \
    "ds_read_b64_tr_b16 v[54:55], %[lz] offset:3296\n\t" \
    "s_waitcnt lgkmcnt(10)\n\t" \
    "v_mfma_f32_16x16x32_bf16 v[28:31], v[72:75], v[116:119], v[28:31]\n\t" \
    "ds_read_b64_tr_b16 v[56:57], %[lz] offset:128\n\t" \
    "ds_read_b64_tr_b16 v[58:59], %[lz] offset:3328\n\t" \
    "s_waitcnt lgkmcnt(10)\n\t" \
    "v_mfma_f32_16x16x32_bf16 v[32:35], v[76:79], v[116:119], v[32:35]\n\t" \
    "ds_read_b64_tr_b16 v[60:61], %[lr] offset:12800\n\t" \
    "ds_read_b64_tr_b16 v[62:63], %[lr] offset:14080\n\t" \
    "s_waitcnt lgkmcnt(10)\n\t" \
    "v_mfma_f32_16x16x32_bf16 v[16:19], v[40:43], v[120:123], v[16:19]\n\t" \
    "global_load_dwordx4 v[208:211], %[off], s[64:65] offset:0 sc1\n\t" \
    "ds_read_b64_tr_b16 v[64:65], %[lr] offset:12832\n\t" \
    "ds_read_b64_tr_b16 v[66:67], %[lr] offset:14112\n\t" \
    "s_waitcnt lgkmcnt(10)\n\t" \
    "v_mfma_f32_16x16x32_bf16 v[20:23], v[44:47], v[120:123], v[20:23]\n\t" \
    "ds_read_b64_tr_b16 v[68:69], %[lr] offset:12864\n\t" \
    "ds_read_b64_tr_b16 v[70:71], %[lr] offset:14144\n\t" \
    "s_waitcnt lgkmcnt(10)\n\t" \
    "v_mfma_f32_16x16x32_bf16 v[24:27], v[48:51], v[120:123], v[24:27]\n\t" \
    "ds_read_b64_tr_b16 v[72:73], %[lr] offset:12896\n\t" \
    "ds_read_b64_tr_b16 v[74:75], %[lr] offset:14176\n\t" \
    "s_waitcnt lgkmcnt(10)\n\t" \
    "v_mfma_f32_16x16x32_bf16 v[28:31], v[52:55], v[120:123], v[28:31]\n\t" \
    "ds_read_b64_tr_b16 v[76:77], %[lr] offset:12928\n\t" \
    "ds_read_b64_tr_b16 v[78:79], %[lr] offset:14208\n\t" \
    "s_waitcnt lgkmcnt(10)\n\t" \
    "v_mfma_f32_16x16x32_bf16 v[32:35], v[56:59], v[120:123], v[32:35]\n\t" \
    "ds_read_b64_tr_b16 v[40:41], %[lr] offset:19200\n\t" \
    "ds_read_b64_tr_b16 v[42:43], %[lr] offset:20480\n\t" \
    "s_waitcnt lgkmcnt(10)\n\t" \
    "v_mfma_f32_16x16x32_bf16 v[16:19], v[60:63], v[124:127], v[16:19]\n\t" \
    "global_load_dwordx4 v[212:215], %[off], s[64:65] offset:1024 sc1\n\t" \
    "ds_read_b64_tr_b16 v[44:45], %[lr] offset:19232\n\t" \
    "ds_read_b64_tr_b16 v[46:47], %[lr] offset:20512\n\t" \
    "s_waitcnt lgkmcnt(10)\n\t" \
    "v_mfma_f32_16x16x32_bf16 v[20:23], v[64:67], v[124:127], v[20:23]\n\t" \
    "ds_read_b64_tr_b16 v[48:49], %[lr] offset:19264\n\t" \
    "ds_read_b64_tr_b16 v[50:51], %[lr] offset:20544\n\t" \
    "s_waitcnt lgkmcnt(10)\n\t" \
    "v_mfma_f32_16x16x32_bf16 v[24:27], v[68:71], v[124:127], v[24:27]\n\t" \
    "ds_read_b64_tr_b16 v[52:53], %[lr] offset:19296\n\t" \
    "ds_read_b64_tr_b16 v[54:55], %[lr] offset:20576\n\t" \
    "s_waitcnt lgkmcnt(10)\n\t" \
    "v_mfma_f32_16x16x32_bf16 v[28:31], v[72:75], v[124:127], v[28:31]\n\t" \
    "ds_read_b64_tr_b16 v[56:57], %[lr] offset:19328\n\t" \
    "ds_read_b64_tr_b16 v[58:59], %[lr] offset:20608\n\t" \
    "s_waitcnt lgkmcnt(10)\n\t" \
    "v_mfma_f32_16x16x32_bf16 v[32:35], v[76:79], v[124:127], v[32:35]\n\t" \
    "ds_read_b64_tr_b16 v[60:61], %[lr] offset:25600\n\t" \
    "ds_read_b64_tr_b16 v[62:63], %[lr] offset:26880\n\t" \
    "s_waitcnt lgkmcnt(10)\n\t" \
    "v_mfma_f32_16x16x32_bf16 v[16:19], v[40:43], v[128:131], v[16:19]\n\t" \
    "global_load_dwordx4 v[216:219], %[off], s[64:65] offset:2048 sc1\n\t" \
    "ds_read_b64_tr_b16 v[64:65], %[lr] offset:25632\n\t" \
    "ds_read_b64_tr_b16 v[66:67], %[lr] offset:26912\n\t" \
    "s_waitcnt lgkmcnt(10)\n\t" \
    "v_mfma_f32_16x16x32_bf16 v[20:23], v[44:47], v[128:131], v[20:23]\n\t" \
    "ds_read_b64_tr_b16 v[68:69], %[lr] offset:25664\n\t" \
    "ds_read_b64_tr_b16 v[70:71], %[lr] offset:26944\n\t" \
    "s_waitcnt lgkmcnt(10)\n\t" \
    "v_mfma_f32_16x16x32_bf16 v[24:27], v[48:51], v[128:131], v[24:27]\n\t" \
    "ds_read_b64_tr_b16 v[72:73], %[lr] offset:25696\n\t" \
    "ds_read_b64_tr_b16 v[74:75], %[lr] offset:26976\n\t" \
    "s_waitcnt lgkmcnt(10)\n\t" \
    "v_mfma_f32_16x16x32_bf16 v[28:31], v[52:55], v[128:131], v[28:31]\n\t" \
    "ds_read_b64_tr_b16 v[76:77], %[lr] offset:25728\n\t" \
    "ds_read_b64_tr_b16 v[78:79], %[lr] offset:27008\n\t" \
    "s_waitcnt lgkmcnt(10)\n\t" \
    "v_mfma_f32_16x16x32_bf16 v[32:35], v[56:59], v[128:131], v[32:35]\n\t" \
    "s_waitcnt vmcnt(7)\n\t" \
    "CK1A_%=:\n\t" \
    "v_max3_u32 v6, v132, v133, v134\n\t" \
    "v_max3_u32 v6, v6, v135, v136\n\t" \
    "v_max3_u32 v6, v6, v137, v138\n\t" \
    "v_max3_u32 v6, v6, v139, v140\n\t" \
    "v_max3_u32 v6, v6, v141, v142\n\t" \
    "v_max3_u32 v6, v6, v143, v144\n\t" \
    "v_max3_u32 v6, v6, v145, v146\n\t" \
    "v_max3_u32 v6, v6, v147, v148\n\t" \
    "v_max3_u32 v6, v6, v149, v150\n\t" \
    "v_max_u32 v6, v6, v151\n\t" \
    "v_cmp_eq_u32 vcc, -1, v6\n\t" \
    "s_cbranch_vccz CK1B_%=\n\t" \
    "s_cmp_eq_u32 s70, 0\n\t" \
    "s_cbranch_scc1 CK1B_%=\n\t" \
    "s_sub_u32 s70, s70, 1\n\t" \
    "s_mov_b64 s[62:63], %[ob]\n\t" \
    "global_load_dwordx4 v[112:115], %[off], s[62:63] offset:0 sc1\n\t" \
    "global_load_dwordx4 v[116:119], %[off], s[62:63] offset:1024 sc1\n\t" \
    "global_load_dwordx4 v[120:123], %[off], s[62:63] offset:2048 sc1\n\t" \
    "global_load_dwordx4 v[124:127], %[off], s[62:63] offset:3072 sc1\n\t" \
    "s_add_u32 s62, s62, 4096\n\t" \
    "s_addc_u32 s63, s63, 0\n\t" \
    "global_load_dwordx4 v[128:131], %[off], s[62:63] offset:0 sc1\n\t" \
    "global_load_dwordx4 v[132:135], %[off], s[62:63] offset:1024 sc1\n\t" \
    "global_load_dwordx4 v[136:139], %[off], s[62:63] offset:2048 sc1\n\t" \
    "global_load_dwordx4 v[140:143], %[off], s[62:63] offset:3072 sc1\n\t" \
    "s_add_u32 s62, s62, 4096\n\t" \
    "s_addc_u32 s63, s63, 0\n\t" \
    "global_load_dwordx4 v[144:147], %[off], s[62:63] offset:0 sc1\n\t" \
    "global_load_dwordx4 v[148:151], %[off], s[62:63] offset:1024 sc1\n\t" \
    "s_waitcnt vmcnt(0)\n\t" \
    "s_branch CK1A_%=\n\t" \
    "CK1B_%=:\n\t" \
    "ds_read_b64_tr_b16 v[40:41], %[lr] offset:32000\n\t" \
    "ds_read_b64_tr_b16 v[42:43], %[lr] offset:33280\n\t" \
    "s_waitcnt lgkmcnt(10)\n\t" \
    "v_mfma_f32_16x16x32_bf16 v[16:19], v[60:63], v[132:135], v[16:19]\n\t" \
    "global_load_dwordx4 v[220:223], %[off], s[64:65] offset:3072 sc1\n\t" \
    "s_add_u32 s64, s64, 4096\n\t" \
    "s_addc_u32 s65, s65, 0\n\t" \
    "ds_read_b64_tr_b16 v[44:45], %[lr] offset:32032\n\t" \
    "ds_read_b64_tr_b16 v[46:47], %[lr] offset:33312\n\t" \
    "s_waitcnt lgkmcnt(10)\n\t" \
    "v_mfma_f32_16x16x32_bf16 v[20:23], v[64:67], v[132:135], v[20:23]\n\t" \
    "ds_read_b64_tr_b16 v[48:49], %[lr] offset:32064\n\t" \
    "ds_read_b64_tr_b16 v[50:51], %[lr] offset:33344\n\t" \
    "s_waitcnt lgkmcnt(10)\n\t" \
    "v_mfma_f32_16x16x32_bf16 v[24:27], v[68:71], v[132:135], v[24:27]\n\t" \
    "ds_read_b64_tr_b16 v[52:53], %[lr] offset:32096\n\t" \
    "ds_read_b64_tr_b16 v[54:55], %[lr] offset:33376\n\t" \
    "s_waitcnt lgkmcnt(10)\n\t" \
    "v_mfma_f32_16x16x32_bf16 v[28:31], v[72:75], v[132:135], v[28:31]\n\t" \
    "ds_read_b64_tr_b16 v[56:57], %[lr] offset:32128\n\t" \
    "ds_read_b64_tr_b16 v[58:59], %[lr] offset:33408\n\t" \
    "s_waitcnt lgkmcnt(10)\n\t" \
    "v_mfma_f32_16x16x32_bf16 v[32:35], v[76:79], v[132:135], v[32:35]\n\t" \
    "ds_read_b64_tr_b16 v[60:61], %[lz] offset:25600\n\t" \
    "ds_read_b64_tr_b16 v[62:63], %[lz] offset:28800\n\t" \
    "s_waitcnt lgkmcnt(10)\n\t" \
    "v_mfma_f32_16x16x32_bf16 v[16:19], v[40:43], v[136:139], v[16:19]\n\t" \
    "global_load_dwordx4 v[224:227], %[off], s[64:65] offset:0 sc1\n\t" \
    "ds_read_b64_tr_b16 v[64:65], %[lz] offset:25632\n\t" \
    "ds_read_b64_tr_b16 v[66:67], %[lz] offset:28832\n\t" \
    "s_waitcnt lgkmcnt(10)\n\t" \
    "v_mfma_f32_16x16x32_bf16 v[20:23], v[44:47], v[136:139], v[20:23]\n\t" \
    "ds_read_b64_tr_b16 v[68:69], %[lz] offset:25664\n\t" \
    "ds_read_b64_tr_b16 v[70:71], %[lz] offset:28864\n\t" \
    "s_waitcnt lgkmcnt(10)\n\t" \
    "v_mfma_f32_16x16x32_bf16 v[24:27], v[48:51], v[136:139], v[24:27]\n\t" \
    "ds_read_b64_tr_b16 v[72:73], %[lz] offset:25696\n\t" \
    "ds_read_b64_tr_b16 v[74:75], %[lz] offset:28896\n\t" \
    "s_waitcnt lgkmcnt(10)\n\t" \
    "v_mfma_f32_16x16x32_bf16 v[28:31], v[52:55], v[136:139], v[28:31]\n\t" \
    "ds_read_b64_tr_b16 v[76:77], %[lz] offset:25728\n\t" \
    "ds_read_b64_tr_b16 v[78:79], %[lz] offset:28928\n\t" \
    "s_waitcnt lgkmcnt(10)\n\t" \
    "v_mfma_f32_16x16x32_bf16 v[32:35], v[56:59], v[136:139], v[32:35]\n\t" \
    "ds_read_b64_tr_b16 v[40:41], %[lr] offset:38400\n\t" \
    "ds_read_b64_tr_b16 v[42:43], %[lr] offset:39680\n\t" \
    "s_waitcnt lgkmcnt(10)\n\t" \
    "v_mfma_f32_16x16x32_bf16 v[16:19], v[60:63], v[140:143], v[16:19]\n\t" \
    "global_load_dwordx4 v[228:231], %[off], s[64:65] offset:1024 sc1\n\t" \
    "ds_read_b64_tr_b16 v[44:45], %[lr] offset:38432\n\t" \
    "ds_read_b64_tr_b16 v[46:47], %[lr] offset:39712\n\t" \
    "s_waitcnt lgkmcnt(10)\n\t" \
    "v_mfma_f32_16x16x32_bf16 v[20:23], v[64:67], v[140:143], v[20:23]\n\t" \
    "ds_read_b64_tr_b16 v[48:49], %[lr] offset:38464\n\t" \
    "ds_read_b64_tr_b16 v[50:51], %[lr] offset:39744\n\t" \
    "s_waitcnt lgkmcnt(10)\n\t" \
    "v_mfma_f32_16x16x32_bf16 v[24:27], v[68:71], v[140:143], v[24:27]\n\t" \
    "ds_read_b64_tr_b16 v[52:53], %[lr] offset:38496\n\t" \
    "ds_read_b64_tr_b16 v[54:55], %[lr] offset:39776\n\t" \
    "s_waitcnt lgkmcnt(10)\n\t" \
    "v_mfma_f32_16x16x32_bf16 v[28:31], v[72:75], v[140:143], v[28:31]\n\t" \
    "ds_read_b64_tr_b16 v[56:57], %[lr] offset:38528\n\t" \
    "ds_read_b64_tr_b16 v[58:59], %[lr] offset:39808\n\t" \
    "s_waitcnt lgkmcnt(10)\n\t" \
    "v_mfma_f32_16x16x32_bf16 v[32:35], v[76:79], v[140:143], v[32:35]\n\t" \
    "ds_read_b64_tr_b16 v[60:61], %[lr] offset:44800\n\t" \
    "ds_read_b64_tr_b16 v[62:63], %[lr] offset:46080\n\t" \
    "s_waitcnt lgkmcnt(10)\n\t" \
    "v_mfma_f32_16x16x32_bf16 v[16:19], v[40:43], v[144:147], v[16:19]\n\t" \
    "global_load_dwordx4 v[232:235], %[off], s[64:65] offset:2048 sc1\n\t" \
    "ds_read_b64_tr_b16 v[64:65], %[lr] offset:44832\n\t" \
    "ds_read_b64_tr_b16 v[66:67], %[lr] offset:46112\n\t" \
    "s_waitcnt lgkmcnt(10)\n\t" \
    "v_mfma_f32_16x16x32_bf16 v[20:23], v[44:47], v[144:147], v[20:23]\n\t" \
    "ds_read_b64_tr_b16 v[68:69], %[lr] offset:44864\n\t" \
    "ds_read_b64_tr_b16 v[70:71], %[lr] offset:46144\n\t" \
    "s_waitcnt lgkmcnt(10)\n\t" \
    "v_mfma_f32_16x16x32_bf16 v[24:27], v[48:51], v[144:147], v[24:27]\n\t" \
    "ds_read_b64_tr_b16 v[72:73], %[lr] offset:44896\n\t" \
    "ds_read_b64_tr_b16 v[74:75], %[lr] offset:46176\n\t" \
    "s_waitcnt lgkmcnt(10)\n\t" \
    "v_mfma_f32_16x16x32_bf16 v[28:31], v[52:55], v[144:147], v[28:31]\n\t" \
    "ds_read_b64_tr_b16 v[76:77], %[lr] offset:44928\n\t" \
    "ds_read_b64_tr_b16 v[78:79], %[lr] offset:46208\n\t" \
    "s_waitcnt lgkmcnt(10)\n\t" \
    "v_mfma_f32_16x16x32_bf16 v[32:35], v[56:59], v[144:147], v[32:35]\n\t" \
    "s_waitcnt lgkmcnt(8)\n\t" \
    "v_mfma_f32_16x16x32_bf16 v[16:19], v[60:63], v[148:151], v[16:19]\n\t" \
    "global_load_dwordx4 v[236:239], %[off], s[64:65] offset:3072 sc1\n\t" \
    "s_add_u32 s64, s64, 4096\n\t" \
    "s_addc_u32 s65, s65, 0\n\t" \
    "s_waitcnt lgkmcnt(6)\n\t" \
    "v_mfma_f32_16x16x32_bf16 v[20:23], v[64:67], v[148:151], v[20:23]\n\t" \
    "s_waitcnt lgkmcnt(4)\n\t" \
    "v_mfma_f32_16x16x32_bf16 v[24:27], v[68:71], v[148:151], v[24:27]\n\t" \
    "s_waitcnt lgkmcnt(2)\n\t" \
    "v_mfma_f32_16x16x32_bf16 v[28:31], v[72:75], v[148:151], v[28:31]\n\t" \
    "s_waitcnt lgkmcnt(0)\n\t" \
    "v_mfma_f32_16x16x32_bf16 v[32:35], v[76:79], v[148:151], v[32:35]\n\t" \
    "s_nop 15\n\t"
#define OP_MAIN_CLOBBERS "v6", "v40", "v41", "v42", "v43", "v44", "v45", "v46", "v47", "v48", "v49", "v50", "v51", "v52", "v53", "v54", "v55", "v56", "v57", "v58", "v59", "v60", "v61", "v62", "v63", "v64", "v65", "v66", "v67", "v68", "v69", "v70", "v71", "v72", "v73", "v74", "v75", "v76", "v77", "v78", "v79", "v112", "v113", "v114", "v115", "v116", "v117", "v118", "v119", "v120", "v121", "v122", "v123", "v124", "v125", "v126", "v127", "v128", "v129", "v130", "v131", "v132", "v133", "v134", "v135", "v136", "v137", "v138", "v139", "v140", "v141", "v142", "v143", "v144", "v145", "v146", "v147", "v148", "v149", "v150", "v151", "s62", "s63", "s64", "s65", "s70", "vcc", "scc", "memory"
#define GU_PRE1_ASM \
    "s_mov_b64 s[60:61], %[wb]\n\t" \
    "global_load_dwordx4 v[160:163], %[off], s[60:61] offset:0 sc1\n\t" \
    "global_load_dwordx4 v[164:167], %[off], s[60:61] offset:1024 sc1\n\t" \
    "global_load_dwordx4 v[168:171], %[off], s[60:61] offset:2048 sc1\n\t" \
    "global_load_dwordx4 v[172:175], %[off], s[60:61] offset:3072 sc1\n\t" \
    "s_add_u32 s60, s60, 4096\n\t" \
    "s_addc_u32 s61, s61, 0\n\t" \
    "global_load_dwordx4 v[176:179], %[off], s[60:61] offset:0 sc1\n\t" \
    "global_load_dwordx4 v[180:183], %[off], s[60:61] offset:1024 sc1\n\t" \
    "global_load_dwordx4 v[184:187], %[off], s[60:61] offset:2048 sc1\n\t" \
    "global_load_dwordx4 v[188:191], %[off], s[60:61] offset:3072 sc1\n\t"
#define GU_PRE2_ASM \
    "s_mov_b64 s[60:61], %[wb]\n\t" \
    "global_load_dwordx4 v[240:243], %[off], s[60:61] offset:0 sc1\n\t" \
    "global_load_dwordx4 v[244:247], %[off], s[60:61] offset:1024 sc1\n\t" \
    "global_load_dwordx4 v[248:251], %[off], s[60:61] offset:2048 sc1\n\t" \
    "global_load_dwordx4 v[252:255], %[off], s[60:61] offset:3072 sc1\n\t"
#define GU_MAIN_ASM \
    "s_mov_b64 s[60:61], %[wb]\n\t" \
    "s_mov_b64 s[62:63], %[hb]\n\t" \
    "s_mov_b32 s70, 30000\n\t" \
    "s_mov_b64 s[64:65], %[wd]\n\t" \
    "global_load_dwordx4 v[80:83], %[off], s[62:63] offset:0 sc1\n\t" \
    "global_load_dwordx4 v[84:87], %[off], s[62:63] offset:1024 sc1\n\t" \
    "global_load_dwordx4 v[88:91], %[off], s[62:63] offset:2048 sc1\n\t" \
    "global_load_dwordx4 v[92:95], %[off], s[62:63] offset:3072 sc1\n\t" \
    "s_add_u32 s62, s62, 4096\n\t" \
    "s_addc_u32 s63, s63, 0\n\t" \
    "s_barrier\n\t" \
    "global_load_dwordx4 v[96:99], %[off], s[62:63] offset:0 sc1\n\t" \
    "global_load_dwordx4 v[100:103], %[off], s[62:63] offset:1024 sc1\n\t" \
    "global_load_dwordx4 v[104:107], %[off], s[62:63] offset:2048 sc1\n\t" \
    "global_load_dwordx4 v[108:111], %[off], s[62:63] offset:3072 sc1\n\t" \
    "s_add_u32 s62, s62, 4096\n\t" \
    "s_addc_u32 s63, s63, 0\n\t" \
    "v_add_u32 v8, %[l0], %[off]\n\t" \
    "v_add_u32 v9, 0x10000, v8\n\t" \
    "v_add_u32 v10, 0x20000, v8\n\t" \
    "v_add_u32 v11, %[wo], v8\n\t" \
    "v_add_u32 v12, 0x10000, v11\n\t" \
    "v_add_u32 v13, 0x20000, v11\n\t" \
    "v_and_b32 v14, 0xf0, %[off]\n\t" \
    "v_lshlrev_b32 v14, 2, v14\n\t" \
    "v_lshrrev_b32 v15, 8, %[off]\n\t" \
    "v_lshl_add_u32 v14, v15, 4, v14\n\t" \
    "v_mov_b32 v16, 0\n\t" \
    "v_mov_b32 v17, 0\n\t" \
    "v_mov_b32 v18, 0\n\t" \
    "v_mov_b32 v19, 0\n\t" \
    "v_mov_b32 v20, 0\n\t" \
    "v_mov_b32 v21, 0\n\t" \
    "v_mov_b32 v22, 0\n\t" \
    "v_mov_b32 v23, 0\n\t" \
    "v_mov_b32 v24, 0\n\t" \
    "v_mov_b32 v25, 0\n\t" \
    "v_mov_b32 v26, 0\n\t" \
    "v_mov_b32 v27, 0\n\t" \
    "v_mov_b32 v28, 0\n\t" \
    "v_mov_b32 v29, 0\n\t" \
    "v_mov_b32 v30, 0\n\t" \
    "v_mov_b32 v31, 0\n\t" \
    "v_mov_b32 v32, 0\n\t" \
    "v_mov_b32 v33, 0\n\t" \
    "v_mov_b32 v34, 0\n\t" \
    "v_mov_b32 v35, 0\n\t" \
    "v_mov_b32 v36, 0\n\t" \
    "v_mov_b32 v37, 0\n\t" \
    "v_mov_b32 v38, 0\n\t" \
    "v_mov_b32 v39, 0\n\t" \
    "v_mov_b32 v40, 0\n\t" \
    "v_mov_b32 v41, 0\n\t" \
    "v_mov_b32 v42, 0\n\t" \
    "v_mov_b32 v43, 0\n\t" \
    "v_mov_b32 v44, 0\n\t" \
    "v_mov_b32 v45, 0\n\t" \
    "v_mov_b32 v46, 0\n\t" \
    "v_mov_b32 v47, 0\n\t" \
    "v_mov_b32 v48, 0\n\t" \
    "v_mov_b32 v49, 0\n\t" \
    "v_mov_b32 v50, 0\n\t" \
    "v_mov_b32 v51, 0\n\t" \
    "v_mov_b32 v52, 0\n\t" \
    "v_mov_b32 v53, 0\n\t" \
    "v_mov_b32 v54, 0\n\t" \
    "v_mov_b32 v55, 0\n\t" \
    "v_mov_b32 v56, 0\n\t" \
    "v_mov_b32 v57, 0\n\t" \
    "v_mov_b32 v58, 0\n\t" \
    "v_mov_b32 v59, 0\n\t" \
    "v_mov_b32 v60, 0\n\t" \
    "v_mov_b32 v61, 0\n\t" \
    "v_mov_b32 v62, 0\n\t" \
    "v_mov_b32 v63, 0\n\t" \
    "v_mov_b32 v64, 0\n\t" \
    "v_mov_b32 v65, 0\n\t" \
    "v_mov_b32 v66, 0\n\t" \
    "v_mov_b32 v67, 0\n\t" \
    "v_mov_b32 v68, 0\n\t" \
    "v_mov_b32 v69, 0\n\t" \
    "v_mov_b32 v70, 0\n\t" \
    "v_mov_b32 v71, 0\n\t" \
    "v_mov_b32 v72, 0\n\t" \
    "v_mov_b32 v73, 0\n\t" \
    "v_mov_b32 v74, 0\n\t" \
    "v_mov_b32 v75, 0\n\t" \
    "v_mov_b32 v76, 0\n\t" \
    "v_mov_b32 v77, 0\n\t" \
    "v_mov_b32 v78, 0\n\t" \
    "v_mov_b32 v79, 0\n\t" \
    "v_add_u32 v15, 4096, %[off]\n\t" \
    "v_add_u32 v7, %[lw], %[off]\n\t" \
    "s_add_u32 s66, s64, 10240\n\t" \
    "s_addc_u32 s67, s65, 0\n\t" \
    "s_waitcnt vmcnt(0)\n\t" \
    "CK0A_%=:\n\t" \
    "v_max3_u32 v6, v80, v81, v82\n\t" \
    "v_max3_u32 v6, v6, v83, v84\n\t" \
    "v_max3_u32 v6, v6, v85, v86\n\t" \
    "v_max3_u32 v6, v6, v87, v88\n\t" \
    "v_max3_u32 v6, v6, v89, v90\n\t" \
    "v_max3_u32 v6, v6, v91, v92\n\t" \
    "v_max3_u32 v6, v6, v93, v94\n\t" \
    "v_max3_u32 v6, v6, v95, v96\n\t" \
    "v_max3_u32 v6, v6, v97, v98\n\t" \
    "v_max3_u32 v6, v6, v99, v100\n\t" \
    "v_max3_u32 v6, v6, v101, v102\n\t" \
    "v_max3_u32 v6, v6, v103, v104\n\t" \
    "v_max3_u32 v6, v6, v105, v106\n\t" \
    "v_max3_u32 v6, v6, v107, v108\n\t" \
    "v_max3_u32 v6, v6, v109, v110\n\t" \
    "v_max_u32 v6, v6, v111\n\t" \
    "v_cmp_eq_u32 vcc, -1, v6\n\t" \
    "s_cbranch_vccz CK0B_%=\n\t" \
    "s_cmp_eq_u32 s70, 0\n\t" \
    "s_cbranch_scc1 CK0B_%=\n\t" \
    "s_sub_u32 s70, s70, 1\n\t" \
    "s_mov_b64 s[68:69], %[hb]\n\t" \
    "global_load_dwordx4 v[80:83], %[off], s[68:69] offset:0 sc1\n\t" \
    "global_load_dwordx4 v[84:87], %[off], s[68:69] offset:1024 sc1\n\t" \
    "global_load_dwordx4 v[88:91], %[off], s[68:69] offset:2048 sc1\n\t" \
    "global_load_dwordx4 v[92:95], %[off], s[68:69] offset:3072 sc1\n\t" \
    "s_add_u32 s68, s68, 4096\n\t" \
    "s_addc_u32 s69, s69, 0\n\t" \
    "global_load_dwordx4 v[96:99], %[off], s[68:69] offset:0 sc1\n\t" \
    "global_load_dwordx4 v[100:103], %[off], s[68:69] offset:1024 sc1\n\t" \
    "global_load_dwordx4 v[104:107], %[off], s[68:69] offset:2048 sc1\n\t" \
    "global_load_dwordx4 v[108:111], %[off], s[68:69] offset:3072 sc1\n\t" \
    "s_waitcnt vmcnt(0)\n\t" \
    "s_branch CK0A_%=\n\t" \
    "CK0B_%=:\n\t" \
    "ds_write_b128 v11, v[80:83] offset:0\n\t" \
    "ds_write_b128 v11, v[84:87] offset:8192\n\t" \
    "ds_write_b128 v11, v[88:91] offset:16384\n\t" \
    "ds_write_b128 v11, v[92:95] offset:24576\n\t" \
    "ds_write_b128 v11, v[96:99] offset:32768\n\t" \
    "ds_write_b128 v11, v[100:103] offset:40960\n\t" \
    "ds_write_b128 v11, v[104:107] offset:49152\n\t" \
    "ds_write_b128 v11, v[108:111] offset:57344\n\t" \
    "s_waitcnt lgkmcnt(0)\n\t" \
    "s_barrier\n\t" \
    "ds_read_b128 v[80:83], v8 offset:0\n\t" \
    "ds_read_b128 v[84:87], v8 offset:1024\n\t" \
    "ds_read_b128 v[88:91], v8 offset:2048\n\t" \
    "ds_read_b128 v[92:95], v8 offset:3072\n\t" \
    "ds_read_b128 v[96:99], v8 offset:4096\n\t" \
    "ds_read_b128 v[100:103], v8 offset:5120\n\t" \
    "ds_read_b128 v[104:107], v8 offset:6144\n\t" \
    "ds_read_b128 v[108:111], v8 offset:7168\n\t" \
    "s_waitcnt lgkmcnt(7)\n\t" \
    "v_mfma_f32_16x16x32_bf16 v[16:19], v[160:163], v[80:83], v[16:19]\n\t" \
    "v_mfma_f32_16x16x32_bf16 v[20:23], v[164:167], v[80:83], v[20:23]\n\t" \
    "s_waitcnt lgkmcnt(6)\n\t" \
    "v_mfma_f32_16x16x32_bf16 v[24:27], v[160:163], v[84:87], v[24:27]\n\t" \
    "ds_read_b128 v[80:83], v8 offset:8192\n\t" \
    "v_mfma_f32_16x16x32_bf16 v[28:31], v[164:167], v[84:87], v[28:31]\n\t" \
    "global_load_dwordx4 v[112:115], %[off], s[62:63] offset:0 sc1\n\t" \
    "s_waitcnt lgkmcnt(6)\n\t" \
    "v_mfma_f32_16x16x32_bf16 v[32:35], v[160:163], v[88:91], v[32:35]\n\t" \
    "ds_read_b128 v[84:87], v8 offset:9216\n\t" \
    "v_mfma_f32_16x16x32_bf16 v[36:39], v[164:167], v[88:91], v[36:39]\n\t" \
    "s_waitcnt lgkmcnt(6)\n\t" \
    "v_mfma_f32_16x16x32_bf16 v[40:43], v[160:163], v[92:95], v[40:43]\n\t" \
    "ds_read_b128 v[88:91], v8 offset:10240\n\t" \
    "v_mfma_f32_16x16x32_bf16 v[44:47], v[164:167], v[92:95], v[44:47]\n\t" \
    "s_waitcnt lgkmcnt(6)\n\t" \
    "v_mfma_f32_16x16x32_bf16 v[48:51], v[160:163], v[96:99], v[48:51]\n\t" \
    "ds_read_b128 v[92:95], v8 offset:11264\n\t" \
    "v_mfma_f32_16x16x32_bf16 v[52:55], v[164:167], v[96:99], v[52:55]\n\t" \
    "global_load_dwordx4 v[116:119], %[off], s[62:63] offset:1024 sc1\n\t" \
    "s_waitcnt lgkmcnt(6)\n\t" \
    "v_mfma_f32_16x16x32_bf16 v[56:59], v[160:163], v[100:103], v[56:59]\n\t" \
    "ds_read_b128 v[96:99], v8 offset:12288\n\t" \
    "v_mfma_f32_16x16x32_bf16 v[60:63], v[164:167], v[100:103], v[60:63]\n\t" \
    "s_waitcnt lgkmcnt(6)\n\t" \
    "v_mfma_f32_16x16x32_bf16 v[64:67], v[160:163], v[104:107], v[64:67]\n\t" \
    "ds_read_b128 v[100:103], v8 offset:13312\n\t" \
    "v_mfma_f32_16x16x32_bf16 v[68:71], v[164:167], v[104:107], v[68:71]\n\t" \
    "global_load_dwordx4 v[120:123], %[off], s[62:63] offset:2048 sc1\n\t" \
    "s_waitcnt lgkmcnt(6)\n\t" \
    "v_mfma_f32_16x16x32_bf16 v[72:75], v[160:163], v[108:111], v[72:75]\n\t" \
    "ds_read_b128 v[104:107], v8 offset:14336\n\t" \
    "v_mfma_f32_16x16x32_bf16 v[76:79], v[164:167], v[108:111], v[76:79]\n\t" \
    "s_nop 0\n\t" \
    "ds_read_b128 v[108:111], v8 offset:15360\n\t" \
    "s_waitcnt lgkmcnt(7)\n\t" \
    "v_mfma_f32_16x16x32_bf16 v[16:19], v[168:171], v[80:83], v[16:19]\n\t" \
    "v_mfma_f32_16x16x32_bf16 v[20:23], v[172:175], v[80:83], v[20:23]\n\t" \
    "s_waitcnt lgkmcnt(6)\n\t" \
    "v_mfma_f32_16x16x32_bf16 v[24:27], v[168:171], v[84:87], v[24:27]\n\t" \
    "ds_read_b128 v[80:83], v8 offset:16384\n\t" \
    "v_mfma_f32_16x16x32_bf16 v[28:31], v[172:175], v[84:87], v[28:31]\n\t" \
    "global_load_dwordx4 v[124:127], %[off], s[62:63] offset:3072 sc1\n\t" \
    "s_add_u32 s62, s62, 4096\n\t" \
    "s_addc_u32 s63, s63, 0\n\t" \
    "s_waitcnt lgkmcnt(6)\n\t" \
    "v_mfma_f32_16x16x32_bf16 v[32:35], v[168:171], v[88:91], v[32:35]\n\t" \
    "ds_read_b128 v[84:87], v8 offset:17408\n\t" \
    "v_mfma_f32_16x16x32_bf16 v[36:39], v[172:175], v[88:91], v[36:39]\n\t" \
    "s_waitcnt lgkmcnt(6)\n\t" \
    "v_mfma_f32_16x16x32_bf16 v[40:43], v[168:171], v[92:95], v[40:43]\n\t" \
    "ds_read_b128 v[88:91], v8 offset:18432\n\t" \
    "v_mfma_f32_16x16x32_bf16 v[44:47], v[172:175], v[92:95], v[44:47]\n\t" \
    "s_waitcnt lgkmcnt(6)\n\t" \
    "v_mfma_f32_16x16x32_bf16 v[48:51], v[168:171], v[96:99], v[48:51]\n\t" \
    "ds_read_b128 v[92:95], v8 offset:19456\n\t" \
    "v_mfma_f32_16x16x32_bf16 v[52:55], v[172:175], v[96:99], v[52:55]\n\t" \
    "global_load_dwordx4 v[128:131], %[off], s[62:63] offset:0 sc1\n\t" \
    "s_waitcnt lgkmcnt(6)\n\t" \
    "v_mfma_f32_16x16x32_bf16 v[56:59], v[168:171], v[100:103], v[56:59]\n\t" \
    "ds_read_b128 v[96:99], v8 offset:20480\n\t" \
    "v_mfma_f32_16x16x32_bf16 v[60:63], v[172:175], v[100:103], v[60:63]\n\t" \
    "s_waitcnt lgkmcnt(6)\n\t" \
    "v_mfma_f32_16x16x32_bf16 v[64:67], v[168:171], v[104:107], v[64:67]\n\t" \
    "ds_read_b128 v[100:103], v8 offset:21504\n\t" \
    "v_mfma_f32_16x16x32_bf16 v[68:71], v[172:175], v[104:107], v[68:71]\n\t" \
    "global_load_dwordx4 v[132:135], %[off], s[62:63] offset:1024 sc1\n\t" \
    "s_waitcnt lgkmcnt(6)\n\t" \
    "v_mfma_f32_16x16x32_bf16 v[72:75], v[168:171], v[108:111], v[72:75]\n\t" \
    "ds_read_b128 v[104:107], v8 offset:22528\n\t" \
    "v_mfma_f32_16x16x32_bf16 v[76:79], v[172:175], v[108:111], v[76:79]\n\t" \
    "s_nop 0\n\t" \
    "ds_read_b128 v[108:111], v8 offset:23552\n\t" \
    "s_waitcnt lgkmcnt(7)\n\t" \
    "v_mfma_f32_16x16x32_bf16 v[16:19], v[176:179], v[80:83], v[16:19]\n\t" \
    "v_mfma_f32_16x16x32_bf16 v[20:23], v[180:183], v[80:83], v[20:23]\n\t" \
    "s_waitcnt lgkmcnt(6)\n\t" \
    "v_mfma_f32_16x16x32_bf16 v[24:27], v[176:179], v[84:87], v[24:27]\n\t" \
    "ds_read_b128 v[80:83], v8 offset:24576\n\t" \
    "v_mfma_f32_16x16x32_bf16 v[28:31], v[180:183], v[84:87], v[28:31]\n\t" \
    "global_load_dwordx4 v[136:139], %[off], s[62:63] offset:2048 sc1\n\t" \
    "s_waitcnt lgkmcnt(6)\n\t" \
    "v_mfma_f32_16x16x32_bf16 v[32:35], v[176:179], v[88:91], v[32:35]\n\t" \
    "ds_read_b128 v[84:87], v8 offset:25600\n\t" \
    "v_mfma_f32_16x16x32_bf16 v[36:39], v[180:183], v[88:91], v[36:39]\n\t" \
    "s_waitcnt lgkmcnt(6)\n\t" \
    "v_mfma_f32_16x16x32_bf16 v[40:43], v[176:179], v[92:95], v[40:43]\n\t" \
    "ds_read_b128 v[88:91], v8 offset:26624\n\t" \
    "v_mfma_f32_16x16x32_bf16 v[44:47], v[180:183], v[92:95], v[44:47]\n\t" \
    "s_waitcnt lgkmcnt(6)\n\t" \
    "v_mfma_f32_16x16x32_bf16 v[48:51], v[176:179], v[96:99], v[48:51]\n\t" \
    "ds_read_b128 v[92:95], v8 offset:27648\n\t" \
    "v_mfma_f32_16x16x32_bf16 v[52:55], v[180:183], v[96:99], v[52:55]\n\t" \
    "global_load_dwordx4 v[140:143], %[off], s[62:63] offset:3072 sc1\n\t" \
    "s_add_u32 s62, s62, 4096\n\t" \
    "s_addc_u32 s63, s63, 0\n\t" \
    "s_waitcnt lgkmcnt(6)\n\t" \
    "v_mfma_f32_16x16x32_bf16 v[56:59], v[176:179], v[100:103], v[56:59]\n\t" \
    "ds_read_b128 v[96:99], v8 offset:28672\n\t" \
    "v_mfma_f32_16x16x32_bf16 v[60:63], v[180:183], v[100:103], v[60:63]\n\t" \
    "s_waitcnt lgkmcnt(6)\n\t" \
    "v_mfma_f32_16x16x32_bf16 v[64:67], v[176:179], v[104:107], v[64:67]\n\t" \
    "ds_read_b128 v[100:103], v8 offset:29696\n\t" \
    "v_mfma_f32_16x16x32_bf16 v[68:71], v[180:183], v[104:107], v[68:71]\n\t" \
    "s_waitcnt lgkmcnt(6)\n\t" \
    "v_mfma_f32_16x16x32_bf16 v[72:75], v[176:179], v[108:111], v[72:75]\n\t" \
    "ds_read_b128 v[104:107], v8 offset:30720\n\t" \
    "v_mfma_f32_16x16x32_bf16 v[76:79], v[180:183], v[108:111], v[76:79]\n\t" \
    "s_nop 0\n\t" \
    "ds_read_b128 v[108:111], v8 offset:31744\n\t" \
    "s_waitcnt lgkmcnt(7)\n\t" \
    "v_mfma_f32_16x16x32_bf16 v[16:19], v[184:187], v[80:83], v[16:19]\n\t" \
    "v_mfma_f32_16x16x32_bf16 v[20:23], v[188:191], v[80:83], v[20:23]\n\t" \
    "s_waitcnt lgkmcnt(6)\n\t" \
    "v_mfma_f32_16x16x32_bf16 v[24:27], v[184:187], v[84:87], v[24:27]\n\t" \
    "ds_read_b128 v[80:83], v8 offset:32768\n\t" \
    "v_mfma_f32_16x16x32_bf16 v[28:31], v[188:191], v[84:87], v[28:31]\n\t" \
    "global_load_dwordx4 v[144:147], %[off], s[62:63] offset:0 sc1\n\t" \
    "s_waitcnt lgkmcnt(6)\n\t" \
    "v_mfma_f32_16x16x32_bf16 v[32:35], v[184:187], v[88:91], v[32:35]\n\t" \
    "ds_read_b128 v[84:87], v8 offset:33792\n\t" \
    "v_mfma_f32_16x16x32_bf16 v[36:39], v[188:191], v[88:91], v[36:39]\n\t" \
    "s_waitcnt lgkmcnt(6)\n\t" \
    "v_mfma_f32_16x16x32_bf16 v[40:43], v[184:187], v[92:95], v[40:43]\n\t" \
    "ds_read_b128 v[88:91], v8 offset:34816\n\t" \
    "v_mfma_f32_16x16x32_bf16 v[44:47], v[188:191], v[92:95], v[44:47]\n\t" \
    "s_waitcnt lgkmcnt(6)\n\t" \
    "v_mfma_f32_16x16x32_bf16 v[48:51], v[184:187], v[96:99], v[48:51]\n\t" \
    "ds_read_b128 v[92:95], v8 offset:35840\n\t" \
    "v_mfma_f32_16x16x32_bf16 v[52:55], v[188:191], v[96:99], v[52:55]\n\t" \
    "global_load_dwordx4 v[148:151], %[off], s[62:63] offset:1024 sc1\n\t" \
    "s_waitcnt lgkmcnt(6)\n\t" \
    "v_mfma_f32_16x16x32_bf16 v[56:59], v[184:187], v[100:103], v[56:59]\n\t" \
    "ds_read_b128 v[96:99], v8 offset:36864\n\t" \
    "v_mfma_f32_16x16x32_bf16 v[60:63], v[188:191], v[100:103], v[60:63]\n\t" \
    "s_waitcnt lgkmcnt(6)\n\t" \
    "v_mfma_f32_16x16x32_bf16 v[64:67], v[184:187], v[104:107], v[64:67]\n\t" \
    "ds_read_b128 v[100:103], v8 offset:37888\n\t" \
    "v_mfma_f32_16x16x32_bf16 v[68:71], v[188:191], v[104:107], v[68:71]\n\t" \
    "s_waitcnt lgkmcnt(6)\n\t" \
    "v_mfma_f32_16x16x32_bf16 v[72:75], v[184:187], v[108:111], v[72:75]\n\t" \
    "ds_read_b128 v[104:107], v8 offset:38912\n\t" \
    "v_mfma_f32_16x16x32_bf16 v[76:79], v[188:191], v[108:111], v[76:79]\n\t" \
    "s_nop 0\n\t" \
    "ds_read_b128 v[108:111], v8 offset:39936\n\t" \
    "s_waitcnt lgkmcnt(7)\n\t" \
    "v_mfma_f32_16x16x32_bf16 v[16:19], v[192:195], v[80:83], v[16:19]\n\t" \
    "v_mfma_f32_16x16x32_bf16 v[20:23], v[196:199], v[80:83], v[20:23]\n\t" \
    "s_waitcnt lgkmcnt(6)\n\t" \
    "v_mfma_f32_16x16x32_bf16 v[24:27], v[192:195], v[84:87], v[24:27]\n\t" \
    "ds_read_b128 v[80:83], v8 offset:40960\n\t" \
    "v_mfma_f32_16x16x32_bf16 v[28:31], v[196:199], v[84:87], v[28:31]\n\t" \
    "global_load_dwordx4 v[152:155], %[off], s[62:63] offset:2048 sc1\n\t" \
    "s_waitcnt lgkmcnt(6)\n\t" \
    "v_mfma_f32_16x16x32_bf16 v[32:35], v[192:195], v[88:91], v[32:35]\n\t" \
    "ds_read_b128 v[84:87], v8 offset:41984\n\t" \
    "v_mfma_f32_16x16x32_bf16 v[36:39], v[196:199], v[88:91], v[36:39]\n\t" \
    "s_waitcnt lgkmcnt(6)\n\t" \
    "v_mfma_f32_16x16x32_bf16 v[40:43], v[192:195], v[92:95], v[40:43]\n\t" \
    "ds_read_b128 v[88:91], v8 offset:43008\n\t" \
    "v_mfma_f32_16x16x32_bf16 v[44:47], v[196:199], v[92:95], v[44:47]\n\t" \
    "s_waitcnt lgkmcnt(6)\n\t" \
    "v_mfma_f32_16x16x32_bf16 v[48:51], v[192:195], v[96:99], v[48:51]\n\t" \
    "ds_read_b128 v[92:95], v8 offset:44032\n\t" \
    "v_mfma_f32_16x16x32_bf16 v[52:55], v[196:199], v[96:99], v[52:55]\n\t" \
    "global_load_dwordx4 v[156:159], %[off], s[62:63] offset:3072 sc1\n\t" \
    "s_waitcnt lgkmcnt(6)\n\t" \
    "v_mfma_f32_16x16x32_bf16 v[56:59], v[192:195], v[100:103], v[56:59]\n\t" \
    "ds_read_b128 v[96:99], v8 offset:45056\n\t" \
    "v_mfma_f32_16x16x32_bf16 v[60:63], v[196:199], v[100:103], v[60:63]\n\t" \
    "s_waitcnt lgkmcnt(6)\n\t" \
    "v_mfma_f32_16x16x32_bf16 v[64:67], v[192:195], v[104:107], v[64:67]\n\t" \
    "ds_read_b128 v[100:103], v8 offset:46080\n\t" \
    "v_mfma_f32_16x16x32_bf16 v[68:71], v[196:199], v[104:107], v[68:71]\n\t" \
    "s_waitcnt lgkmcnt(6)\n\t" \
    "v_mfma_f32_16x16x32_bf16 v[72:75], v[192:195], v[108:111], v[72:75]\n\t" \
    "ds_read_b128 v[104:107], v8 offset:47104\n\t" \
    "v_mfma_f32_16x16x32_bf16 v[76:79], v[196:199], v[108:111], v[76:79]\n\t" \
    "s_nop 0\n\t" \
    "ds_read_b128 v[108:111], v8 offset:48128\n\t" \
    "s_waitcnt lgkmcnt(7)\n\t" \
    "v_mfma_f32_16x16x32_bf16 v[16:19], v[200:203], v[80:83], v[16:19]\n\t" \
    "v_mfma_f32_16x16x32_bf16 v[20:23], v[204:207], v[80:83], v[20:23]\n\t" \
    "s_waitcnt lgkmcnt(6)\n\t" \
    "v_mfma_f32_16x16x32_bf16 v[24:27], v[200:203], v[84:87], v[24:27]\n\t" \
    "ds_read_b128 v[80:83], v8 offset:49152\n\t" \
    "v_mfma_f32_16x16x32_bf16 v[28:31], v[204:207], v[84:87], v[28:31]\n\t" \
    "s_waitcnt lgkmcnt(6)\n\t" \
    "v_mfma_f32_16x16x32_bf16 v[32:35], v[200:203], v[88:91], v[32:35]\n\t" \
    "ds_read_b128 v[84:87], v8 offset:50176\n\t" \
    "v_mfma_f32_16x16x32_bf16 v[36:39], v[204:207], v[88:91], v[36:39]\n\t" \
    "global_load_dwordx4 v[160:163], %[off], s[60:61] offset:0 sc1\n\t" \
    "s_waitcnt lgkmcnt(6)\n\t" \
    "v_mfma_f32_16x16x32_bf16 v[40:43], v[200:203], v[92:95], v[40:43]\n\t" \
    "ds_read_b128 v[88:91], v8 offset:51200\n\t" \
    "v_mfma_f32_16x16x32_bf16 v[44:47], v[204:207], v[92:95], v[44:47]\n\t" \
    "s_waitcnt lgkmcnt(6)\n\t" \
    "v_mfma_f32_16x16x32_bf16 v[48:51], v[200:203], v[96:99], v[48:51]\n\t" \
    "ds_read_b128 v[92:95], v8 offset:52224\n\t" \
    "v_mfma_f32_16x16x32_bf16 v[52:55], v[204:207], v[96:99], v[52:55]\n\t" \
    "s_waitcnt lgkmcnt(6)\n\t" \
    "v_mfma_f32_16x16x32_bf16 v[56:59], v[200:203], v[100:103], v[56:59]\n\t" \
    "ds_read_b128 v[96:99], v8 offset:53248\n\t" \
    "v_mfma_f32_16x16x32_bf16 v[60:63], v[204:207], v[100:103], v[60:63]\n\t" \
    "global_load_dwordx4 v[164:167], %[off], s[60:61] offset:1024 sc1\n\t" \
    "s_waitcnt lgkmcnt(6)\n\t" \
    "v_mfma_f32_16x16x32_bf16 v[64:67], v[200:203], v[104:107], v[64:67]\n\t" \
    "ds_read_b128 v[100:103], v8 offset:54272\n\t" \
    "v_mfma_f32_16x16x32_bf16 v[68:71], v[204:207], v[104:107], v[68:71]\n\t" \
    "s_waitcnt lgkmcnt(6)\n\t" \
    "v_mfma_f32_16x16x32_bf16 v[72:75], v[200:203], v[108:111], v[72:75]\n\t" \
    "ds_read_b128 v[104:107], v8 offset:55296\n\t" \
    "v_mfma_f32_16x16x32_bf16 v[76:79], v[204:207], v[108:111], v[76:79]\n\t" \
    "s_nop 0\n\t" \
    "ds_read_b128 v[108:111], v8 offset:56320\n\t" \
    "s_waitcnt vmcnt(2)\n\t" \
    "CK1A_%=:\n\t" \
    "v_max3_u32 v6, v112, v113, v114\n\t" \
    "v_max3_u32 v6, v6, v115, v116\n\t" \
    "v_max3_u32 v6, v6, v117, v118\n\t" \
    "v_max3_u32 v6, v6, v119, v120\n\t" \
    "v_max3_u32 v6, v6, v121, v122\n\t" \
    "v_max3_u32 v6, v6, v123, v124\n\t" \
    "v_max3_u32 v6, v6, v125, v126\n\t" \
    "v_max3_u32 v6, v6, v127, v128\n\t" \
    "v_max3_u32 v6, v6, v129, v130\n\t" \
    "v_max3_u32 v6, v6, v131, v132\n\t" \
    "v_max3_u32 v6, v6, v133, v134\n\t" \
    "v_max3_u32 v6, v6, v135, v136\n\t" \
    "v_max3_u32 v6, v6, v137, v138\n\t" \
    "v_max3_u32 v6, v6, v139, v140\n\t" \
    "v_max3_u32 v6, v6, v141, v142\n\t" \
    "v_max3_u32 v6, v6, v143, v144\n\t" \
    "v_max3_u32 v6, v6, v145, v146\n\t" \
    "v_max3_u32 v6, v6, v147, v148\n\t" \
    "v_max3_u32 v6, v6, v149, v150\n\t" \
    "v_max3_u32 v6, v6, v151, v152\n\t" \
    "v_max3_u32 v6, v6, v153, v154\n\t" \
    "v_max3_u32 v6, v6, v155, v156\n\t" \
    "v_max3_u32 v6, v6, v157, v158\n\t" \
    "v_max_u32 v6, v6, v159\n\t" \
    "v_cmp_eq_u32 vcc, -1, v6\n\t" \
    "s_cbranch_vccz CK1B_%=\n\t" \
    "s_cmp_eq_u32 s70, 0\n\t" \
    "s_cbranch_scc1 CK1B_%=\n\t" \
    "s_sub_u32 s70, s70, 1\n\t" \
    "s_mov_b64 s[68:69], %[hb]\n\t" \
    "s_add_u32 s68, s68, 8192\n\t" \
    "s_addc_u32 s69, s69, 0\n\t" \
    "global_load_dwordx4 v[112:115], %[off], s[68:69] offset:0 sc1\n\t" \
    "global_load_dwordx4 v[116:119], %[off], s[68:69] offset:1024 sc1\n\t" \
    "global_load_dwordx4 v[120:123], %[off], s[68:69] offset:2048 sc1\n\t" \
    "global_load_dwordx4 v[124:127], %[off], s[68:69] offset:3072 sc1\n\t" \
    "s_add_u32 s68, s68, 4096\n\t" \
    "s_addc_u32 s69, s69, 0\n\t" \
    "global_load_dwordx4 v[128:131], %[off], s[68:69] offset:0 sc1\n\t" \
    "global_load_dwordx4 v[132:135], %[off], s[68:69] offset:1024 sc1\n\t" \
    "global_load_dwordx4 v[136:139], %[off], s[68:69] offset:2048 sc1\n\t" \
    "global_load_dwordx4 v[140:143], %[off], s[68:69] offset:3072 sc1\n\t" \
    "s_add_u32 s68, s68, 4096\n\t" \
    "s_addc_u32 s69, s69, 0\n\t" \
    "global_load_dwordx4 v[144:147], %[off], s[68:69] offset:0 sc1\n\t" \
    "global_load_dwordx4 v[148:151], %[off], s[68:69] offset:1024 sc1\n\t" \
    "global_load_dwordx4 v[152:155], %[off], s[68:69] offset:2048 sc1\n\t" \
    "global_load_dwordx4 v[156:159], %[off], s[68:69] offset:3072 sc1\n\t" \
    "s_waitcnt vmcnt(0)\n\t" \
    "s_branch CK1A_%=\n\t" \
    "CK1B_%=:\n\t" \
    "ds_write_b128 v12, v[112:115] offset:0\n\t" \
    "ds_write_b128 v12, v[116:119] offset:8192\n\t" \
    "ds_write_b128 v12, v[120:123] offset:16384\n\t" \
    "ds_write_b128 v12, v[124:127] offset:24576\n\t" \
    "ds_write_b128 v12, v[128:131] offset:32768\n\t" \
    "ds_write_b128 v12, v[132:135] offset:40960\n\t" \
    "ds_write_b128 v12, v[136:139] offset:49152\n\t" \
    "ds_write_b128 v12, v[140:143] offset:57344\n\t" \
    "ds_write_b128 v13, v[144:147] offset:0\n\t" \
    "ds_write_b128 v13, v[148:151] offset:8192\n\t" \
    "ds_write_b128 v13, v[152:155] offset:16384\n\t" \
    "ds_write_b128 v13, v[156:159] offset:24576\n\t" \
    "s_waitcnt lgkmcnt(15)\n\t" \
    "v_mfma_f32_16x16x32_bf16 v[16:19], v[208:211], v[80:83], v[16:19]\n\t" \
    "v_mfma_f32_16x16x32_bf16 v[20:23], v[212:215], v[80:83], v[20:23]\n\t" \
    "s_waitcnt lgkmcnt(15)\n\t" \
    "v_mfma_f32_16x16x32_bf16 v[24:27], v[208:211], v[84:87], v[24:27]\n\t" \
    "ds_read_b128 v[80:83], v8 offset:57344\n\t" \
    "v_mfma_f32_16x16x32_bf16 v[28:31], v[212:215], v[84:87], v[28:31]\n\t" \
    "s_waitcnt lgkmcnt(15)\n\t" \
    "v_mfma_f32_16x16x32_bf16 v[32:35], v[208:211], v[88:91], v[32:35]\n\t" \
    "ds_read_b128 v[84:87], v8 offset:58368\n\t" \
    "v_mfma_f32_16x16x32_bf16 v[36:39], v[212:215], v[88:91], v[36:39]\n\t" \
    "global_load_dwordx4 v[168:171], %[off], s[60:61] offset:2048 sc1\n\t" \
    "s_waitcnt lgkmcnt(15)\n\t" \
    "v_mfma_f32_16x16x32_bf16 v[40:43], v[208:211], v[92:95], v[40:43]\n\t" \
    "ds_read_b128 v[88:91], v8 offset:59392\n\t" \
    "v_mfma_f32_16x16x32_bf16 v[44:47], v[212:215], v[92:95], v[44:47]\n\t" \
    "s_waitcnt lgkmcnt(15)\n\t" \
    "v_mfma_f32_16x16x32_bf16 v[48:51], v[208:211], v[96:99], v[48:51]\n\t" \
    "ds_read_b128 v[92:95], v8 offset:60416\n\t" \
    "v_mfma_f32_16x16x32_bf16 v[52:55], v[212:215], v[96:99], v[52:55]\n\t" \
    "s_waitcnt lgkmcnt(15)\n\t" \
    "v_mfma_f32_16x16x32_bf16 v[56:59], v[208:211], v[100:103], v[56:59]\n\t" \
    "ds_read_b128 v[96:99], v8 offset:61440\n\t" \
    "v_mfma_f32_16x16x32_bf16 v[60:63], v[212:215], v[100:103], v[60:63]\n\t" \
    "global_load_dwordx4 v[172:175], %[off], s[60:61] offset:3072 sc1\n\t" \
    "s_add_u32 s60, s60, 4096\n\t" \
    "s_addc_u32 s61, s61, 0\n\t" \
    "s_waitcnt lgkmcnt(15)\n\t" \
    "v_mfma_f32_16x16x32_bf16 v[64:67], v[208:211], v[104:107], v[64:67]\n\t" \
    "ds_read_b128 v[100:103], v8 offset:62464\n\t" \
    "v_mfma_f32_16x16x32_bf16 v[68:71], v[212:215], v[104:107], v[68:71]\n\t" \
    "s_waitcnt lgkmcnt(15)\n\t" \
    "v_mfma_f32_16x16x32_bf16 v[72:75], v[208:211], v[108:111], v[72:75]\n\t" \
    "ds_read_b128 v[104:107], v8 offset:63488\n\t" \
    "v_mfma_f32_16x16x32_bf16 v[76:79], v[212:215], v[108:111], v[76:79]\n\t" \
    "s_nop 0\n\t" \
    "ds_read_b128 v[108:111], v8 offset:64512\n\t" \
    "s_barrier\n\t" \
    "s_waitcnt lgkmcnt(7)\n\t" \
    "v_mfma_f32_16x16x32_bf16 v[16:19], v[216:219], v[80:83], v[16:19]\n\t" \
    "v_mfma_f32_16x16x32_bf16 v[20:23], v[220:223], v[80:83], v[20:23]\n\t" \
    "s_waitcnt lgkmcnt(6)\n\t" \
    "v_mfma_f32_16x16x32_bf16 v[24:27], v[216:219], v[84:87], v[24:27]\n\t" \
    "ds_read_b128 v[80:83], v9 offset:0\n\t" \
    "v_mfma_f32_16x16x32_bf16 v[28:31], v[220:223], v[84:87], v[28:31]\n\t" \
    "s_waitcnt lgkmcnt(6)\n\t" \
    "v_mfma_f32_16x16x32_bf16 v[32:35], v[216:219], v[88:91], v[32:35]\n\t" \
    "ds_read_b128 v[84:87], v9 offset:1024\n\t" \
    "v_mfma_f32_16x16x32_bf16 v[36:39], v[220:223], v[88:91], v[36:39]\n\t" \
    "global_load_dwordx4 v[176:179], %[off], s[60:61] offset:0 sc1\n\t" \
    "s_waitcnt lgkmcnt(6)\n\t" \
    "v_mfma_f32_16x16x32_bf16 v[40:43], v[216:219], v[92:95], v[40:43]\n\t" \
    "ds_read_b128 v[88:91], v9 offset:2048\n\t" \
    "v_mfma_f32_16x16x32_bf16 v[44:47], v[220:223], v[92:95], v[44:47]\n\t" \
    "s_waitcnt lgkmcnt(6)\n\t" \
    "v_mfma_f32_16x16x32_bf16 v[48:51], v[216:219], v[96:99], v[48:51]\n\t" \
    "ds_read_b128 v[92:95], v9 offset:3072\n\t" \
    "v_mfma_f32_16x16x32_bf16 v[52:55], v[220:223], v[96:99], v[52:55]\n\t" \
    "s_waitcnt lgkmcnt(6)\n\t" \
    "v_mfma_f32_16x16x32_bf16 v[56:59], v[216:219], v[100:103], v[56:59]\n\t" \
    "ds_read_b128 v[96:99], v9 offset:4096\n\t" \
    "v_mfma_f32_16x16x32_bf16 v[60:63], v[220:223], v[100:103], v[60:63]\n\t" \
    "global_load_dwordx4 v[180:183], %[off], s[60:61] offset:1024 sc1\n\t" \
    "s_waitcnt lgkmcnt(6)\n\t" \
    "v_mfma_f32_16x16x32_bf16 v[64:67], v[216:219], v[104:107], v[64:67]\n\t" \
    "ds_read_b128 v[100:103], v9 offset:5120\n\t" \
    "v_mfma_f32_16x16x32_bf16 v[68:71], v[220:223], v[104:107], v[68:71]\n\t" \
    "s_waitcnt lgkmcnt(6)\n\t" \
    "v_mfma_f32_16x16x32_bf16 v[72:75], v[216:219], v[108:111], v[72:75]\n\t" \
    "ds_read_b128 v[104:107], v9 offset:6144\n\t" \
    "v_mfma_f32_16x16x32_bf16 v[76:79], v[220:223], v[108:111], v[76:79]\n\t" \
    "global_load_dwordx4 v[112:115], v14, %[sb] sc1\n\t" \
    "s_nop 0\n\t" \
    "ds_read_b128 v[108:111], v9 offset:7168\n\t" \
    "s_waitcnt lgkmcnt(7)\n\t" \
    "v_mfma_f32_16x16x32_bf16 v[16:19], v[224:227], v[80:83], v[16:19]\n\t" \
    "v_mfma_f32_16x16x32_bf16 v[20:23], v[228:231], v[80:83], v[20:23]\n\t" \
    "s_waitcnt lgkmcnt(6)\n\t" \
    "v_mfma_f32_16x16x32_bf16 v[24:27], v[224:227], v[84:87], v[24:27]\n\t" \
    "ds_read_b128 v[80:83], v9 offset:8192\n\t" \
    "v_mfma_f32_16x16x32_bf16 v[28:31], v[228:231], v[84:87], v[28:31]\n\t" \
    "s_waitcnt lgkmcnt(6)\n\t" \
    "v_mfma_f32_16x16x32_bf16 v[32:35], v[224:227], v[88:91], v[32:35]\n\t" \
    "ds_read_b128 v[84:87], v9 offset:9216\n\t" \
    "v_mfma_f32_16x16x32_bf16 v[36:39], v[228:231], v[88:91], v[36:39]\n\t" \
    "global_load_dwordx4 v[184:187], %[off], s[60:61] offset:2048 sc1\n\t" \
    "s_waitcnt lgkmcnt(6)\n\t" \
    "v_mfma_f32_16x16x32_bf16 v[40:43], v[224:227], v[92:95], v[40:43]\n\t" \
    "ds_read_b128 v[88:91], v9 offset:10240\n\t" \
    "v_mfma_f32_16x16x32_bf16 v[44:47], v[228:231], v[92:95], v[44:47]\n\t" \
    "s_waitcnt lgkmcnt(6)\n\t" \
    "v_mfma_f32_16x16x32_bf16 v[48:51], v[224:227], v[96:99], v[48:51]\n\t" \
    "ds_read_b128 v[92:95], v9 offset:11264\n\t" \
    "v_mfma_f32_16x16x32_bf16 v[52:55], v[228:231], v[96:99], v[52:55]\n\t" \
    "s_waitcnt lgkmcnt(6)\n\t" \
    "v_mfma_f32_16x16x32_bf16 v[56:59], v[224:227], v[100:103], v[56:59]\n\t" \
    "ds_read_b128 v[96:99], v9 offset:12288\n\t" \
    "v_mfma_f32_16x16x32_bf16 v[60:63], v[228:231], v[100:103], v[60:63]\n\t" \
    "global_load_dwordx4 v[188:191], %[off], s[60:61] offset:3072 sc1\n\t" \
    "s_add_u32 s60, s60, 4096\n\t" \
    "s_addc_u32 s61, s61, 0\n\t" \
    "s_waitcnt lgkmcnt(6)\n\t" \
    "v_mfma_f32_16x16x32_bf16 v[64:67], v[224:227], v[104:107], v[64:67]\n\t" \
    "ds_read_b128 v[100:103], v9 offset:13312\n\t" \
    "v_mfma_f32_16x16x32_bf16 v[68:71], v[228:231], v[104:107], v[68:71]\n\t" \
    "s_waitcnt lgkmcnt(6)\n\t" \
    "v_mfma_f32_16x16x32_bf16 v[72:75], v[224:227], v[108:111], v[72:75]\n\t" \
    "ds_read_b128 v[104:107], v9 offset:14336\n\t" \
    "v_mfma_f32_16x16x32_bf16 v[76:79], v[228:231], v[108:111], v[76:79]\n\t" \
    "global_load_dwordx4 v[116:119], v14, %[cb] sc1\n\t" \
    "s_nop 0\n\t" \
    "ds_read_b128 v[108:111], v9 offset:15360\n\t" \
    "s_waitcnt lgkmcnt(7)\n\t" \
    "v_mfma_f32_16x16x32_bf16 v[16:19], v[232:235], v[80:83], v[16:19]\n\t" \
    "v_mfma_f32_16x16x32_bf16 v[20:23], v[236:239], v[80:83], v[20:23]\n\t" \
    "s_waitcnt lgkmcnt(6)\n\t" \
    "v_mfma_f32_16x16x32_bf16 v[24:27], v[232:235], v[84:87], v[24:27]\n\t" \
    "ds_read_b128 v[80:83], v9 offset:16384\n\t" \
    "v_mfma_f32_16x16x32_bf16 v[28:31], v[236:239], v[84:87], v[28:31]\n\t" \
    "s_waitcnt lgkmcnt(6)\n\t" \
    "v_mfma_f32_16x16x32_bf16 v[32:35], v[232:235], v[88:91], v[32:35]\n\t" \
    "ds_read_b128 v[84:87], v9 offset:17408\n\t" \
    "v_mfma_f32_16x16x32_bf16 v[36:39], v[236:239], v[88:91], v[36:39]\n\t" \
    "global_load_dwordx4 v[192:195], %[off], s[60:61] offset:0 sc1\n\t" \
    "s_waitcnt lgkmcnt(6)\n\t" \
    "v_mfma_f32_16x16x32_bf16 v[40:43], v[232:235], v[92:95], v[40:43]\n\t" \
    "ds_read_b128 v[88:91], v9 offset:18432\n\t" \
    "v_mfma_f32_16x16x32_bf16 v[44:47], v[236:239], v[92:95], v[44:47]\n\t" \
    "s_waitcnt lgkmcnt(6)\n\t" \
    "v_mfma_f32_16x16x32_bf16 v[48:51], v[232:235], v[96:99], v[48:51]\n\t" \
    "ds_read_b128 v[92:95], v9 offset:19456\n\t" \
    "v_mfma_f32_16x16x32_bf16 v[52:55], v[236:239], v[96:99], v[52:55]\n\t" \
    "s_waitcnt lgkmcnt(6)\n\t" \
    "v_mfma_f32_16x16x32_bf16 v[56:59], v[232:235], v[100:103], v[56:59]\n\t" \
    "ds_read_b128 v[96:99], v9 offset:20480\n\t" \
    "v_mfma_f32_16x16x32_bf16 v[60:63], v[236:239], v[100:103], v[60:63]\n\t" \
    "global_load_dwordx4 v[196:199], %[off], s[60:61] offset:1024 sc1\n\t" \
    "s_waitcnt lgkmcnt(6)\n\t" \
    "v_mfma_f32_16x16x32_bf16 v[64:67], v[232:235], v[104:107], v[64:67]\n\t" \
    "ds_read_b128 v[100:103], v9 offset:21504\n\t" \
    "v_mfma_f32_16x16x32_bf16 v[68:71], v[236:239], v[104:107], v[68:71]\n\t" \
    "s_waitcnt lgkmcnt(6)\n\t" \
    "v_mfma_f32_16x16x32_bf16 v[72:75], v[232:235], v[108:111], v[72:75]\n\t" \
    "ds_read_b128 v[104:107], v9 offset:22528\n\t" \
    "v_mfma_f32_16x16x32_bf16 v[76:79], v[236:239], v[108:111], v[76:79]\n\t" \
    "global_load_dwordx4 v[120:123], v14, %[cb] offset:1024 sc1\n\t" \
    "s_nop 0\n\t" \
    "ds_read_b128 v[108:111], v9 offset:23552\n\t" \
    "s_waitcnt lgkmcnt(7)\n\t" \
    "v_mfma_f32_16x16x32_bf16 v[16:19], v[240:243], v[80:83], v[16:19]\n\t" \
    "v_mfma_f32_16x16x32_bf16 v[20:23], v[244:247], v[80:83], v[20:23]\n\t" \
    "s_waitcnt lgkmcnt(6)\n\t" \
    "v_mfma_f32_16x16x32_bf16 v[24:27], v[240:243], v[84:87], v[24:27]\n\t" \
    "ds_read_b128 v[80:83], v9 offset:24576\n\t" \
    "v_mfma_f32_16x16x32_bf16 v[28:31], v[244:247], v[84:87], v[28:31]\n\t" \
    "s_waitcnt lgkmcnt(6)\n\t" \
    "v_mfma_f32_16x16x32_bf16 v[32:35], v[240:243], v[88:91], v[32:35]\n\t" \
    "ds_read_b128 v[84:87], v9 offset:25600\n\t" \
    "v_mfma_f32_16x16x32_bf16 v[36:39], v[244:247], v[88:91], v[36:39]\n\t" \
    "global_load_dwordx4 v[200:203], %[off], s[60:61] offset:2048 sc1\n\t" \
    "s_waitcnt lgkmcnt(6)\n\t" \
    "v_mfma_f32_16x16x32_bf16 v[40:43], v[240:243], v[92:95], v[40:43]\n\t" \
    "ds_read_b128 v[88:91], v9 offset:26624\n\t" \
    "v_mfma_f32_16x16x32_bf16 v[44:47], v[244:247], v[92:95], v[44:47]\n\t" \
    "s_waitcnt lgkmcnt(6)\n\t" \
    "v_mfma_f32_16x16x32_bf16 v[48:51], v[240:243], v[96:99], v[48:51]\n\t" \
    "ds_read_b128 v[92:95], v9 offset:27648\n\t" \
    "v_mfma_f32_16x16x32_bf16 v[52:55], v[244:247], v[96:99], v[52:55]\n\t" \
    "s_waitcnt lgkmcnt(6)\n\t" \
    "v_mfma_f32_16x16x32_bf16 v[56:59], v[240:243], v[100:103], v[56:59]\n\t" \
    "ds_read_b128 v[96:99], v9 offset:28672\n\t" \
    "v_mfma_f32_16x16x32_bf16 v[60:63], v[244:247], v[100:103], v[60:63]\n\t" \
    "global_load_dwordx4 v[204:207], %[off], s[60:61] offset:3072 sc1\n\t" \
    "s_add_u32 s60, s60, 4096\n\t" \
    "s_addc_u32 s61, s61, 0\n\t" \
    "s_waitcnt lgkmcnt(6)\n\t" \
    "v_mfma_f32_16x16x32_bf16 v[64:67], v[240:243], v[104:107], v[64:67]\n\t" \
    "ds_read_b128 v[100:103], v9 offset:29696\n\t" \
    "v_mfma_f32_16x16x32_bf16 v[68:71], v[244:247], v[104:107], v[68:71]\n\t" \
    "s_waitcnt lgkmcnt(6)\n\t" \
    "v_mfma_f32_16x16x32_bf16 v[72:75], v[240:243], v[108:111], v[72:75]\n\t" \
    "ds_read_b128 v[104:107], v9 offset:30720\n\t" \
    "v_mfma_f32_16x16x32_bf16 v[76:79], v[244:247], v[108:111], v[76:79]\n\t" \
    "global_load_dwordx4 v[124:127], v14, %[cb] offset:1280 sc1\n\t" \
    "s_nop 0\n\t" \
    "ds_read_b128 v[108:111], v9 offset:31744\n\t" \
    "s_waitcnt lgkmcnt(7)\n\t" \
    "v_mfma_f32_16x16x32_bf16 v[16:19], v[248:251], v[80:83], v[16:19]\n\t" \
    "v_mfma_f32_16x16x32_bf16 v[20:23], v[252:255], v[80:83], v[20:23]\n\t" \
    "s_waitcnt lgkmcnt(6)\n\t" \
    "v_mfma_f32_16x16x32_bf16 v[24:27], v[248:251], v[84:87], v[24:27]\n\t" \
    "ds_read_b128 v[80:83], v9 offset:32768\n\t" \
    "v_mfma_f32_16x16x32_bf16 v[28:31], v[252:255], v[84:87], v[28:31]\n\t" \
    "global_load_dwordx4 v[128:131], %[off], s[64:65] offset:0 sc1\n\t" \
    "s_waitcnt lgkmcnt(6)\n\t" \
    "v_mfma_f32_16x16x32_bf16 v[32:35], v[248:251], v[88:91], v[32:35]\n\t" \
    "ds_read_b128 v[84:87], v9 offset:33792\n\t" \
    "v_mfma_f32_16x16x32_bf16 v[36:39], v[252:255], v[88:91], v[36:39]\n\t" \
    "global_load_dwordx4 v[208:211], %[off], s[60:61] offset:0 sc1\n\t" \
    "s_waitcnt lgkmcnt(6)\n\t" \
    "v_mfma_f32_16x16x32_bf16 v[40:43], v[248:251], v[92:95], v[40:43]\n\t" \
    "ds_read_b128 v[88:91], v9 offset:34816\n\t" \
    "v_mfma_f32_16x16x32_bf16 v[44:47], v[252:255], v[92:95], v[44:47]\n\t" \
    "s_waitcnt lgkmcnt(6)\n\t" \
    "v_mfma_f32_16x16x32_bf16 v[48:51], v[248:251], v[96:99], v[48:51]\n\t" \
    "ds_read_b128 v[92:95], v9 offset:35840\n\t" \
    "v_mfma_f32_16x16x32_bf16 v[52:55], v[252:255], v[96:99], v[52:55]\n\t" \
    "s_waitcnt lgkmcnt(6)\n\t" \
    "v_mfma_f32_16x16x32_bf16 v[56:59], v[248:251], v[100:103], v[56:59]\n\t" \
    "ds_read_b128 v[96:99], v9 offset:36864\n\t" \
    "v_mfma_f32_16x16x32_bf16 v[60:63], v[252:255], v[100:103], v[60:63]\n\t" \
    "global_load_dwordx4 v[212:215], %[off], s[60:61] offset:1024 sc1\n\t" \
    "s_waitcnt lgkmcnt(6)\n\t" \
    "v_mfma_f32_16x16x32_bf16 v[64:67], v[248:251], v[104:107], v[64:67]\n\t" \
    "ds_read_b128 v[100:103], v9 offset:37888\n\t" \
    "v_mfma_f32_16x16x32_bf16 v[68:71], v[252:255], v[104:107], v[68:71]\n\t" \
    "s_waitcnt lgkmcnt(6)\n\t" \
    "v_mfma_f32_16x16x32_bf16 v[72:75], v[248:251], v[108:111], v[72:75]\n\t" \
    "ds_read_b128 v[104:107], v9 offset:38912\n\t" \
    "v_mfma_f32_16x16x32_bf16 v[76:79], v[252:255], v[108:111], v[76:79]\n\t" \
    "s_nop 0\n\t" \
    "ds_read_b128 v[108:111], v9 offset:39936\n\t" \
    "s_waitcnt vmcnt(17)\n\t" \
    "s_waitcnt lgkmcnt(7)\n\t" \
    "v_mfma_f32_16x16x32_bf16 v[16:19], v[160:163], v[80:83], v[16:19]\n\t" \
    "v_mfma_f32_16x16x32_bf16 v[20:23], v[164:167], v[80:83], v[20:23]\n\t" \
    "s_waitcnt lgkmcnt(6)\n\t" \
    "v_mfma_f32_16x16x32_bf16 v[24:27], v[160:163], v[84:87], v[24:27]\n\t" \
    "ds_read_b128 v[80:83], v9 offset:40960\n\t" \
    "v_mfma_f32_16x16x32_bf16 v[28:31], v[164:167], v[84:87], v[28:31]\n\t" \
    "global_load_dwordx4 v[132:135], %[off], s[64:65] offset:1024 sc1\n\t" \
    "s_waitcnt lgkmcnt(6)\n\t" \
    "v_mfma_f32_16x16x32_bf16 v[32:35], v[160:163], v[88:91], v[32:35]\n\t" \
    "ds_read_b128 v[84:87], v9 offset:41984\n\t" \
    "v_mfma_f32_16x16x32_bf16 v[36:39], v[164:167], v[88:91], v[36:39]\n\t" \
    "global_load_dwordx4 v[216:219], %[off], s[60:61] offset:2048 sc1\n\t" \
    "s_waitcnt lgkmcnt(6)\n\t" \
    "v_mfma_f32_16x16x32_bf16 v[40:43], v[160:163], v[92:95], v[40:43]\n\t" \
    "ds_read_b128 v[88:91], v9 offset:43008\n\t" \
    "v_mfma_f32_16x16x32_bf16 v[44:47], v[164:167], v[92:95], v[44:47]\n\t" \
    "s_waitcnt lgkmcnt(6)\n\t" \
    "v_mfma_f32_16x16x32_bf16 v[48:51], v[160:163], v[96:99], v[48:51]\n\t" \
    "ds_read_b128 v[92:95], v9 offset:44032\n\t" \
    "v_mfma_f32_16x16x32_bf16 v[52:55], v[164:167], v[96:99], v[52:55]\n\t" \
    "s_waitcnt lgkmcnt(6)\n\t" \
    "v_mfma_f32_16x16x32_bf16 v[56:59], v[160:163], v[100:103], v[56:59]\n\t" \
    "ds_read_b128 v[96:99], v9 offset:45056\n\t" \
    "v_mfma_f32_16x16x32_bf16 v[60:63], v[164:167], v[100:103], v[60:63]\n\t" \
    "global_load_dwordx4 v[220:223], %[off], s[60:61] offset:3072 sc1\n\t" \
    "s_add_u32 s60, s60, 4096\n\t" \
    "s_addc_u32 s61, s61, 0\n\t" \
    "s_waitcnt lgkmcnt(6)\n\t" \
    "v_mfma_f32_16x16x32_bf16 v[64:67], v[160:163], v[104:107], v[64:67]\n\t" \
    "ds_read_b128 v[100:103], v9 offset:46080\n\t" \
    "v_mfma_f32_16x16x32_bf16 v[68:71], v[164:167], v[104:107], v[68:71]\n\t" \
    "s_waitcnt lgkmcnt(6)\n\t" \
    "v_mfma_f32_16x16x32_bf16 v[72:75], v[160:163], v[108:111], v[72:75]\n\t" \
    "ds_read_b128 v[104:107], v9 offset:47104\n\t" \
    "v_mfma_f32_16x16x32_bf16 v[76:79], v[164:167], v[108:111], v[76:79]\n\t" \
    "s_nop 0\n\t" \
    "ds_read_b128 v[108:111], v9 offset:48128\n\t" \
    "s_waitcnt vmcnt(18)\n\t" \
    "s_waitcnt lgkmcnt(7)\n\t" \
    "v_mfma_f32_16x16x32_bf16 v[16:19], v[168:171], v[80:83], v[16:19]\n\t" \
    "v_mfma_f32_16x16x32_bf16 v[20:23], v[172:175], v[80:83], v[20:23]\n\t" \
    "s_waitcnt lgkmcnt(6)\n\t" \
    "v_mfma_f32_16x16x32_bf16 v[24:27], v[168:171], v[84:87], v[24:27]\n\t" \
    "ds_read_b128 v[80:83], v9 offset:49152\n\t" \
    "v_mfma_f32_16x16x32_bf16 v[28:31], v[172:175], v[84:87], v[28:31]\n\t" \
    "global_load_dwordx4 v[224:227], v14, %[cb] offset:2304 sc1\n\t" \
    "s_waitcnt lgkmcnt(6)\n\t" \
    "v_mfma_f32_16x16x32_bf16 v[32:35], v[168:171], v[88:91], v[32:35]\n\t" \
    "ds_read_b128 v[84:87], v9 offset:50176\n\t" \
    "v_mfma_f32_16x16x32_bf16 v[36:39], v[172:175], v[88:91], v[36:39]\n\t" \
    "s_waitcnt lgkmcnt(6)\n\t" \
    "v_mfma_f32_16x16x32_bf16 v[40:43], v[168:171], v[92:95], v[40:43]\n\t" \
    "ds_read_b128 v[88:91], v9 offset:51200\n\t" \
    "v_mfma_f32_16x16x32_bf16 v[44:47], v[172:175], v[92:95], v[44:47]\n\t" \
    "s_waitcnt lgkmcnt(6)\n\t" \
    "v_mfma_f32_16x16x32_bf16 v[48:51], v[168:171], v[96:99], v[48:51]\n\t" \
    "ds_read_b128 v[92:95], v9 offset:52224\n\t" \
    "v_mfma_f32_16x16x32_bf16 v[52:55], v[172:175], v[96:99], v[52:55]\n\t" \
    "global_load_dwordx4 v[136:139], %[off], s[64:65] offset:2048 sc1\n\t" \
    "s_waitcnt lgkmcnt(6)\n\t" \
    "v_mfma_f32_16x16x32_bf16 v[56:59], v[168:171], v[100:103], v[56:59]\n\t" \
    "ds_read_b128 v[96:99], v9 offset:53248\n\t" \
    "v_mfma_f32_16x16x32_bf16 v[60:63], v[172:175], v[100:103], v[60:63]\n\t" \
    "s_waitcnt lgkmcnt(6)\n\t" \
    "v_mfma_f32_16x16x32_bf16 v[64:67], v[168:171], v[104:107], v[64:67]\n\t" \
    "ds_read_b128 v[100:103], v9 offset:54272\n\t" \
    "v_mfma_f32_16x16x32_bf16 v[68:71], v[172:175], v[104:107], v[68:71]\n\t" \
    "s_waitcnt lgkmcnt(6)\n\t" \
    "v_mfma_f32_16x16x32_bf16 v[72:75], v[168:171], v[108:111], v[72:75]\n\t" \
    "ds_read_b128 v[104:107], v9 offset:55296\n\t" \
    "v_mfma_f32_16x16x32_bf16 v[76:79], v[172:175], v[108:111], v[76:79]\n\t" \
    "s_add_u32 s68, s64, 20480\n\t" \
    "s_addc_u32 s69, s65, 0\n\t" \
    "global_load_dwordx4 v[160:163], %[off], s[68:69] offset:0 sc1\n\t" \
    "s_nop 0\n\t" \
    "ds_read_b128 v[108:111], v9 offset:56320\n\t" \
    "s_waitcnt vmcnt(19)\n\t" \
    "s_waitcnt lgkmcnt(7)\n\t" \
    "v_mfma_f32_16x16x32_bf16 v[16:19], v[176:179], v[80:83], v[16:19]\n\t" \
    "v_mfma_f32_16x16x32_bf16 v[20:23], v[180:183], v[80:83], v[20:23]\n\t" \
    "s_waitcnt lgkmcnt(6)\n\t" \
    "v_mfma_f32_16x16x32_bf16 v[24:27], v[176:179], v[84:87], v[24:27]\n\t" \
    "ds_read_b128 v[80:83], v9 offset:57344\n\t" \
    "v_mfma_f32_16x16x32_bf16 v[28:31], v[180:183], v[84:87], v[28:31]\n\t" \
    "global_load_dwordx4 v[140:143], %[off], s[64:65] offset:3072 sc1\n\t" \
    "s_waitcnt lgkmcnt(6)\n\t" \
    "v_mfma_f32_16x16x32_bf16 v[32:35], v[176:179], v[88:91], v[32:35]\n\t" \
    "ds_read_b128 v[84:87], v9 offset:58368\n\t" \
    "v_mfma_f32_16x16x32_bf16 v[36:39], v[180:183], v[88:91], v[36:39]\n\t" \
    "s_waitcnt lgkmcnt(6)\n\t" \
    "v_mfma_f32_16x16x32_bf16 v[40:43], v[176:179], v[92:95], v[40:43]\n\t" \
    "ds_read_b128 v[88:91], v9 offset:59392\n\t" \
    "v_mfma_f32_16x16x32_bf16 v[44:47], v[180:183], v[92:95], v[44:47]\n\t" \
    "s_waitcnt lgkmcnt(6)\n\t" \
    "v_mfma_f32_16x16x32_bf16 v[48:51], v[176:179], v[96:99], v[48:51]\n\t" \
    "ds_read_b128 v[92:95], v9 offset:60416\n\t" \
    "v_mfma_f32_16x16x32_bf16 v[52:55], v[180:183], v[96:99], v[52:55]\n\t" \
    "global_load_dwordx4 v[164:167], %[off], s[68:69] offset:1024 sc1\n\t" \
    "s_waitcnt lgkmcnt(6)\n\t" \
    "v_mfma_f32_16x16x32_bf16 v[56:59], v[176:179], v[100:103], v[56:59]\n\t" \
    "ds_read_b128 v[96:99], v9 offset:61440\n\t" \
    "v_mfma_f32_16x16x32_bf16 v[60:63], v[180:183], v[100:103], v[60:63]\n\t" \
    "s_waitcnt lgkmcnt(6)\n\t" \
    "v_mfma_f32_16x16x32_bf16 v[64:67], v[176:179], v[104:107], v[64:67]\n\t" \
    "ds_read_b128 v[100:103], v9 offset:62464\n\t" \
    "v_mfma_f32_16x16x32_bf16 v[68:71], v[180:183], v[104:107], v[68:71]\n\t" \
    "s_waitcnt lgkmcnt(6)\n\t" \
    "v_mfma_f32_16x16x32_bf16 v[72:75], v[176:179], v[108:111], v[72:75]\n\t" \
    "ds_read_b128 v[104:107], v9 offset:63488\n\t" \
    "v_mfma_f32_16x16x32_bf16 v[76:79], v[180:183], v[108:111], v[76:79]\n\t" \
    "global_load_dwordx4 v[168:171], %[off], s[68:69] offset:2048 sc1\n\t" \
    "s_nop 0\n\t" \
    "ds_read_b128 v[108:111], v9 offset:64512\n\t" \
    "s_waitcnt vmcnt(19)\n\t" \
    "s_waitcnt lgkmcnt(7)\n\t" \
    "v_mfma_f32_16x16x32_bf16 v[16:19], v[184:187], v[80:83], v[16:19]\n\t" \
    "v_mfma_f32_16x16x32_bf16 v[20:23], v[188:191], v[80:83], v[20:23]\n\t" \
    "s_waitcnt lgkmcnt(6)\n\t" \
    "v_mfma_f32_16x16x32_bf16 v[24:27], v[184:187], v[84:87], v[24:27]\n\t" \
    "ds_read_b128 v[80:83], v10 offset:0\n\t" \
    "v_mfma_f32_16x16x32_bf16 v[28:31], v[188:191], v[84:87], v[28:31]\n\t" \
    "global_load_dwordx4 v[144:147], v15, s[64:65] sc1\n\t" \
    "s_waitcnt lgkmcnt(6)\n\t" \
    "v_mfma_f32_16x16x32_bf16 v[32:35], v[184:187], v[88:91], v[32:35]\n\t" \
    "ds_read_b128 v[84:87], v10 offset:1024\n\t" \
    "v_mfma_f32_16x16x32_bf16 v[36:39], v[188:191], v[88:91], v[36:39]\n\t" \
    "s_waitcnt lgkmcnt(6)\n\t" \
    "v_mfma_f32_16x16x32_bf16 v[40:43], v[184:187], v[92:95], v[40:43]\n\t" \
    "ds_read_b128 v[88:91], v10 offset:2048\n\t" \
    "v_mfma_f32_16x16x32_bf16 v[44:47], v[188:191], v[92:95], v[44:47]\n\t" \
    "s_waitcnt lgkmcnt(6)\n\t" \
    "v_mfma_f32_16x16x32_bf16 v[48:51], v[184:187], v[96:99], v[48:51]\n\t" \
    "ds_read_b128 v[92:95], v10 offset:3072\n\t" \
    "v_mfma_f32_16x16x32_bf16 v[52:55], v[188:191], v[96:99], v[52:55]\n\t" \
    "global_load_dwordx4 v[172:175], %[off], s[68:69] offset:3072 sc1\n\t" \
    "s_waitcnt lgkmcnt(6)\n\t" \
    "v_mfma_f32_16x16x32_bf16 v[56:59], v[184:187], v[100:103], v[56:59]\n\t" \
    "ds_read_b128 v[96:99], v10 offset:4096\n\t" \
    "v_mfma_f32_16x16x32_bf16 v[60:63], v[188:191], v[100:103], v[60:63]\n\t" \
    "s_waitcnt lgkmcnt(6)\n\t" \
    "v_mfma_f32_16x16x32_bf16 v[64:67], v[184:187], v[104:107], v[64:67]\n\t" \
    "ds_read_b128 v[100:103], v10 offset:5120\n\t" \
    "v_mfma_f32_16x16x32_bf16 v[68:71], v[188:191], v[104:107], v[68:71]\n\t" \
    "s_waitcnt lgkmcnt(6)\n\t" \
    "v_mfma_f32_16x16x32_bf16 v[72:75], v[184:187], v[108:111], v[72:75]\n\t" \
    "ds_read_b128 v[104:107], v10 offset:6144\n\t" \
    "v_mfma_f32_16x16x32_bf16 v[76:79], v[188:191], v[108:111], v[76:79]\n\t" \
    "global_load_dwordx4 v[228:231], %[off], s[66:67] offset:0 sc1\n\t" \
    "s_nop 0\n\t" \
    "ds_read_b128 v[108:111], v10 offset:7168\n\t" \
    "s_waitcnt vmcnt(19)\n\t" \
    "s_waitcnt lgkmcnt(7)\n\t" \
    "v_mfma_f32_16x16x32_bf16 v[16:19], v[192:195], v[80:83], v[16:19]\n\t" \
    "v_mfma_f32_16x16x32_bf16 v[20:23], v[196:199], v[80:83], v[20:23]\n\t" \
    "s_waitcnt lgkmcnt(6)\n\t" \
    "v_mfma_f32_16x16x32_bf16 v[24:27], v[192:195], v[84:87], v[24:27]\n\t" \
    "ds_read_b128 v[80:83], v10 offset:8192\n\t" \
    "v_mfma_f32_16x16x32_bf16 v[28:31], v[196:199], v[84:87], v[28:31]\n\t" \
    "global_load_dwordx4 v[232:235], %[off], s[66:67] offset:1024 sc1\n\t" \
    "s_waitcnt lgkmcnt(6)\n\t" \
    "v_mfma_f32_16x16x32_bf16 v[32:35], v[192:195], v[88:91], v[32:35]\n\t" \
    "ds_read_b128 v[84:87], v10 offset:9216\n\t" \
    "v_mfma_f32_16x16x32_bf16 v[36:39], v[196:199], v[88:91], v[36:39]\n\t" \
    "s_waitcnt lgkmcnt(6)\n\t" \
    "v_mfma_f32_16x16x32_bf16 v[40:43], v[192:195], v[92:95], v[40:43]\n\t" \
    "ds_read_b128 v[88:91], v10 offset:10240\n\t" \
    "v_mfma_f32_16x16x32_bf16 v[44:47], v[196:199], v[92:95], v[44:47]\n\t" \
    "s_waitcnt lgkmcnt(6)\n\t" \
    "v_mfma_f32_16x16x32_bf16 v[48:51], v[192:195], v[96:99], v[48:51]\n\t" \
    "ds_read_b128 v[92:95], v10 offset:11264\n\t" \
    "v_mfma_f32_16x16x32_bf16 v[52:55], v[196:199], v[96:99], v[52:55]\n\t" \
    "global_load_dwordx4 v[176:179], v15, s[68:69] sc1\n\t" \
    "s_waitcnt lgkmcnt(6)\n\t" \
    "v_mfma_f32_16x16x32_bf16 v[56:59], v[192:195], v[100:103], v[56:59]\n\t" \
    "ds_read_b128 v[96:99], v10 offset:12288\n\t" \
    "v_mfma_f32_16x16x32_bf16 v[60:63], v[196:199], v[100:103], v[60:63]\n\t" \
    "s_waitcnt lgkmcnt(6)\n\t" \
    "v_mfma_f32_16x16x32_bf16 v[64:67], v[192:195], v[104:107], v[64:67]\n\t" \
    "ds_read_b128 v[100:103], v10 offset:13312\n\t" \
    "v_mfma_f32_16x16x32_bf16 v[68:71], v[196:199], v[104:107], v[68:71]\n\t" \
    "s_waitcnt lgkmcnt(6)\n\t" \
    "v_mfma_f32_16x16x32_bf16 v[72:75], v[192:195], v[108:111], v[72:75]\n\t" \
    "ds_read_b128 v[104:107], v10 offset:14336\n\t" \
    "v_mfma_f32_16x16x32_bf16 v[76:79], v[196:199], v[108:111], v[76:79]\n\t" \
    "s_add_u32 s68, s64, 30720\n\t" \
    "s_addc_u32 s69, s65, 0\n\t" \
    "global_load_dwordx4 v[180:183], %[off], s[68:69] offset:0 sc1\n\t" \
    "s_nop 0\n\t" \
    "ds_read_b128 v[108:111], v10 offset:15360\n\t" \
    "s_waitcnt vmcnt(19)\n\t" \
    "s_waitcnt lgkmcnt(7)\n\t" \
    "v_mfma_f32_16x16x32_bf16 v[16:19], v[200:203], v[80:83], v[16:19]\n\t" \
    "v_mfma_f32_16x16x32_bf16 v[20:23], v[204:207], v[80:83], v[20:23]\n\t" \
    "s_waitcnt lgkmcnt(6)\n\t" \
    "v_mfma_f32_16x16x32_bf16 v[24:27], v[200:203], v[84:87], v[24:27]\n\t" \
    "ds_read_b128 v[80:83], v10 offset:16384\n\t" \
    "v_mfma_f32_16x16x32_bf16 v[28:31], v[204:207], v[84:87], v[28:31]\n\t" \
    "global_load_dwordx4 v[236:239], %[off], s[66:67] offset:2048 sc1\n\t" \
    "s_waitcnt lgkmcnt(6)\n\t" \
    "v_mfma_f32_16x16x32_bf16 v[32:35], v[200:203], v[88:91], v[32:35]\n\t" \
    "ds_read_b128 v[84:87], v10 offset:17408\n\t" \
    "v_mfma_f32_16x16x32_bf16 v[36:39], v[204:207], v[88:91], v[36:39]\n\t" \
    "s_waitcnt lgkmcnt(6)\n\t" \
    "v_mfma_f32_16x16x32_bf16 v[40:43], v[200:203], v[92:95], v[40:43]\n\t" \
    "ds_read_b128 v[88:91], v10 offset:18432\n\t" \
    "v_mfma_f32_16x16x32_bf16 v[44:47], v[204:207], v[92:95], v[44:47]\n\t" \
    "s_waitcnt lgkmcnt(6)\n\t" \
    "v_mfma_f32_16x16x32_bf16 v[48:51], v[200:203], v[96:99], v[48:51]\n\t" \
    "ds_read_b128 v[92:95], v10 offset:19456\n\t" \
    "v_mfma_f32_16x16x32_bf16 v[52:55], v[204:207], v[96:99], v[52:55]\n\t" \
    "global_load_dwordx4 v[184:187], %[off], s[68:69] offset:1024 sc1\n\t" \
    "s_waitcnt lgkmcnt(6)\n\t" \
    "v_mfma_f32_16x16x32_bf16 v[56:59], v[200:203], v[100:103], v[56:59]\n\t" \
    "ds_read_b128 v[96:99], v10 offset:20480\n\t" \
    "v_mfma_f32_16x16x32_bf16 v[60:63], v[204:207], v[100:103], v[60:63]\n\t" \
    "s_waitcnt lgkmcnt(6)\n\t" \
    "v_mfma_f32_16x16x32_bf16 v[64:67], v[200:203], v[104:107], v[64:67]\n\t" \
    "ds_read_b128 v[100:103], v10 offset:21504\n\t" \
    "v_mfma_f32_16x16x32_bf16 v[68:71], v[204:207], v[104:107], v[68:71]\n\t" \
    "s_waitcnt lgkmcnt(6)\n\t" \
    "v_mfma_f32_16x16x32_bf16 v[72:75], v[200:203], v[108:111], v[72:75]\n\t" \
    "ds_read_b128 v[104:107], v10 offset:22528\n\t" \
    "v_mfma_f32_16x16x32_bf16 v[76:79], v[204:207], v[108:111], v[76:79]\n\t" \
    "global_load_dwordx4 v[188:191], %[off], s[68:69] offset:2048 sc1\n\t" \
    "s_nop 0\n\t" \
    "ds_read_b128 v[108:111], v10 offset:23552\n\t" \
    "s_waitcnt vmcnt(18)\n\t" \
    "s_waitcnt lgkmcnt(7)\n\t" \
    "v_mfma_f32_16x16x32_bf16 v[16:19], v[208:211], v[80:83], v[16:19]\n\t" \
    "v_mfma_f32_16x16x32_bf16 v[20:23], v[212:215], v[80:83], v[20:23]\n\t" \
    "s_waitcnt lgkmcnt(6)\n\t" \
    "v_mfma_f32_16x16x32_bf16 v[24:27], v[208:211], v[84:87], v[24:27]\n\t" \
    "ds_read_b128 v[80:83], v10 offset:24576\n\t" \
    "v_mfma_f32_16x16x32_bf16 v[28:31], v[212:215], v[84:87], v[28:31]\n\t" \
    "global_load_dwordx4 v[240:243], %[off], s[66:67] offset:3072 sc1\n\t" \
    "s_waitcnt lgkmcnt(6)\n\t" \
    "v_mfma_f32_16x16x32_bf16 v[32:35], v[208:211], v[88:91], v[32:35]\n\t" \
    "ds_read_b128 v[84:87], v10 offset:25600\n\t" \
    "v_mfma_f32_16x16x32_bf16 v[36:39], v[212:215], v[88:91], v[36:39]\n\t" \
    "s_waitcnt lgkmcnt(6)\n\t" \
    "v_mfma_f32_16x16x32_bf16 v[40:43], v[208:211], v[92:95], v[40:43]\n\t" \
    "ds_read_b128 v[88:91], v10 offset:26624\n\t" \
    "v_mfma_f32_16x16x32_bf16 v[44:47], v[212:215], v[92:95], v[44:47]\n\t" \
    "s_waitcnt lgkmcnt(6)\n\t" \
    "v_mfma_f32_16x16x32_bf16 v[48:51], v[208:211], v[96:99], v[48:51]\n\t" \
    "ds_read_b128 v[92:95], v10 offset:27648\n\t" \
    "v_mfma_f32_16x16x32_bf16 v[52:55], v[212:215], v[96:99], v[52:55]\n\t" \
    "global_load_dwordx4 v[244:247], v15, s[66:67] sc1\n\t" \
    "s_waitcnt lgkmcnt(6)\n\t" \
    "v_mfma_f32_16x16x32_bf16 v[56:59], v[208:211], v[100:103], v[56:59]\n\t" \
    "ds_read_b128 v[96:99], v10 offset:28672\n\t" \
    "v_mfma_f32_16x16x32_bf16 v[60:63], v[212:215], v[100:103], v[60:63]\n\t" \
    "s_waitcnt lgkmcnt(6)\n\t" \
    "v_mfma_f32_16x16x32_bf16 v[64:67], v[208:211], v[104:107], v[64:67]\n\t" \
    "ds_read_b128 v[100:103], v10 offset:29696\n\t" \
    "v_mfma_f32_16x16x32_bf16 v[68:71], v[212:215], v[104:107], v[68:71]\n\t" \
    "s_waitcnt lgkmcnt(6)\n\t" \
    "v_mfma_f32_16x16x32_bf16 v[72:75], v[208:211], v[108:111], v[72:75]\n\t" \
    "ds_read_b128 v[104:107], v10 offset:30720\n\t" \
    "v_mfma_f32_16x16x32_bf16 v[76:79], v[212:215], v[108:111], v[76:79]\n\t" \
    "global_load_dwordx4 v[192:195], %[off], s[68:69] offset:3072 sc1\n\t" \
    "s_nop 0\n\t" \
    "ds_read_b128 v[108:111], v10 offset:31744\n\t" \
    "s_waitcnt vmcnt(18)\n\t" \
    "s_waitcnt lgkmcnt(7)\n\t" \
    "v_mfma_f32_16x16x32_bf16 v[16:19], v[216:219], v[80:83], v[16:19]\n\t" \
    "v_mfma_f32_16x16x32_bf16 v[20:23], v[220:223], v[80:83], v[20:23]\n\t" \
    "s_waitcnt lgkmcnt(6)\n\t" \
    "v_mfma_f32_16x16x32_bf16 v[24:27], v[216:219], v[84:87], v[24:27]\n\t" \
    "v_mfma_f32_16x16x32_bf16 v[28:31], v[220:223], v[84:87], v[28:31]\n\t" \
    "global_load_dwordx4 v[196:199], v15, s[68:69] sc1\n\t" \
    "s_waitcnt lgkmcnt(5)\n\t" \
    "v_mfma_f32_16x16x32_bf16 v[32:35], v[216:219], v[88:91], v[32:35]\n\t" \
    "v_mfma_f32_16x16x32_bf16 v[36:39], v[220:223], v[88:91], v[36:39]\n\t" \
    "s_waitcnt lgkmcnt(4)\n\t" \
    "v_mfma_f32_16x16x32_bf16 v[40:43], v[216:219], v[92:95], v[40:43]\n\t" \
    "v_mfma_f32_16x16x32_bf16 v[44:47], v[220:223], v[92:95], v[44:47]\n\t" \
    "s_waitcnt lgkmcnt(3)\n\t" \
    "v_mfma_f32_16x16x32_bf16 v[48:51], v[216:219], v[96:99], v[48:51]\n\t" \
    "v_mfma_f32_16x16x32_bf16 v[52:55], v[220:223], v[96:99], v[52:55]\n\t" \
    "s_waitcnt lgkmcnt(2)\n\t" \
    "v_mfma_f32_16x16x32_bf16 v[56:59], v[216:219], v[100:103], v[56:59]\n\t" \
    "v_mfma_f32_16x16x32_bf16 v[60:63], v[220:223], v[100:103], v[60:63]\n\t" \
    "s_waitcnt lgkmcnt(1)\n\t" \
    "v_mfma_f32_16x16x32_bf16 v[64:67], v[216:219], v[104:107], v[64:67]\n\t" \
    "v_mfma_f32_16x16x32_bf16 v[68:71], v[220:223], v[104:107], v[68:71]\n\t" \
    "s_waitcnt lgkmcnt(0)\n\t" \
    "v_mfma_f32_16x16x32_bf16 v[72:75], v[216:219], v[108:111], v[72:75]\n\t" \
    "v_mfma_f32_16x16x32_bf16 v[76:79], v[220:223], v[108:111], v[76:79]\n\t" \
    "s_waitcnt vmcnt(18)\n\t" \
    "CK2A_%=:\n\t" \
    "v_max3_u32 v6, v112, v113, v114\n\t" \
    "v_max_u32 v6, v6, v115\n\t" \
    "v_cmp_eq_u32 vcc, -1, v6\n\t" \
    "s_cbranch_vccz CK2B_%=\n\t" \
    "s_cmp_eq_u32 s70, 0\n\t" \
    "s_cbranch_scc1 CK2B_%=\n\t" \
    "s_sub_u32 s70, s70, 1\n\t" \
    "global_load_dwordx4 v[112:115], v14, %[sb] sc1\n\t" \
    "s_waitcnt vmcnt(0)\n\t" \
    "s_branch CK2A_%=\n\t" \
    "CK2B_%=:\n\t" \
    "s_nop 15\n\t"
#define GU_MAIN_CLOBBERS "v6", "v7", "v8", "v9", "v10", "v11", "v12", "v13", "v14", "v15", "v80", "v81", "v82", "v83", "v84", "v85", "v86", "v87", "v88", "v89", "v90", "v91", "v92", "v93", "v94", "v95", "v96", "v97", "v98", "v99", "v100", "v101", "v102", "v103", "v104", "v105", "v106", "v107", "v108", "v109", "v110", "v111", "s60", "s61", "s62", "s63", "s64", "s65", "s66", "s67", "s68", "s69", "s70", "vcc", "scc", "memory"
#define DN_PRE01_ISSUE \
    "s_mov_b64 s[64:65], %[wb]\n\t" \
    "v_add_u32 v9, 4096, %[off]\n\t" \
    "global_load_dwordx4 v[112:115], %[off], s[64:65] offset:0 sc1\n\t" \
    "global_load_dwordx4 v[116:119], %[off], s[64:65] offset:1024 sc1\n\t" \
    "global_load_dwordx4 v[120:123], %[off], s[64:65] offset:2048 sc1\n\t" \
    "global_load_dwordx4 v[124:127], %[off], s[64:65] offset:3072 sc1\n\t" \
    "global_load_dwordx4 v[128:131], v9, s[64:65] sc1\n\t" \
    "s_add_u32 s64, s64, %[st]\n\t" \
    "s_addc_u32 s65, s65, 0\n\t" \
    "global_load_dwordx4 v[132:135], %[off], s[64:65] offset:0 sc1\n\t" \
    "global_load_dwordx4 v[136:139], %[off], s[64:65] offset:1024 sc1\n\t" \
    "global_load_dwordx4 v[140:143], %[off], s[64:65] offset:2048 sc1\n\t" \
    "global_load_dwordx4 v[144:147], %[off], s[64:65] offset:3072 sc1\n\t" \
    "global_load_dwordx4 v[148:151], v9, s[64:65] sc1\n\t" \
    "s_add_u32 s64, s64, %[st]\n\t" \
    "s_addc_u32 s65, s65, 0\n\t"
#define DN_PRE01_ISSUE_CLOBBERS "v9", "s64", "s65", "scc", "memory"
#define DN_PRE01_COMMIT \
    "v_add_u32 v9, 4096, %[off]\n\t" \
    "v_add_u32 v10, %[lw], %[off]\n\t" \
    "v_add_u32 v11, 81920, v10\n\t" \
    "s_waitcnt vmcnt(0)\n\t" \
    "ds_write_b128 v10, v[112:115] offset:0\n\t" \
    "ds_write_b128 v10, v[116:119] offset:1024\n\t" \
    "ds_write_b128 v10, v[120:123] offset:2048\n\t" \
    "ds_write_b128 v10, v[124:127] offset:3072\n\t" \
    "ds_write_b128 v10, v[128:131] offset:4096\n\t" \
    "ds_write_b128 v10, v[132:135] offset:40960\n\t" \
    "ds_write_b128 v10, v[136:139] offset:41984\n\t" \
    "ds_write_b128 v10, v[140:143] offset:43008\n\t" \
    "ds_write_b128 v10, v[144:147] offset:44032\n\t" \
    "ds_write_b128 v10, v[148:151] offset:45056\n\t" \
    "s_waitcnt lgkmcnt(0)\n\t"
#define DN_PRE01_COMMIT_CLOBBERS "v9", "v10", "v11", "memory"
#define DN_PRE23_ISSUE \
    "s_mov_b64 s[64:65], %[wb]\n\t" \
    "v_add_u32 v9, 4096, %[off]\n\t" \
    "global_load_dwordx4 v[160:163], %[off], s[64:65] offset:0 sc1\n\t" \
    "global_load_dwordx4 v[164:167], %[off], s[64:65] offset:1024 sc1\n\t" \
    "global_load_dwordx4 v[168:171], %[off], s[64:65] offset:2048 sc1\n\t" \
    "global_load_dwordx4 v[172:175], %[off], s[64:65] offset:3072 sc1\n\t" \
    "global_load_dwordx4 v[176:179], v9, s[64:65] sc1\n\t" \
    "s_add_u32 s64, s64, %[st]\n\t" \
    "s_addc_u32 s65, s65, 0\n\t" \
    "global_load_dwordx4 v[180:183], %[off], s[64:65] offset:0 sc1\n\t" \
    "global_load_dwordx4 v[184:187], %[off], s[64:65] offset:1024 sc1\n\t" \
    "global_load_dwordx4 v[188:191], %[off], s[64:65] offset:2048 sc1\n\t" \
    "global_load_dwordx4 v[192:195], %[off], s[64:65] offset:3072 sc1\n\t" \
    "global_load_dwordx4 v[196:199], v9, s[64:65] sc1\n\t" \
    "s_add_u32 s64, s64, %[st]\n\t" \
    "s_addc_u32 s65, s65, 0\n\t"
#define DN_PRE23_ISSUE_CLOBBERS "v9", "s64", "s65", "scc", "memory"
#define DN_PRE23_COMMIT \
    "v_add_u32 v9, 4096, %[off]\n\t" \
    "v_add_u32 v10, %[lw], %[off]\n\t" \
    "v_add_u32 v11, 81920, v10\n\t" \
    "s_waitcnt vmcnt(4)\n\t" \
    "ds_write_b128 v11, v[160:163] offset:0\n\t" \
    "ds_write_b128 v11, v[164:167] offset:1024\n\t" \
    "ds_write_b128 v11, v[168:171] offset:2048\n\t" \
    "ds_write_b128 v11, v[172:175] offset:3072\n\t" \
    "ds_write_b128 v11, v[176:179] offset:4096\n\t" \
    "ds_write_b128 v11, v[180:183] offset:40960\n\t" \
    "ds_write_b128 v11, v[184:187] offset:41984\n\t" \
    "ds_write_b128 v11, v[188:191] offset:43008\n\t" \
    "ds_write_b128 v11, v[192:195] offset:44032\n\t" \
    "ds_write_b128 v11, v[196:199] offset:45056\n\t" \
    "s_waitcnt lgkmcnt(0)\n\t"
#define DN_PRE23_COMMIT_CLOBBERS "v9", "v10", "v11", "memory"
#define DN_COMMIT4 \
    "v_add_u32 v9, 4096, %[off]\n\t" \
    "v_add_u32 v10, %[lw], %[off]\n\t" \
    "v_add_u32 v11, 81920, v10\n\t" \
    "s_waitcnt vmcnt(4)\n\t" \
    "ds_write_b128 v10, v[128:131] offset:0\n\t" \
    "ds_write_b128 v10, v[132:135] offset:1024\n\t" \
    "ds_write_b128 v10, v[136:139] offset:2048\n\t" \
    "ds_write_b128 v10, v[140:143] offset:3072\n\t" \
    "ds_write_b128 v10, v[144:147] offset:4096\n\t" \
    "ds_write_b128 v10, v[228:231] offset:40960\n\t" \
    "ds_write_b128 v10, v[232:235] offset:41984\n\t" \
    "ds_write_b128 v10, v[236:239] offset:43008\n\t" \
    "ds_write_b128 v10, v[240:243] offset:44032\n\t" \
    "ds_write_b128 v10, v[244:247] offset:45056\n\t" \
    "ds_write_b128 v11, v[160:163] offset:0\n\t" \
    "ds_write_b128 v11, v[164:167] offset:1024\n\t" \
    "ds_write_b128 v11, v[168:171] offset:2048\n\t" \
    "ds_write_b128 v11, v[172:175] offset:3072\n\t" \
    "ds_write_b128 v11, v[176:179] offset:4096\n\t" \
    "ds_write_b128 v11, v[180:183] offset:40960\n\t" \
    "ds_write_b128 v11, v[184:187] offset:41984\n\t" \
    "ds_write_b128 v11, v[188:191] offset:43008\n\t" \
    "ds_write_b128 v11, v[192:195] offset:44032\n\t" \
    "ds_write_b128 v11, v[196:199] offset:45056\n\t" \
    "s_waitcnt lgkmcnt(0)\n\t"
#define DN_COMMIT4_CLOBBERS "v9", "v10", "v11", "memory"
#define DN_MAIN_ASM \
    "s_mov_b64 s[64:65], %[wb]\n\t" \
    "s_mov_b64 s[60:61], %[hb]\n\t" \
    "s_mov_b32 s70, 30000\n\t" \
    "s_add_u32 s62, s60, 55296\n\t" \
    "s_addc_u32 s63, s61, 0\n\t" \
    "v_add_u32 v9, 4096, %[off]\n\t" \
    "v_add_u32 v10, %[lw], %[off]\n\t" \
    "v_add_u32 v11, 81920, v10\n\t" \
    "s_waitcnt vmcnt(0)\n\t" \
    "global_load_dwordx4 v[112:115], %[off], s[60:61] offset:0 sc1\n\t" \
    "global_load_dwordx4 v[168:171], %[off], s[62:63] offset:0 sc1\n\t" \
    "global_load_dwordx4 v[116:119], %[off], s[60:61] offset:1024 sc1\n\t" \
    "global_load_dwordx4 v[172:175], %[off], s[62:63] offset:1024 sc1\n\t" \
    "global_load_dwordx4 v[120:123], %[off], s[60:61] offset:2048 sc1\n\t" \
    "global_load_dwordx4 v[176:179], %[off], s[62:63] offset:2048 sc1\n\t" \
    "v_add_u32 v12, %[l0], %[off]\n\t" \
    "v_add_u32 v13, 40960, v12\n\t" \
    "v_add_u32 v14, 81920, v12\n\t" \
    "v_add_u32 v15, 122880, v12\n\t" \
    "v_mov_b32 v16, 0\n\t" \
    "v_mov_b32 v17, 0\n\t" \
    "v_mov_b32 v18, 0\n\t" \
    "v_mov_b32 v19, 0\n\t" \
    "v_mov_b32 v20, 0\n\t" \
    "v_mov_b32 v21, 0\n\t" \
    "v_mov_b32 v22, 0\n\t" \
    "v_mov_b32 v23, 0\n\t" \
    "v_mov_b32 v24, 0\n\t" \
    "v_mov_b32 v25, 0\n\t" \
    "v_mov_b32 v26, 0\n\t" \
    "v_mov_b32 v27, 0\n\t" \
    "v_mov_b32 v28, 0\n\t" \
    "v_mov_b32 v29, 0\n\t" \
    "v_mov_b32 v30, 0\n\t" \
    "v_mov_b32 v31, 0\n\t" \
    "v_mov_b32 v32, 0\n\t" \
    "v_mov_b32 v33, 0\n\t" \
    "v_mov_b32 v34, 0\n\t" \
    "v_mov_b32 v35, 0\n\t" \
    "v_mov_b32 v36, 0\n\t" \
    "v_mov_b32 v37, 0\n\t" \
    "v_mov_b32 v38, 0\n\t" \
    "v_mov_b32 v39, 0\n\t" \
    "v_mov_b32 v40, 0\n\t" \
    "v_mov_b32 v41, 0\n\t" \
    "v_mov_b32 v42, 0\n\t" \
    "v_mov_b32 v43, 0\n\t" \
    "v_mov_b32 v44, 0\n\t" \
    "v_mov_b32 v45, 0\n\t" \
    "v_mov_b32 v46, 0\n\t" \
    "v_mov_b32 v47, 0\n\t" \
    "v_mov_b32 v48, 0\n\t" \
    "v_mov_b32 v49, 0\n\t" \
    "v_mov_b32 v50, 0\n\t" \
    "v_mov_b32 v51, 0\n\t" \
    "v_mov_b32 v52, 0\n\t" \
    "v_mov_b32 v53, 0\n\t" \
    "v_mov_b32 v54, 0\n\t" \
    "v_mov_b32 v55, 0\n\t" \
    "ds_read_b128 v[56:59], v12 offset:0\n\t" \
    "ds_read_b128 v[60:63], v12 offset:1024\n\t" \
    "ds_read_b128 v[64:67], v12 offset:2048\n\t" \
    "ds_read_b128 v[68:71], v12 offset:3072\n\t" \
    "ds_read_b128 v[72:75], v12 offset:4096\n\t" \
    "s_waitcnt vmcnt(4)\n\t" \
    "CK0A_%=:\n\t" \
    "v_max3_u32 v8, v112, v113, v114\n\t" \
    "v_max3_u32 v8, v8, v115, v168\n\t" \
    "v_max3_u32 v8, v8, v169, v170\n\t" \
    "v_max_u32 v8, v8, v171\n\t" \
    "v_cmp_eq_u32 vcc, -1, v8\n\t" \
    "s_cbranch_vccz CK0B_%=\n\t" \
    "s_cmp_eq_u32 s70, 0\n\t" \
    "s_cbranch_scc1 CK0B_%=\n\t" \
    "s_sub_u32 s70, s70, 1\n\t" \
    "s_mov_b64 s[68:69], %[hb]\n\t" \
    "s_add_u32 s68, s68, 0\n\t" \
    "s_addc_u32 s69, s69, 0\n\t" \
    "global_load_dwordx4 v[112:115], %[off], s[68:69] offset:0 sc1\n\t" \
    "global_load_dwordx4 v[116:119], %[off], s[68:69] offset:1024 sc1\n\t" \
    "global_load_dwordx4 v[120:123], %[off], s[68:69] offset:2048 sc1\n\t" \
    "s_add_u32 s68, s68, 55296\n\t" \
    "s_addc_u32 s69, s69, 0\n\t" \
    "global_load_dwordx4 v[168:171], %[off], s[68:69] offset:0 sc1\n\t" \
    "global_load_dwordx4 v[172:175], %[off], s[68:69] offset:1024 sc1\n\t" \
    "global_load_dwordx4 v[176:179], %[off], s[68:69] offset:2048 sc1\n\t" \
    "s_waitcnt vmcnt(0)\n\t" \
    "s_branch CK0A_%=\n\t" \
    "CK0B_%=:\n\t" \
    "ds_read_b128 v[76:79], v12 offset:20480\n\t" \
    "s_waitcnt lgkmcnt(5)\n\t" \
    "v_mfma_f32_16x16x32_bf16 v[16:19], v[56:59], v[112:115], v[16:19]\n\t" \
    "v_mfma_f32_16x16x32_bf16 v[36:39], v[56:59], v[168:171], v[36:39]\n\t" \
    "global_load_dwordx4 v[124:127], %[off], s[60:61] offset:3072 sc1\n\t" \
    "s_add_u32 s60, s60, 4096\n\t" \
    "s_addc_u32 s61, s61, 0\n\t" \
    "ds_read_b128 v[80:83], v12 offset:21504\n\t" \
    "s_waitcnt lgkmcnt(5)\n\t" \
    "v_mfma_f32_16x16x32_bf16 v[20:23], v[60:63], v[112:115], v[20:23]\n\t" \
    "v_mfma_f32_16x16x32_bf16 v[40:43], v[60:63], v[168:171], v[40:43]\n\t" \
    "global_load_dwordx4 v[180:183], %[off], s[62:63] offset:3072 sc1\n\t" \
    "s_add_u32 s62, s62, 4096\n\t" \
    "s_addc_u32 s63, s63, 0\n\t" \
    "ds_read_b128 v[84:87], v12 offset:22528\n\t" \
    "s_waitcnt lgkmcnt(5)\n\t" \
    "v_mfma_f32_16x16x32_bf16 v[24:27], v[64:67], v[112:115], v[24:27]\n\t" \
    "v_mfma_f32_16x16x32_bf16 v[44:47], v[64:67], v[168:171], v[44:47]\n\t" \
    "global_load_dwordx4 v[224:227], %[off], s[64:65] offset:0 sc1\n\t" \
    "ds_read_b128 v[88:91], v12 offset:23552\n\t" \
    "s_waitcnt lgkmcnt(5)\n\t" \
    "v_mfma_f32_16x16x32_bf16 v[28:31], v[68:71], v[112:115], v[28:31]\n\t" \
    "v_mfma_f32_16x16x32_bf16 v[48:51], v[68:71], v[168:171], v[48:51]\n\t" \
    "global_load_dwordx4 v[228:231], %[off], s[64:65] offset:1024 sc1\n\t" \
    "ds_read_b128 v[92:95], v12 offset:24576\n\t" \
    "s_waitcnt lgkmcnt(5)\n\t" \
    "v_mfma_f32_16x16x32_bf16 v[32:35], v[72:75], v[112:115], v[32:35]\n\t" \
    "v_mfma_f32_16x16x32_bf16 v[52:55], v[72:75], v[168:171], v[52:55]\n\t" \
    "global_load_dwordx4 v[232:235], %[off], s[64:65] offset:2048 sc1\n\t" \
    "s_barrier\n\t" \
    "s_waitcnt vmcnt(7)\n\t" \
    "CK1A_%=:\n\t" \
    "v_max3_u32 v8, v116, v117, v118\n\t" \
    "v_max3_u32 v8, v8, v119, v172\n\t" \
    "v_max3_u32 v8, v8, v173, v174\n\t" \
    "v_max_u32 v8, v8, v175\n\t" \
    "v_cmp_eq_u32 vcc, -1, v8\n\t" \
    "s_cbranch_vccz CK1B_%=\n\t" \
    "s_cmp_eq_u32 s70, 0\n\t" \
    "s_cbranch_scc1 CK1B_%=\n\t" \
    "s_sub_u32 s70, s70, 1\n\t" \
    "s_mov_b64 s[68:69], %[hb]\n\t" \
    "s_add_u32 s68, s68, 1024\n\t" \
    "s_addc_u32 s69, s69, 0\n\t" \
    "global_load_dwordx4 v[116:119], %[off], s[68:69] offset:0 sc1\n\t" \
    "global_load_dwordx4 v[120:123], %[off], s[68:69] offset:1024 sc1\n\t" \
    "global_load_dwordx4 v[124:127], %[off], s[68:69] offset:2048 sc1\n\t" \
    "s_add_u32 s68, s68, 55296\n\t" \
    "s_addc_u32 s69, s69, 0\n\t" \
    "global_load_dwordx4 v[172:175], %[off], s[68:69] offset:0 sc1\n\t" \
    "global_load_dwordx4 v[176:179], %[off], s[68:69] offset:1024 sc1\n\t" \
    "global_load_dwordx4 v[180:183], %[off], s[68:69] offset:2048 sc1\n\t" \
    "s_waitcnt vmcnt(0)\n\t" \
    "s_branch CK1A_%=\n\t" \
    "CK1B_%=:\n\t" \
    "ds_read_b128 v[56:59], v13 offset:0\n\t" \
    "s_waitcnt lgkmcnt(5)\n\t" \
    "v_mfma_f32_16x16x32_bf16 v[16:19], v[76:79], v[116:119], v[16:19]\n\t" \
    "v_mfma_f32_16x16x32_bf16 v[36:39], v[76:79], v[172:175], v[36:39]\n\t" \
    "global_load_dwordx4 v[236:239], %[off], s[64:65] offset:3072 sc1\n\t" \
    "ds_read_b128 v[60:63], v13 offset:1024\n\t" \
    "s_waitcnt lgkmcnt(5)\n\t" \
    "v_mfma_f32_16x16x32_bf16 v[20:23], v[80:83], v[116:119], v[20:23]\n\t" \
    "v_mfma_f32_16x16x32_bf16 v[40:43], v[80:83], v[172:175], v[40:43]\n\t" \
    "global_load_dwordx4 v[240:243], v9, s[64:65] sc1\n\t" \
    "s_add_u32 s64, s64, %[st]\n\t" \
    "s_addc_u32 s65, s65, 0\n\t" \
    "ds_read_b128 v[64:67], v13 offset:2048\n\t" \
    "s_waitcnt lgkmcnt(5)\n\t" \
    "v_mfma_f32_16x16x32_bf16 v[24:27], v[84:87], v[116:119], v[24:27]\n\t" \
    "v_mfma_f32_16x16x32_bf16 v[44:47], v[84:87], v[172:175], v[44:47]\n\t" \
    "global_load_dwordx4 v[128:131], %[off], s[60:61] offset:0 sc1\n\t" \
    "ds_read_b128 v[68:71], v13 offset:3072\n\t" \
    "s_waitcnt lgkmcnt(5)\n\t" \
    "v_mfma_f32_16x16x32_bf16 v[28:31], v[88:91], v[116:119], v[28:31]\n\t" \
    "v_mfma_f32_16x16x32_bf16 v[48:51], v[88:91], v[172:175], v[48:51]\n\t" \
    "global_load_dwordx4 v[184:187], %[off], s[62:63] offset:0 sc1\n\t" \
    "ds_read_b128 v[72:75], v13 offset:4096\n\t" \
    "s_waitcnt lgkmcnt(5)\n\t" \
    "v_mfma_f32_16x16x32_bf16 v[32:35], v[92:95], v[116:119], v[32:35]\n\t" \
    "v_mfma_f32_16x16x32_bf16 v[52:55], v[92:95], v[172:175], v[52:55]\n\t" \
    "global_load_dwordx4 v[132:135], %[off], s[60:61] offset:1024 sc1\n\t" \
    "s_waitcnt vmcnt(10)\n\t" \
    "CK2A_%=:\n\t" \
    "v_max3_u32 v8, v120, v121, v122\n\t" \
    "v_max3_u32 v8, v8, v123, v176\n\t" \
    "v_max3_u32 v8, v8, v177, v178\n\t" \
    "v_max_u32 v8, v8, v179\n\t" \
    "v_cmp_eq_u32 vcc, -1, v8\n\t" \
    "s_cbranch_vccz CK2B_%=\n\t" \
    "s_cmp_eq_u32 s70, 0\n\t" \
    "s_cbranch_scc1 CK2B_%=\n\t" \
    "s_sub_u32 s70, s70, 1\n\t" \
    "s_mov_b64 s[68:69], %[hb]\n\t" \
    "s_add_u32 s68, s68, 2048\n\t" \
    "s_addc_u32 s69, s69, 0\n\t" \
    "global_load_dwordx4 v[120:123], %[off], s[68:69] offset:0 sc1\n\t" \
    "global_load_dwordx4 v[124:127], %[off], s[68:69] offset:1024 sc1\n\t" \
    "global_load_dwordx4 v[128:131], %[off], s[68:69] offset:2048 sc1\n\t" \
    "s_add_u32 s68, s68, 55296\n\t" \
    "s_addc_u32 s69, s69, 0\n\t" \
    "global_load_dwordx4 v[176:179], %[off], s[68:69] offset:0 sc1\n\t" \
    "global_load_dwordx4 v[180:183], %[off], s[68:69] offset:1024 sc1\n\t" \
    "global_load_dwordx4 v[184:187], %[off], s[68:69] offset:2048 sc1\n\t" \
    "s_waitcnt vmcnt(0)\n\t" \
    "s_branch CK2A_%=\n\t" \
    "CK2B_%=:\n\t" \
    "ds_read_b128 v[76:79], v13 offset:20480\n\t" \
    "s_waitcnt lgkmcnt(5)\n\t" \
    "v_mfma_f32_16x16x32_bf16 v[16:19], v[56:59], v[120:123], v[16:19]\n\t" \
    "v_mfma_f32_16x16x32_bf16 v[36:39], v[56:59], v[176:179], v[36:39]\n\t" \
    "global_load_dwordx4 v[188:191], %[off], s[62:63] offset:1024 sc1\n\t" \
    "ds_read_b128 v[80:83], v13 offset:21504\n\t" \
    "s_waitcnt lgkmcnt(5)\n\t" \
    "v_mfma_f32_16x16x32_bf16 v[20:23], v[60:63], v[120:123], v[20:23]\n\t" \
    "v_mfma_f32_16x16x32_bf16 v[40:43], v[60:63], v[176:179], v[40:43]\n\t" \
    "global_load_dwordx4 v[96:99], %[off], s[64:65] offset:0 sc1\n\t" \
    "ds_read_b128 v[84:87], v13 offset:22528\n\t" \
    "s_waitcnt lgkmcnt(5)\n\t" \
    "v_mfma_f32_16x16x32_bf16 v[24:27], v[64:67], v[120:123], v[24:27]\n\t" \
    "v_mfma_f32_16x16x32_bf16 v[44:47], v[64:67], v[176:179], v[44:47]\n\t" \
    "global_load_dwordx4 v[100:103], %[off], s[64:65] offset:1024 sc1\n\t" \
    "ds_read_b128 v[88:91], v13 offset:23552\n\t" \
    "s_waitcnt lgkmcnt(5)\n\t" \
    "v_mfma_f32_16x16x32_bf16 v[28:31], v[68:71], v[120:123], v[28:31]\n\t" \
    "v_mfma_f32_16x16x32_bf16 v[48:51], v[68:71], v[176:179], v[48:51]\n\t" \
    "global_load_dwordx4 v[104:107], %[off], s[64:65] offset:2048 sc1\n\t" \
    "ds_read_b128 v[92:95], v13 offset:24576\n\t" \
    "s_waitcnt lgkmcnt(5)\n\t" \
    "v_mfma_f32_16x16x32_bf16 v[32:35], v[72:75], v[120:123], v[32:35]\n\t" \
    "v_mfma_f32_16x16x32_bf16 v[52:55], v[72:75], v[176:179], v[52:55]\n\t" \
    "global_load_dwordx4 v[108:111], %[off], s[64:65] offset:3072 sc1\n\t" \
    "s_barrier\n\t" \
    "s_waitcnt vmcnt(8)\n\t" \
    "ds_write_b128 v10, v[224:227] offset:0\n\t" \
    "ds_write_b128 v10, v[228:231] offset:1024\n\t" \
    "ds_write_b128 v10, v[232:235] offset:2048\n\t" \
    "ds_write_b128 v10, v[236:239] offset:3072\n\t" \
    "ds_write_b128 v10, v[240:243] offset:4096\n\t" \
    "s_waitcnt vmcnt(13)\n\t" \
    "CK3A_%=:\n\t" \
    "v_max3_u32 v8, v124, v125, v126\n\t" \
    "v_max3_u32 v8, v8, v127, v180\n\t" \
    "v_max3_u32 v8, v8, v181, v182\n\t" \
    "v_max_u32 v8, v8, v183\n\t" \
    "v_cmp_eq_u32 vcc, -1, v8\n\t" \
    "s_cbranch_vccz CK3B_%=\n\t" \
    "s_cmp_eq_u32 s70, 0\n\t" \
    "s_cbranch_scc1 CK3B_%=\n\t" \
    "s_sub_u32 s70, s70, 1\n\t" \
    "s_mov_b64 s[68:69], %[hb]\n\t" \
    "s_add_u32 s68, s68, 3072\n\t" \
    "s_addc_u32 s69, s69, 0\n\t" \
    "global_load_dwordx4 v[124:127], %[off], s[68:69] offset:0 sc1\n\t" \
    "global_load_dwordx4 v[128:131], %[off], s[68:69] offset:1024 sc1\n\t" \
    "global_load_dwordx4 v[132:135], %[off], s[68:69] offset:2048 sc1\n\t" \
    "s_add_u32 s68, s68, 55296\n\t" \
    "s_addc_u32 s69, s69, 0\n\t" \
    "global_load_dwordx4 v[180:183], %[off], s[68:69] offset:0 sc1\n\t" \
    "global_load_dwordx4 v[184:187], %[off], s[68:69] offset:1024 sc1\n\t" \
    "global_load_dwordx4 v[188:191], %[off], s[68:69] offset:2048 sc1\n\t" \
    "s_waitcnt vmcnt(0)\n\t" \
    "s_branch CK3A_%=\n\t" \
    "CK3B_%=:\n\t" \
    "ds_read_b128 v[56:59], v14 offset:0\n\t" \
    "s_waitcnt lgkmcnt(10)\n\t" \
    "v_mfma_f32_16x16x32_bf16 v[16:19], v[76:79], v[124:127], v[16:19]\n\t" \
    "v_mfma_f32_16x16x32_bf16 v[36:39], v[76:79], v[180:183], v[36:39]\n\t" \
    "global_load_dwordx4 v[244:247], v9, s[64:65] sc1\n\t" \
    "s_add_u32 s64, s64, %[st]\n\t" \
    "s_addc_u32 s65, s65, 0\n\t" \
    "ds_read_b128 v[60:63], v14 offset:1024\n\t" \
    "s_waitcnt lgkmcnt(10)\n\t" \
    "v_mfma_f32_16x16x32_bf16 v[20:23], v[80:83], v[124:127], v[20:23]\n\t" \
    "v_mfma_f32_16x16x32_bf16 v[40:43], v[80:83], v[180:183], v[40:43]\n\t" \
    "global_load_dwordx4 v[136:139], %[off], s[60:61] offset:2048 sc1\n\t" \
    "ds_read_b128 v[64:67], v14 offset:2048\n\t" \
    "s_waitcnt lgkmcnt(10)\n\t" \
    "v_mfma_f32_16x16x32_bf16 v[24:27], v[84:87], v[124:127], v[24:27]\n\t" \
    "v_mfma_f32_16x16x32_bf16 v[44:47], v[84:87], v[180:183], v[44:47]\n\t" \
    "global_load_dwordx4 v[192:195], %[off], s[62:63] offset:2048 sc1\n\t" \
    "ds_read_b128 v[68:71], v14 offset:3072\n\t" \
    "s_waitcnt lgkmcnt(10)\n\t" \
    "v_mfma_f32_16x16x32_bf16 v[28:31], v[88:91], v[124:127], v[28:31]\n\t" \
    "v_mfma_f32_16x16x32_bf16 v[48:51], v[88:91], v[180:183], v[48:51]\n\t" \
    "global_load_dwordx4 v[140:143], %[off], s[60:61] offset:3072 sc1\n\t" \
    "s_add_u32 s60, s60, 4096\n\t" \
    "s_addc_u32 s61, s61, 0\n\t" \
    "ds_read_b128 v[72:75], v14 offset:4096\n\t" \
    "s_waitcnt lgkmcnt(10)\n\t" \
    "v_mfma_f32_16x16x32_bf16 v[32:35], v[92:95], v[124:127], v[32:35]\n\t" \
    "v_mfma_f32_16x16x32_bf16 v[52:55], v[92:95], v[180:183], v[52:55]\n\t" \
    "global_load_dwordx4 v[196:199], %[off], s[62:63] offset:3072 sc1\n\t" \
    "s_add_u32 s62, s62, 4096\n\t" \
    "s_addc_u32 s63, s63, 0\n\t" \
    "s_waitcnt vmcnt(11)\n\t" \
    "CK4A_%=:\n\t" \
    "v_max3_u32 v8, v128, v129, v130\n\t" \
    "v_max3_u32 v8, v8, v131, v184\n\t" \
    "v_max3_u32 v8, v8, v185, v186\n\t" \
    "v_max_u32 v8, v8, v187\n\t" \
    "v_cmp_eq_u32 vcc, -1, v8\n\t" \
    "s_cbranch_vccz CK4B_%=\n\t" \
    "s_cmp_eq_u32 s70, 0\n\t" \
    "s_cbranch_scc1 CK4B_%=\n\t" \
    "s_sub_u32 s70, s70, 1\n\t" \
    "s_mov_b64 s[68:69], %[hb]\n\t" \
    "s_add_u32 s68, s68, 4096\n\t" \
    "s_addc_u32 s69, s69, 0\n\t" \
    "global_load_dwordx4 v[128:131], %[off], s[68:69] offset:0 sc1\n\t" \
    "global_load_dwordx4 v[132:135], %[off], s[68:69] offset:1024 sc1\n\t" \
    "global_load_dwordx4 v[136:139], %[off], s[68:69] offset:2048 sc1\n\t" \
    "s_add_u32 s68, s68, 55296\n\t" \
    "s_addc_u32 s69, s69, 0\n\t" \
    "global_load_dwordx4 v[184:187], %[off], s[68:69] offset:0 sc1\n\t" \
    "global_load_dwordx4 v[188:191], %[off], s[68:69] offset:1024 sc1\n\t" \
    "global_load_dwordx4 v[192:195], %[off], s[68:69] offset:2048 sc1\n\t" \
    "s_waitcnt vmcnt(0)\n\t" \
    "s_branch CK4A_%=\n\t" \
    "CK4B_%=:\n\t" \
    "ds_read_b128 v[76:79], v14 offset:20480\n\t" \
    "s_waitcnt lgkmcnt(5)\n\t" \
    "v_mfma_f32_16x16x32_bf16 v[16:19], v[56:59], v[128:131], v[16:19]\n\t" \
    "v_mfma_f32_16x16x32_bf16 v[36:39], v[56:59], v[184:187], v[36:39]\n\t" \
    "global_load_dwordx4 v[224:227], %[off], s[64:65] offset:0 sc1\n\t" \
    "ds_read_b128 v[80:83], v14 offset:21504\n\t" \
    "s_waitcnt lgkmcnt(5)\n\t" \
    "v_mfma_f32_16x16x32_bf16 v[20:23], v[60:63], v[128:131], v[20:23]\n\t" \
    "v_mfma_f32_16x16x32_bf16 v[40:43], v[60:63], v[184:187], v[40:43]\n\t" \
    "global_load_dwordx4 v[228:231], %[off], s[64:65] offset:1024 sc1\n\t" \
    "ds_read_b128 v[84:87], v14 offset:22528\n\t" \
    "s_waitcnt lgkmcnt(5)\n\t" \
    "v_mfma_f32_16x16x32_bf16 v[24:27], v[64:67], v[128:131], v[24:27]\n\t" \
    "v_mfma_f32_16x16x32_bf16 v[44:47], v[64:67], v[184:187], v[44:47]\n\t" \
    "global_load_dwordx4 v[232:235], %[off], s[64:65] offset:2048 sc1\n\t" \
    "ds_read_b128 v[88:91], v14 offset:23552\n\t" \
    "s_waitcnt lgkmcnt(5)\n\t" \
    "v_mfma_f32_16x16x32_bf16 v[28:31], v[68:71], v[128:131], v[28:31]\n\t" \
    "v_mfma_f32_16x16x32_bf16 v[48:51], v[68:71], v[184:187], v[48:51]\n\t" \
    "global_load_dwordx4 v[236:239], %[off], s[64:65] offset:3072 sc1\n\t" \
    "ds_read_b128 v[92:95], v14 offset:24576\n\t" \
    "s_waitcnt lgkmcnt(5)\n\t" \
    "v_mfma_f32_16x16x32_bf16 v[32:35], v[72:75], v[128:131], v[32:35]\n\t" \
    "v_mfma_f32_16x16x32_bf16 v[52:55], v[72:75], v[184:187], v[52:55]\n\t" \
    "global_load_dwordx4 v[240:243], v9, s[64:65] sc1\n\t" \
    "s_add_u32 s64, s64, %[st]\n\t" \
    "s_addc_u32 s65, s65, 0\n\t" \
    "s_barrier\n\t" \
    "s_waitcnt vmcnt(9)\n\t" \
    "ds_write_b128 v10, v[96:99] offset:40960\n\t" \
    "ds_write_b128 v10, v[100:103] offset:41984\n\t" \
    "ds_write_b128 v10, v[104:107] offset:43008\n\t" \
    "ds_write_b128 v10, v[108:111] offset:44032\n\t" \
    "ds_write_b128 v10, v[244:247] offset:45056\n\t" \
    "s_waitcnt vmcnt(14)\n\t" \
    "CK5A_%=:\n\t" \
    "v_max3_u32 v8, v132, v133, v134\n\t" \
    "v_max3_u32 v8, v8, v135, v188\n\t" \
    "v_max3_u32 v8, v8, v189, v190\n\t" \
    "v_max_u32 v8, v8, v191\n\t" \
    "v_cmp_eq_u32 vcc, -1, v8\n\t" \
    "s_cbranch_vccz CK5B_%=\n\t" \
    "s_cmp_eq_u32 s70, 0\n\t" \
    "s_cbranch_scc1 CK5B_%=\n\t" \
    "s_sub_u32 s70, s70, 1\n\t" \
    "s_mov_b64 s[68:69], %[hb]\n\t" \
    "s_add_u32 s68, s68, 5120\n\t" \
    "s_addc_u32 s69, s69, 0\n\t" \
    "global_load_dwordx4 v[132:135], %[off], s[68:69] offset:0 sc1\n\t" \
    "global_load_dwordx4 v[136:139], %[off], s[68:69] offset:1024 sc1\n\t" \
    "global_load_dwordx4 v[140:143], %[off], s[68:69] offset:2048 sc1\n\t" \
    "s_add_u32 s68, s68, 55296\n\t" \
    "s_addc_u32 s69, s69, 0\n\t" \
    "global_load_dwordx4 v[188:191], %[off], s[68:69] offset:0 sc1\n\t" \
    "global_load_dwordx4 v[192:195], %[off], s[68:69] offset:1024 sc1\n\t" \
    "global_load_dwordx4 v[196:199], %[off], s[68:69] offset:2048 sc1\n\t" \
    "s_waitcnt vmcnt(0)\n\t" \
    "s_branch CK5A_%=\n\t" \
    "CK5B_%=:\n\t" \
    "ds_read_b128 v[56:59], v15 offset:0\n\t" \
    "s_waitcnt lgkmcnt(10)\n\t" \
    "v_mfma_f32_16x16x32_bf16 v[16:19], v[76:79], v[132:135], v[16:19]\n\t" \
    "v_mfma_f32_16x16x32_bf16 v[36:39], v[76:79], v[188:191], v[36:39]\n\t" \
    "global_load_dwordx4 v[144:147], %[off], s[60:61] offset:0 sc1\n\t" \
    "ds_read_b128 v[60:63], v15 offset:1024\n\t" \
    "s_waitcnt lgkmcnt(10)\n\t" \
    "v_mfma_f32_16x16x32_bf16 v[20:23], v[80:83], v[132:135], v[20:23]\n\t" \
    "v_mfma_f32_16x16x32_bf16 v[40:43], v[80:83], v[188:191], v[40:43]\n\t" \
    "global_load_dwordx4 v[200:203], %[off], s[62:63] offset:0 sc1\n\t" \
    "ds_read_b128 v[64:67], v15 offset:2048\n\t" \
    "s_waitcnt lgkmcnt(10)\n\t" \
    "v_mfma_f32_16x16x32_bf16 v[24:27], v[84:87], v[132:135], v[24:27]\n\t" \
    "v_mfma_f32_16x16x32_bf16 v[44:47], v[84:87], v[188:191], v[44:47]\n\t" \
    "global_load_dwordx4 v[148:151], %[off], s[60:61] offset:1024 sc1\n\t" \
    "ds_read_b128 v[68:71], v15 offset:3072\n\t" \
    "s_waitcnt lgkmcnt(10)\n\t" \
    "v_mfma_f32_16x16x32_bf16 v[28:31], v[88:91], v[132:135], v[28:31]\n\t" \
    "v_mfma_f32_16x16x32_bf16 v[48:51], v[88:91], v[188:191], v[48:51]\n\t" \
    "global_load_dwordx4 v[204:207], %[off], s[62:63] offset:1024 sc1\n\t" \
    "ds_read_b128 v[72:75], v15 offset:4096\n\t" \
    "s_waitcnt lgkmcnt(10)\n\t" \
    "v_mfma_f32_16x16x32_bf16 v[32:35], v[92:95], v[132:135], v[32:35]\n\t" \
    "v_mfma_f32_16x16x32_bf16 v[52:55], v[92:95], v[188:191], v[52:55]\n\t" \
    "global_load_dwordx4 v[152:155], %[off], s[60:61] offset:2048 sc1\n\t" \
    "s_waitcnt vmcnt(12)\n\t" \
    "CK6A_%=:\n\t" \
    "v_max3_u32 v8, v136, v137, v138\n\t" \
    "v_max3_u32 v8, v8, v139, v192\n\t" \
    "v_max3_u32 v8, v8, v193, v194\n\t" \
    "v_max_u32 v8, v8, v195\n\t" \
    "v_cmp_eq_u32 vcc, -1, v8\n\t" \
    "s_cbranch_vccz CK6B_%=\n\t" \
    "s_cmp_eq_u32 s70, 0\n\t" \
    "s_cbranch_scc1 CK6B_%=\n\t" \
    "s_sub_u32 s70, s70, 1\n\t" \
    "s_mov_b64 s[68:69], %[hb]\n\t" \
    "s_add_u32 s68, s68, 6144\n\t" \
    "s_addc_u32 s69, s69, 0\n\t" \
    "global_load_dwordx4 v[136:139], %[off], s[68:69] offset:0 sc1\n\t" \
    "global_load_dwordx4 v[140:143], %[off], s[68:69] offset:1024 sc1\n\t" \
    "global_load_dwordx4 v[144:147], %[off], s[68:69] offset:2048 sc1\n\t" \
    "s_add_u32 s68, s68, 55296\n\t" \
    "s_addc_u32 s69, s69, 0\n\t" \
    "global_load_dwordx4 v[192:195], %[off], s[68:69] offset:0 sc1\n\t" \
    "global_load_dwordx4 v[196:199], %[off], s[68:69] offset:1024 sc1\n\t" \
    "global_load_dwordx4 v[200:203], %[off], s[68:69] offset:2048 sc1\n\t" \
    "s_waitcnt vmcnt(0)\n\t" \
    "s_branch CK6A_%=\n\t" \
    "CK6B_%=:\n\t" \
    "ds_read_b128 v[76:79], v15 offset:20480\n\t" \
    "s_waitcnt lgkmcnt(5)\n\t" \
    "v_mfma_f32_16x16x32_bf16 v[16:19], v[56:59], v[136:139], v[16:19]\n\t" \
    "v_mfma_f32_16x16x32_bf16 v[36:39], v[56:59], v[192:195], v[36:39]\n\t" \
    "global_load_dwordx4 v[208:211], %[off], s[62:63] offset:2048 sc1\n\t" \
    "ds_read_b128 v[80:83], v15 offset:21504\n\t" \
    "s_waitcnt lgkmcnt(5)\n\t" \
    "v_mfma_f32_16x16x32_bf16 v[20:23], v[60:63], v[136:139], v[20:23]\n\t" \
    "v_mfma_f32_16x16x32_bf16 v[40:43], v[60:63], v[192:195], v[40:43]\n\t" \
    "global_load_dwordx4 v[156:159], %[off], s[60:61] offset:3072 sc1\n\t" \
    "s_add_u32 s60, s60, 4096\n\t" \
    "s_addc_u32 s61, s61, 0\n\t" \
    "ds_read_b128 v[84:87], v15 offset:22528\n\t" \
    "s_waitcnt lgkmcnt(5)\n\t" \
    "v_mfma_f32_16x16x32_bf16 v[24:27], v[64:67], v[136:139], v[24:27]\n\t" \
    "v_mfma_f32_16x16x32_bf16 v[44:47], v[64:67], v[192:195], v[44:47]\n\t" \
    "global_load_dwordx4 v[212:215], %[off], s[62:63] offset:3072 sc1\n\t" \
    "s_add_u32 s62, s62, 4096\n\t" \
    "s_addc_u32 s63, s63, 0\n\t" \
    "ds_read_b128 v[88:91], v15 offset:23552\n\t" \
    "s_waitcnt lgkmcnt(5)\n\t" \
    "v_mfma_f32_16x16x32_bf16 v[28:31], v[68:71], v[136:139], v[28:31]\n\t" \
    "v_mfma_f32_16x16x32_bf16 v[48:51], v[68:71], v[192:195], v[48:51]\n\t" \
    "global_load_dwordx4 v[160:163], %[off], s[60:61] offset:0 sc1\n\t" \
    "ds_read_b128 v[92:95], v15 offset:24576\n\t" \
    "s_waitcnt lgkmcnt(5)\n\t" \
    "v_mfma_f32_16x16x32_bf16 v[32:35], v[72:75], v[136:139], v[32:35]\n\t" \
    "v_mfma_f32_16x16x32_bf16 v[52:55], v[72:75], v[192:195], v[52:55]\n\t" \
    "global_load_dwordx4 v[216:219], %[off], s[62:63] offset:0 sc1\n\t" \
    "s_barrier\n\t" \
    "s_waitcnt vmcnt(10)\n\t" \
    "ds_write_b128 v11, v[224:227] offset:0\n\t" \
    "ds_write_b128 v11, v[228:231] offset:1024\n\t" \
    "ds_write_b128 v11, v[232:235] offset:2048\n\t" \
    "ds_write_b128 v11, v[236:239] offset:3072\n\t" \
    "ds_write_b128 v11, v[240:243] offset:4096\n\t" \
    "s_waitcnt vmcnt(15)\n\t" \
    "CK7A_%=:\n\t" \
    "v_max3_u32 v8, v140, v141, v142\n\t" \
    "v_max3_u32 v8, v8, v143, v196\n\t" \
    "v_max3_u32 v8, v8, v197, v198\n\t" \
    "v_max_u32 v8, v8, v199\n\t" \
    "v_cmp_eq_u32 vcc, -1, v8\n\t" \
    "s_cbranch_vccz CK7B_%=\n\t" \
    "s_cmp_eq_u32 s70, 0\n\t" \
    "s_cbranch_scc1 CK7B_%=\n\t" \
    "s_sub_u32 s70, s70, 1\n\t" \
    "s_mov_b64 s[68:69], %[hb]\n\t" \
    "s_add_u32 s68, s68, 7168\n\t" \
    "s_addc_u32 s69, s69, 0\n\t" \
    "global_load_dwordx4 v[140:143], %[off], s[68:69] offset:0 sc1\n\t" \
    "global_load_dwordx4 v[144:147], %[off], s[68:69] offset:1024 sc1\n\t" \
    "global_load_dwordx4 v[148:151], %[off], s[68:69] offset:2048 sc1\n\t" \
    "s_add_u32 s68, s68, 55296\n\t" \
    "s_addc_u32 s69, s69, 0\n\t" \
    "global_load_dwordx4 v[196:199], %[off], s[68:69] offset:0 sc1\n\t" \
    "global_load_dwordx4 v[200:203], %[off], s[68:69] offset:1024 sc1\n\t" \
    "global_load_dwordx4 v[204:207], %[off], s[68:69] offset:2048 sc1\n\t" \
    "s_waitcnt vmcnt(0)\n\t" \
    "s_branch CK7A_%=\n\t" \
    "CK7B_%=:\n\t" \
    "ds_read_b128 v[56:59], v12 offset:0\n\t" \
    "s_waitcnt lgkmcnt(10)\n\t" \
    "v_mfma_f32_16x16x32_bf16 v[16:19], v[76:79], v[140:143], v[16:19]\n\t" \
    "v_mfma_f32_16x16x32_bf16 v[36:39], v[76:79], v[196:199], v[36:39]\n\t" \
    "global_load_dwordx4 v[164:167], %[off], s[60:61] offset:1024 sc1\n\t" \
    "ds_read_b128 v[60:63], v12 offset:1024\n\t" \
    "s_waitcnt lgkmcnt(10)\n\t" \
    "v_mfma_f32_16x16x32_bf16 v[20:23], v[80:83], v[140:143], v[20:23]\n\t" \
    "v_mfma_f32_16x16x32_bf16 v[40:43], v[80:83], v[196:199], v[40:43]\n\t" \
    "global_load_dwordx4 v[220:223], %[off], s[62:63] offset:1024 sc1\n\t" \
    "ds_read_b128 v[64:67], v12 offset:2048\n\t" \
    "s_waitcnt lgkmcnt(10)\n\t" \
    "v_mfma_f32_16x16x32_bf16 v[24:27], v[84:87], v[140:143], v[24:27]\n\t" \
    "v_mfma_f32_16x16x32_bf16 v[44:47], v[84:87], v[196:199], v[44:47]\n\t" \
    "ds_read_b128 v[68:71], v12 offset:3072\n\t" \
    "s_waitcnt lgkmcnt(10)\n\t" \
    "v_mfma_f32_16x16x32_bf16 v[28:31], v[88:91], v[140:143], v[28:31]\n\t" \
    "v_mfma_f32_16x16x32_bf16 v[48:51], v[88:91], v[196:199], v[48:51]\n\t" \
    "ds_read_b128 v[72:75], v12 offset:4096\n\t" \
    "s_waitcnt lgkmcnt(10)\n\t" \
    "v_mfma_f32_16x16x32_bf16 v[32:35], v[92:95], v[140:143], v[32:35]\n\t" \
    "v_mfma_f32_16x16x32_bf16 v[52:55], v[92:95], v[196:199], v[52:55]\n\t" \
    "s_waitcnt vmcnt(10)\n\t" \
    "CK8A_%=:\n\t" \
    "v_max3_u32 v8, v144, v145, v146\n\t" \
    "v_max3_u32 v8, v8, v147, v200\n\t" \
    "v_max3_u32 v8, v8, v201, v202\n\t" \
    "v_max_u32 v8, v8, v203\n\t" \
    "v_cmp_eq_u32 vcc, -1, v8\n\t" \
    "s_cbranch_vccz CK8B_%=\n\t" \
    "s_cmp_eq_u32 s70, 0\n\t" \
    "s_cbranch_scc1 CK8B_%=\n\t" \
    "s_sub_u32 s70, s70, 1\n\t" \
    "s_mov_b64 s[68:69], %[hb]\n\t" \
    "s_add_u32 s68, s68, 8192\n\t" \
    "s_addc_u32 s69, s69, 0\n\t" \
    "global_load_dwordx4 v[144:147], %[off], s[68:69] offset:0 sc1\n\t" \
    "global_load_dwordx4 v[148:151], %[off], s[68:69] offset:1024 sc1\n\t" \
    "global_load_dwordx4 v[152:155], %[off], s[68:69] offset:2048 sc1\n\t" \
    "s_add_u32 s68, s68, 55296\n\t" \
    "s_addc_u32 s69, s69, 0\n\t" \
    "global_load_dwordx4 v[200:203], %[off], s[68:69] offset:0 sc1\n\t" \
    "global_load_dwordx4 v[204:207], %[off], s[68:69] offset:1024 sc1\n\t" \
    "global_load_dwordx4 v[208:211], %[off], s[68:69] offset:2048 sc1\n\t" \
    "s_waitcnt vmcnt(0)\n\t" \
    "s_branch CK8A_%=\n\t" \
    "CK8B_%=:\n\t" \
    "ds_read_b128 v[76:79], v12 offset:20480\n\t" \
    "s_waitcnt lgkmcnt(5)\n\t" \
    "v_mfma_f32_16x16x32_bf16 v[16:19], v[56:59], v[144:147], v[16:19]\n\t" \
    "v_mfma_f32_16x16x32_bf16 v[36:39], v[56:59], v[200:203], v[36:39]\n\t" \
    "ds_read_b128 v[80:83], v12 offset:21504\n\t" \
    "s_waitcnt lgkmcnt(5)\n\t" \
    "v_mfma_f32_16x16x32_bf16 v[20:23], v[60:63], v[144:147], v[20:23]\n\t" \
    "v_mfma_f32_16x16x32_bf16 v[40:43], v[60:63], v[200:203], v[40:43]\n\t" \
    "ds_read_b128 v[84:87], v12 offset:22528\n\t" \
    "s_waitcnt lgkmcnt(5)\n\t" \
    "v_mfma_f32_16x16x32_bf16 v[24:27], v[64:67], v[144:147], v[24:27]\n\t" \
    "v_mfma_f32_16x16x32_bf16 v[44:47], v[64:67], v[200:203], v[44:47]\n\t" \
    "ds_read_b128 v[88:91], v12 offset:23552\n\t" \
    "s_waitcnt lgkmcnt(5)\n\t" \
    "v_mfma_f32_16x16x32_bf16 v[28:31], v[68:71], v[144:147], v[28:31]\n\t" \
    "v_mfma_f32_16x16x32_bf16 v[48:51], v[68:71], v[200:203], v[48:51]\n\t" \
    "ds_read_b128 v[92:95], v12 offset:24576\n\t" \
    "s_waitcnt lgkmcnt(5)\n\t" \
    "v_mfma_f32_16x16x32_bf16 v[32:35], v[72:75], v[144:147], v[32:35]\n\t" \
    "v_mfma_f32_16x16x32_bf16 v[52:55], v[72:75], v[200:203], v[52:55]\n\t" \
    "s_barrier\n\t" \
    "global_load_dwordx2 v[224:225], %[off], %[r0] sc1\n\t" \
    "global_load_dwordx2 v[226:227], %[off], %[r1] sc1\n\t" \
    "global_load_dwordx2 v[228:229], %[off], %[r2] sc1\n\t" \
    "s_waitcnt vmcnt(11)\n\t" \
    "CK9A_%=:\n\t" \
    "v_max3_u32 v8, v148, v149, v150\n\t" \
    "v_max3_u32 v8, v8, v151, v204\n\t" \
    "v_max3_u32 v8, v8, v205, v206\n\t" \
    "v_max_u32 v8, v8, v207\n\t" \
    "v_cmp_eq_u32 vcc, -1, v8\n\t" \
    "s_cbranch_vccz CK9B_%=\n\t" \
    "s_cmp_eq_u32 s70, 0\n\t" \
    "s_cbranch_scc1 CK9B_%=\n\t" \
    "s_sub_u32 s70, s70, 1\n\t" \
    "s_mov_b64 s[68:69], %[hb]\n\t" \
    "s_add_u32 s68, s68, 9216\n\t" \
    "s_addc_u32 s69, s69, 0\n\t" \
    "global_load_dwordx4 v[148:151], %[off], s[68:69] offset:0 sc1\n\t" \
    "global_load_dwordx4 v[152:155], %[off], s[68:69] offset:1024 sc1\n\t" \
    "global_load_dwordx4 v[156:159], %[off], s[68:69] offset:2048 sc1\n\t" \
    "s_add_u32 s68, s68, 55296\n\t" \
    "s_addc_u32 s69, s69, 0\n\t" \
    "global_load_dwordx4 v[204:207], %[off], s[68:69] offset:0 sc1\n\t" \
    "global_load_dwordx4 v[208:211], %[off], s[68:69] offset:1024 sc1\n\t" \
    "global_load_dwordx4 v[212:215], %[off], s[68:69] offset:2048 sc1\n\t" \
    "s_waitcnt vmcnt(0)\n\t" \
    "s_branch CK9A_%=\n\t" \
    "CK9B_%=:\n\t" \
    "ds_read_b128 v[56:59], v13 offset:0\n\t" \
    "s_waitcnt lgkmcnt(5)\n\t" \
    "v_mfma_f32_16x16x32_bf16 v[16:19], v[76:79], v[148:151], v[16:19]\n\t" \
    "v_mfma_f32_16x16x32_bf16 v[36:39], v[76:79], v[204:207], v[36:39]\n\t" \
    "ds_read_b128 v[60:63], v13 offset:1024\n\t" \
    "s_waitcnt lgkmcnt(5)\n\t" \
    "v_mfma_f32_16x16x32_bf16 v[20:23], v[80:83], v[148:151], v[20:23]\n\t" \
    "v_mfma_f32_16x16x32_bf16 v[40:43], v[80:83], v[204:207], v[40:43]\n\t" \
    "ds_read_b128 v[64:67], v13 offset:2048\n\t" \
    "s_waitcnt lgkmcnt(5)\n\t" \
    "v_mfma_f32_16x16x32_bf16 v[24:27], v[84:87], v[148:151], v[24:27]\n\t" \
    "v_mfma_f32_16x16x32_bf16 v[44:47], v[84:87], v[204:207], v[44:47]\n\t" \
    "ds_read_b128 v[68:71], v13 offset:3072\n\t" \
    "s_waitcnt lgkmcnt(5)\n\t" \
    "v_mfma_f32_16x16x32_bf16 v[28:31], v[88:91], v[148:151], v[28:31]\n\t" \
    "v_mfma_f32_16x16x32_bf16 v[48:51], v[88:91], v[204:207], v[48:51]\n\t" \
    "ds_read_b128 v[72:75], v13 offset:4096\n\t" \
    "s_waitcnt lgkmcnt(5)\n\t" \
    "v_mfma_f32_16x16x32_bf16 v[32:35], v[92:95], v[148:151], v[32:35]\n\t" \
    "v_mfma_f32_16x16x32_bf16 v[52:55], v[92:95], v[204:207], v[52:55]\n\t" \
    "s_waitcnt vmcnt(9)\n\t" \
    "CK10A_%=:\n\t" \
    "v_max3_u32 v8, v152, v153, v154\n\t" \
    "v_max3_u32 v8, v8, v155, v208\n\t" \
    "v_max3_u32 v8, v8, v209, v210\n\t" \
    "v_max_u32 v8, v8, v211\n\t" \
    "v_cmp_eq_u32 vcc, -1, v8\n\t" \
    "s_cbranch_vccz CK10B_%=\n\t" \
    "s_cmp_eq_u32 s70, 0\n\t" \
    "s_cbranch_scc1 CK10B_%=\n\t" \
    "s_sub_u32 s70, s70, 1\n\t" \
    "s_mov_b64 s[68:69], %[hb]\n\t" \
    "s_add_u32 s68, s68, 10240\n\t" \
    "s_addc_u32 s69, s69, 0\n\t" \
    "global_load_dwordx4 v[152:155], %[off], s[68:69] offset:0 sc1\n\t" \
    "global_load_dwordx4 v[156:159], %[off], s[68:69] offset:1024 sc1\n\t" \
    "global_load_dwordx4 v[160:163], %[off], s[68:69] offset:2048 sc1\n\t" \
    "s_add_u32 s68, s68, 55296\n\t" \
    "s_addc_u32 s69, s69, 0\n\t" \
    "global_load_dwordx4 v[208:211], %[off], s[68:69] offset:0 sc1\n\t" \
    "global_load_dwordx4 v[212:215], %[off], s[68:69] offset:1024 sc1\n\t" \
    "global_load_dwordx4 v[216:219], %[off], s[68:69] offset:2048 sc1\n\t" \
    "s_waitcnt vmcnt(0)\n\t" \
    "s_branch CK10A_%=\n\t" \
    "CK10B_%=:\n\t" \
    "ds_read_b128 v[76:79], v13 offset:20480\n\t" \
    "s_waitcnt lgkmcnt(5)\n\t" \
    "v_mfma_f32_16x16x32_bf16 v[16:19], v[56:59], v[152:155], v[16:19]\n\t" \
    "v_mfma_f32_16x16x32_bf16 v[36:39], v[56:59], v[208:211], v[36:39]\n\t" \
    "ds_read_b128 v[80:83], v13 offset:21504\n\t" \
    "s_waitcnt lgkmcnt(5)\n\t" \
    "v_mfma_f32_16x16x32_bf16 v[20:23], v[60:63], v[152:155], v[20:23]\n\t" \
    "v_mfma_f32_16x16x32_bf16 v[40:43], v[60:63], v[208:211], v[40:43]\n\t" \
    "ds_read_b128 v[84:87], v13 offset:22528\n\t" \
    "s_waitcnt lgkmcnt(5)\n\t" \
    "v_mfma_f32_16x16x32_bf16 v[24:27], v[64:67], v[152:155], v[24:27]\n\t" \
    "v_mfma_f32_16x16x32_bf16 v[44:47], v[64:67], v[208:211], v[44:47]\n\t" \
    "ds_read_b128 v[88:91], v13 offset:23552\n\t" \
    "s_waitcnt lgkmcnt(5)\n\t" \
    "v_mfma_f32_16x16x32_bf16 v[28:31], v[68:71], v[152:155], v[28:31]\n\t" \
    "v_mfma_f32_16x16x32_bf16 v[48:51], v[68:71], v[208:211], v[48:51]\n\t" \
    "ds_read_b128 v[92:95], v13 offset:24576\n\t" \
    "s_waitcnt lgkmcnt(5)\n\t" \
    "v_mfma_f32_16x16x32_bf16 v[32:35], v[72:75], v[152:155], v[32:35]\n\t" \
    "v_mfma_f32_16x16x32_bf16 v[52:55], v[72:75], v[208:211], v[52:55]\n\t" \
    "s_barrier\n\t" \
    "s_waitcnt vmcnt(7)\n\t" \
    "CK11A_%=:\n\t" \
    "v_max3_u32 v8, v156, v157, v158\n\t" \
    "v_max3_u32 v8, v8, v159, v212\n\t" \
    "v_max3_u32 v8, v8, v213, v214\n\t" \
    "v_max_u32 v8, v8, v215\n\t" \
    "v_cmp_eq_u32 vcc, -1, v8\n\t" \
    "s_cbranch_vccz CK11B_%=\n\t" \
    "s_cmp_eq_u32 s70, 0\n\t" \
    "s_cbranch_scc1 CK11B_%=\n\t" \
    "s_sub_u32 s70, s70, 1\n\t" \
    "s_mov_b64 s[68:69], %[hb]\n\t" \
    "s_add_u32 s68, s68, 11264\n\t" \
    "s_addc_u32 s69, s69, 0\n\t" \
    "global_load_dwordx4 v[156:159], %[off], s[68:69] offset:0 sc1\n\t" \
    "global_load_dwordx4 v[160:163], %[off], s[68:69] offset:1024 sc1\n\t" \
    "global_load_dwordx4 v[164:167], %[off], s[68:69] offset:2048 sc1\n\t" \
    "s_add_u32 s68, s68, 55296\n\t" \
    "s_addc_u32 s69, s69, 0\n\t" \
    "global_load_dwordx4 v[212:215], %[off], s[68:69] offset:0 sc1\n\t" \
    "global_load_dwordx4 v[216:219], %[off], s[68:69] offset:1024 sc1\n\t" \
    "global_load_dwordx4 v[220:223], %[off], s[68:69] offset:2048 sc1\n\t" \
    "s_waitcnt vmcnt(0)\n\t" \
    "s_branch CK11A_%=\n\t" \
    "CK11B_%=:\n\t" \
    "ds_read_b128 v[56:59], v14 offset:0\n\t" \
    "s_waitcnt lgkmcnt(5)\n\t" \
    "v_mfma_f32_16x16x32_bf16 v[16:19], v[76:79], v[156:159], v[16:19]\n\t" \
    "v_mfma_f32_16x16x32_bf16 v[36:39], v[76:79], v[212:215], v[36:39]\n\t" \
    "ds_read_b128 v[60:63], v14 offset:1024\n\t" \
    "s_waitcnt lgkmcnt(5)\n\t" \
    "v_mfma_f32_16x16x32_bf16 v[20:23], v[80:83], v[156:159], v[20:23]\n\t" \
    "v_mfma_f32_16x16x32_bf16 v[40:43], v[80:83], v[212:215], v[40:43]\n\t" \
    "ds_read_b128 v[64:67], v14 offset:2048\n\t" \
    "s_waitcnt lgkmcnt(5)\n\t" \
    "v_mfma_f32_16x16x32_bf16 v[24:27], v[84:87], v[156:159], v[24:27]\n\t" \
    "v_mfma_f32_16x16x32_bf16 v[44:47], v[84:87], v[212:215], v[44:47]\n\t" \
    "ds_read_b128 v[68:71], v14 offset:3072\n\t" \
    "s_waitcnt lgkmcnt(5)\n\t" \
    "v_mfma_f32_16x16x32_bf16 v[28:31], v[88:91], v[156:159], v[28:31]\n\t" \
    "v_mfma_f32_16x16x32_bf16 v[48:51], v[88:91], v[212:215], v[48:51]\n\t" \
    "ds_read_b128 v[72:75], v14 offset:4096\n\t" \
    "s_waitcnt lgkmcnt(5)\n\t" \
    "v_mfma_f32_16x16x32_bf16 v[32:35], v[92:95], v[156:159], v[32:35]\n\t" \
    "v_mfma_f32_16x16x32_bf16 v[52:55], v[92:95], v[212:215], v[52:55]\n\t" \
    "s_waitcnt vmcnt(5)\n\t" \
    "CK12A_%=:\n\t" \
    "v_max3_u32 v8, v160, v161, v162\n\t" \
    "v_max3_u32 v8, v8, v163, v216\n\t" \
    "v_max3_u32 v8, v8, v217, v218\n\t" \
    "v_max_u32 v8, v8, v219\n\t" \
    "v_cmp_eq_u32 vcc, -1, v8\n\t" \
    "s_cbranch_vccz CK12B_%=\n\t" \
    "s_cmp_eq_u32 s70, 0\n\t" \
    "s_cbranch_scc1 CK12B_%=\n\t" \
    "s_sub_u32 s70, s70, 1\n\t" \
    "s_mov_b64 s[68:69], %[hb]\n\t" \
    "s_add_u32 s68, s68, 12288\n\t" \
    "s_addc_u32 s69, s69, 0\n\t" \
    "global_load_dwordx4 v[160:163], %[off], s[68:69] offset:0 sc1\n\t" \
    "global_load_dwordx4 v[164:167], %[off], s[68:69] offset:1024 sc1\n\t" \
    "s_add_u32 s68, s68, 55296\n\t" \
    "s_addc_u32 s69, s69, 0\n\t" \
    "global_load_dwordx4 v[216:219], %[off], s[68:69] offset:0 sc1\n\t" \
    "global_load_dwordx4 v[220:223], %[off], s[68:69] offset:1024 sc1\n\t" \
    "s_waitcnt vmcnt(0)\n\t" \
    "s_branch CK12A_%=\n\t" \
    "CK12B_%=:\n\t" \
    "ds_read_b128 v[76:79], v14 offset:20480\n\t" \
    "s_waitcnt lgkmcnt(5)\n\t" \
    "v_mfma_f32_16x16x32_bf16 v[16:19], v[56:59], v[160:163], v[16:19]\n\t" \
    "v_mfma_f32_16x16x32_bf16 v[36:39], v[56:59], v[216:219], v[36:39]\n\t" \
    "ds_read_b128 v[80:83], v14 offset:21504\n\t" \
    "s_waitcnt lgkmcnt(5)\n\t" \
    "v_mfma_f32_16x16x32_bf16 v[20:23], v[60:63], v[160:163], v[20:23]\n\t" \
    "v_mfma_f32_16x16x32_bf16 v[40:43], v[60:63], v[216:219], v[40:43]\n\t" \
    "ds_read_b128 v[84:87], v14 offset:22528\n\t" \
    "s_waitcnt lgkmcnt(5)\n\t" \
    "v_mfma_f32_16x16x32_bf16 v[24:27], v[64:67], v[160:163], v[24:27]\n\t" \
    "v_mfma_f32_16x16x32_bf16 v[44:47], v[64:67], v[216:219], v[44:47]\n\t" \
    "ds_read_b128 v[88:91], v14 offset:23552\n\t" \
    "s_waitcnt lgkmcnt(5)\n\t" \
    "v_mfma_f32_16x16x32_bf16 v[28:31], v[68:71], v[160:163], v[28:31]\n\t" \
    "v_mfma_f32_16x16x32_bf16 v[48:51], v[68:71], v[216:219], v[48:51]\n\t" \
    "ds_read_b128 v[92:95], v14 offset:24576\n\t" \
    "s_waitcnt lgkmcnt(5)\n\t" \
    "v_mfma_f32_16x16x32_bf16 v[32:35], v[72:75], v[160:163], v[32:35]\n\t" \
    "v_mfma_f32_16x16x32_bf16 v[52:55], v[72:75], v[216:219], v[52:55]\n\t" \
    "s_cmp_eq_u32 %[skip], 0\n\t" \
    "s_cbranch_scc0 DNEND_%=\n\t" \
    "s_waitcnt vmcnt(3)\n\t" \
    "CK13A_%=:\n\t" \
    "v_max3_u32 v8, v164, v165, v166\n\t" \
    "v_max3_u32 v8, v8, v167, v220\n\t" \
    "v_max3_u32 v8, v8, v221, v222\n\t" \
    "v_max_u32 v8, v8, v223\n\t" \
    "v_cmp_eq_u32 vcc, -1, v8\n\t" \
    "s_cbranch_vccz CK13B_%=\n\t" \
    "s_cmp_eq_u32 s70, 0\n\t" \
    "s_cbranch_scc1 CK13B_%=\n\t" \
    "s_sub_u32 s70, s70, 1\n\t" \
    "s_mov_b64 s[68:69], %[hb]\n\t" \
    "s_add_u32 s68, s68, 13312\n\t" \
    "s_addc_u32 s69, s69, 0\n\t" \
    "global_load_dwordx4 v[164:167], %[off], s[68:69] offset:0 sc1\n\t" \
    "s_add_u32 s68, s68, 55296\n\t" \
    "s_addc_u32 s69, s69, 0\n\t" \
    "global_load_dwordx4 v[220:223], %[off], s[68:69] offset:0 sc1\n\t" \
    "s_waitcnt vmcnt(0)\n\t" \
    "s_branch CK13A_%=\n\t" \
    "CK13B_%=:\n\t" \
    "s_waitcnt lgkmcnt(4)\n\t" \
    "v_mfma_f32_16x16x32_bf16 v[16:19], v[76:79], v[164:167], v[16:19]\n\t" \
    "v_mfma_f32_16x16x32_bf16 v[36:39], v[76:79], v[220:223], v[36:39]\n\t" \
    "s_waitcnt lgkmcnt(3)\n\t" \
    "v_mfma_f32_16x16x32_bf16 v[20:23], v[80:83], v[164:167], v[20:23]\n\t" \
    "v_mfma_f32_16x16x32_bf16 v[40:43], v[80:83], v[220:223], v[40:43]\n\t" \
    "s_waitcnt lgkmcnt(2)\n\t" \
    "v_mfma_f32_16x16x32_bf16 v[24:27], v[84:87], v[164:167], v[24:27]\n\t" \
    "v_mfma_f32_16x16x32_bf16 v[44:47], v[84:87], v[220:223], v[44:47]\n\t" \
    "s_waitcnt lgkmcnt(1)\n\t" \
    "v_mfma_f32_16x16x32_bf16 v[28:31], v[88:91], v[164:167], v[28:31]\n\t" \
    "v_mfma_f32_16x16x32_bf16 v[48:51], v[88:91], v[220:223], v[48:51]\n\t" \
    "s_waitcnt lgkmcnt(0)\n\t" \
    "v_mfma_f32_16x16x32_bf16 v[32:35], v[92:95], v[164:167], v[32:35]\n\t" \
    "v_mfma_f32_16x16x32_bf16 v[52:55], v[92:95], v[220:223], v[52:55]\n\t" \
    "DNEND_%=:\n\t" \
    "s_waitcnt vmcnt(0) lgkmcnt(0)\n\t" \
    "s_nop 15\n\t"
#define DN_MAIN_CLOBBERS "v8", "v9", "v10", "v11", "v12", "v13", "v14", "v15", "v56", "v57", "v58", "v59", "v60", "v61", "v62", "v63", "v64", "v65", "v66", "v67", "v68", "v69", "v70", "v71", "v72", "v73", "v74", "v75", "v76", "v77", "v78", "v79", "v80", "v81", "v82", "v83", "v84", "v85", "v86", "v87", "v88", "v89", "v90", "v91", "v92", "v93", "v94", "v95", "v96", "v97", "v98", "v99", "v100", "v101", "v102", "v103", "v104", "v105", "v106", "v107", "v108", "v109", "v110", "v111", "v112", "v113", "v114", "v115", "v116", "v117", "v118", "v119", "v120", "v121", "v122", "v123", "v124", "v125", "v126", "v127", "v128", "v129", "v130", "v131", "v132", "v133", "v134", "v135", "v136", "v137", "v138", "v139", "v140", "v141", "v142", "v143", "v144", "v145", "v146", "v147", "v148", "v149", "v150", "v151", "v152", "v153", "v154", "v155", "v156", "v157", "v158", "v159", "v160", "v161", "v162", "v163", "v164", "v165", "v166", "v167", "v168", "v169", "v170", "v171", "v172", "v173", "v174", "v175", "v176", "v177", "v178", "v179", "v180", "v181", "v182", "v183", "v184", "v185", "v186", "v187", "v188", "v189", "v190", "v191", "v192", "v193", "v194", "v195", "v196", "v197", "v198", "v199", "v200", "v201", "v202", "v203", "v204", "v205", "v206", "v207", "v208", "v209", "v210", "v211", "v212", "v213", "v214", "v215", "v216", "v217", "v218", "v219", "v220", "v221", "v222", "v223", "v240", "v241", "v242", "v243", "v244", "v245", "v246", "v247", "v248", "v249", "v250", "v251", "v252", "v253", "v254", "v255", "s60", "s61", "s62", "s63", "s64", "s65", "s68", "s69", "s70", "vcc", "scc", "memory"

constexpr int XT_STRIDE = 1296;
constexpr int L_XT = 0, L_WKV = 64 * XT_STRIDE, L_RING = L_WKV + 25600, L_SLOT = 128 * 192, L_RED = L_RING + 2 * L_SLOT;
constexpr int L_QX = L_RING;
constexpr int L_WO = 0, L_SCR = 102400, L_RT = 122880, L_CTL = 163776;
constexpr int A_TILE = 3 * 1024 + 256;
constexpr int L_EX = 122880, L_STAT = 153600;
constexpr int LDS_BYTES = 163840;
constexpr int CODE_LINES = 340;
static_assert(L_RED + 6144 <= LDS_BYTES && L_SCR + 16 * A_TILE <= L_CTL && L_QX + 12288 <= L_RT, "lds");

__global__ void __launch_bounds__(512, 2) k_block(const u16* __restrict__ x, const u16* __restrict__ wq, const u16* __restrict__ wk, const u16* __restrict__ wv, const u16* __restrict__ wo,
                                                  const u16* __restrict__ wg, const u16* __restrict__ wu, const u16* __restrict__ wd, char* __restrict__ ws, u16* __restrict__ out) {
    extern __shared__ __attribute__((aligned(16))) char smem[];
    const ldsp lds = (ldsp)smem; const unsigned lds0 = (unsigned)(uintptr_t)smem;
    const int tid = threadIdx.x, lane = tid & 63, w = __builtin_amdgcn_readfirstlane(tid >> 6);
    const int b = blockIdx.x, x8 = b & 7, r32 = b >> 3;
    const int tt = x8 * 4 + (r32 >> 3), hg = r32 & 7;
    const int G = (x8 >> 1) * 4 + (r32 >> 3), nt = (x8 & 1) * 7 + hg;
    const __amdgpu_buffer_rsrc_t rs = __builtin_amdgcn_make_buffer_rsrc((void*)ws, 0, (int)WS_END, 0x00020000);
    char* flg = ws + WS_FLG;
    STAMP(0);
    const int row = w * 8 + (lane >> 3);
    u32x4 v[10]; u32x2 nonce2;
    { const char* xp = (const char*)x + (size_t)(tt * 64 + row) * (D * 2) + (lane & 7) * 16; const char* np = flg + FL_NONCE;
      asm volatile("global_load_dwordx2 %10, %12, off sc1\n\t"
                   "global_load_dwordx4 %0, %11, off\n\tglobal_load_dwordx4 %1, %11, off offset:128\n\tglobal_load_dwordx4 %2, %11, off offset:256\n\tglobal_load_dwordx4 %3, %11, off offset:384\n\t"
                   "global_load_dwordx4 %4, %11, off offset:512\n\tglobal_load_dwordx4 %5, %11, off offset:640\n\tglobal_load_dwordx4 %6, %11, off offset:768\n\tglobal_load_dwordx4 %7, %11, off offset:896\n\t"
                   "global_load_dwordx4 %8, %11, off offset:1024\n\tglobal_load_dwordx4 %9, %11, off offset:1152\n\ts_barrier"
                   : "=&v"(v[0]), "=&v"(v[1]), "=&v"(v[2]), "=&v"(v[3]), "=&v"(v[4]), "=&v"(v[5]), "=&v"(v[6]), "=&v"(v[7]), "=&v"(v[8]), "=&v"(v[9]), "=&v"(nonce2) : "v"(xp), "v"(np) : "memory"); }
    const u16* wkv = (hg & 1) ? wv : wk;
#pragma unroll
    for (int i = 0; i < 4; ++i) { const int ins = w + 8 * i < 25 ? w + 8 * i : 24; glds16((const char*)wkv + ins * 1024 + lane * 16, lds0 + L_WKV + ins * 1024); }
    unsigned wqoff[3];
#pragma unroll
    for (int i = 0; i < 3; ++i) { const int p = (3 * w + i) * 64 + lane, r = p / 12; int c = p - r * 12; if (c >= 10) c -= 2; wqoff[i] = (unsigned)(r * (D * 2) + hg * 160 + c * 16); }
#define F_DMAQ(c, slotoff) do { _Pragma("unroll") for (int i_ = 0; i_ < 3; ++i_) glds16((const char*)wq + (size_t)(c) * 128 * D * 2 + wqoff[i_], lds0 + (slotoff) + (3 * w + i_) * 1024); } while (0)
    F_DMAQ(0, L_RING); F_DMAQ(1, L_RING + L_SLOT);
    unsigned tall;
    { const int ln = (b * 8 + w) * 35 + (lane < 35 ? lane : 34); const char* p;
      if (ln < 6400) p = (const char*)wq + ln * 128; else if (ln < 12800) p = (const char*)wo + (ln - 6400) * 128; else if (ln < 30080) p = (const char*)wg + (ln - 12800) * 128;
      else if (ln < 47360) p = (const char*)wu + (ln - 30080) * 128; else if (ln < 64640) p = (const char*)wd + (ln - 47360) * 128; else if (ln < 67040) p = ws + (ln - 64640) * 128; else p = (const char*)wq;
      asm volatile("global_load_dword %0, %1, off" : "=&v"(tall) : "v"(p) : "memory"); }
    asm volatile("s_waitcnt vmcnt(11)" : "+v"(v[0]), "+v"(v[1]), "+v"(v[2]), "+v"(v[3]), "+v"(v[4]), "+v"(v[5]), "+v"(v[6]), "+v"(v[7]), "+v"(v[8]), "+v"(v[9]), "+v"(nonce2) :: "memory");
    const u64 nonce = ((u64)(unsigned)__builtin_amdgcn_readfirstlane(nonce2[1]) << 32) | (unsigned)__builtin_amdgcn_readfirstlane(nonce2[0]);
    constexpr u64 M48 = (1ull << 48) - 1;
#define TAG(ph) ((nonce + (ph)) & M48)
    {
        float s0 = 0.f, s1 = 0.f, q0 = 0.f, q1 = 0.f;
#pragma unroll
        for (int i = 0; i < 10; ++i) {
            s0 = dot2(v[i][0], 0x3F803F80u, s0); s1 = dot2(v[i][1], 0x3F803F80u, s1); s0 = dot2(v[i][2], 0x3F803F80u, s0); s1 = dot2(v[i][3], 0x3F803F80u, s1);
            q0 = dot2(v[i][0], v[i][0], q0); q1 = dot2(v[i][1], v[i][1], q1); q0 = dot2(v[i][2], v[i][2], q0); q1 = dot2(v[i][3], v[i][3], q1); }
        const float mean = oct_sum(s0 + s1) * (1.f / D);
        const float var = fmaxf(oct_sum(q0 + q1) * (1.f / D) - mean * mean, 0.f);
        const float rstd = __builtin_amdgcn_rsqf(var + LN_EPS), nmr = -mean * rstd;
        const f32x2 r2 = {rstd, rstd}, n2 = {nmr, nmr};
#pragma unroll
        for (int i = 0; i < 10; ++i) { u32x4 o;
#pragma unroll
            for (int j = 0; j < 4; ++j) { const f32x2 t = __builtin_elementwise_fma(f32x2{bflo(v[i][j]), bfhi(v[i][j])}, r2, n2); o[j] = pk2(t[0], t[1]); }
            *(LAS u32x4*)(lds + L_XT + row * XT_STRIDE + ((lane & 7) + 8 * i) * 16) = o; }
    }
    STAMP(1);
    WAITV(1);
    BAR();
    STAMP(2);
    unsigned tch0 = 0;
    if (w >= 4) { const char* pc = (const char*)__builtin_amdgcn_s_getpc() + (size_t)(min((r32 * 4 + w - 4) * 4 + (lane & 3), CODE_LINES - 1) * 128);
        asm volatile("global_load_dword %0, %1, off" : "=&v"(tch0) : "v"(pc) : "memory"); }
    const int hi = w & 3, tj = w >> 2;
    const int aoff = (8 * (lane >> 5) + ((lane & 15) >> 2)) * 192 + hi * 40 + ((lane >> 4) & 1) * 32 + (lane & 3) * 8;
    const int tk = hg >> 1; const bool isV = hg & 1;
    f32x4 kv0 = {0.f, 0.f, 0.f, 0.f}, kv1 = kv0;
    if (w < 4) {
        const int g = lane >> 4, q = (lane & 15) >> 2, p = lane & 3;
        const ldscp xb = lds + L_XT + (tk * 16 + (lane & 15)) * XT_STRIDE + g * 16;
        const ldscp wb = lds + L_WKV + (8 * g + q) * 40 + p * 8;
#pragma unroll
        for (int s = 0; s < 5; ++s) { const int k0 = w * 160 + s * 32;
            const bf16x8 xf = ldsr16(xb + k0 * 2);
            const bf16x8 w0 = cat8(trd(wb + k0 * 40), trd(wb + k0 * 40 + 160));
            const bf16x8 w1 = cat8(trd(wb + k0 * 40 + 32), trd(wb + k0 * 40 + 160 + 32));
            if (isV) { kv0 = MFMA16(xf, w0, kv0); kv1 = MFMA16(xf, w1, kv1); }
            else     { kv0 = MFMA16(w0, xf, kv0); kv1 = MFMA16(w1, xf, kv1); } }
        if (w > 0) { *(LAS f32x4*)(lds + L_RED + ((w - 1) * 2 + 0) * 1024 + lane * 16) = kv0; *(LAS f32x4*)(lds + L_RED + ((w - 1) * 2 + 1) * 1024 + lane * 16) = kv1; }
    }
    bf16x8 wf[16];
#pragma unroll
    for (int s = 0; s < 16; ++s) { const ldscp ab = lds + L_RING + (s >> 3) * L_SLOT + aoff + (s & 7) * 16 * 192; wf[s] = cat8(trd(ab), trd(ab + 4 * 192)); }
#pragma unroll
    for (int s = 0; s < 16; ++s) asm volatile("" : "+v"(wf[s]));
    BAR();
    if (w > 0) { F_DMAQ(2, L_RING); F_DMAQ(3, L_RING + L_SLOT); F_DMAQ(4, L_WKV); }
    else {
#pragma unroll
        for (int i = 0; i < 3; ++i) { kv0 += *(const LAS f32x4*)(lds + L_RED + (i * 2 + 0) * 1024 + lane * 16); kv1 += *(const LAS f32x4*)(lds + L_RED + (i * 2 + 1) * 1024 + lane * 16); }
        const int g = lane >> 4, c16 = lane & 15, tok0 = tt * 64 + tk * 16, t32 = tok0 >> 5;
        const u32x2 p0 = {pk2(kv0[0], kv0[1]), pk2(kv0[2], kv0[3])};
        u32x2 p1 = {pk2(kv1[0], kv1[1]), pk2(kv1[2], kv1[3])};
        unsigned lo = 0;
        if (!isV) {
            const int kk = (tok0 & 31) + c16; const int kf = (int)WS_KF + t32 * 1536;
            __builtin_amdgcn_raw_buffer_store_b64(p0, rs, kf + ((g & 1) * 32 + kk) * 16 + (g >> 1) * 8, 0, SC1);
            float n2 = dot2(p0[0], p0[0], dot2(p0[1], p0[1], 0.f));
            if (g == 0) n2 = dot2(p1[0], p1[0], dot2(p1[1], p1[1], n2));
            if (g == 2) { p1[0] = 0x00003F80u; p1[1] = 0u; }
            if ((g & 1) == 0) __builtin_amdgcn_raw_buffer_store_b64(p1, rs, kf + 1024 + ((g >> 1) * 32 + kk) * 8, 0, SC1);
            n2 = row16_max(x32_sum(x16_sum(n2)));
            lo = (__float_as_uint(n2) >> 16) + 1u;
        } else {
            const int vf = (int)WS_VF + t32 * 2048 + (tk & 1) * 1024 + (g >> 1) * 8;
            __builtin_amdgcn_raw_buffer_store_b64(p0, rs, vf + ((g & 1) * 32 + c16) * 16, 0, SC1);
            if (c16 == 4) { p1[0] = 0x3F803F80u; p1[1] = 0x3F803F80u; } else if (c16 > 4) { p1[0] = 0u; p1[1] = 0u; }
            __builtin_amdgcn_raw_buffer_store_b64(p1, rs, vf + ((g & 1) * 32 + 16 + c16) * 16, 0, SC1);
        }
        DRAIN();
        if (lane == 0) set_flag(flg + FL_KV + b * 8, TAG(1), lo);
        F_DMAQ(2, L_RING); F_DMAQ(3, L_RING + L_SLOT); F_DMAQ(4, L_WKV);
    }
    STAMP(3);
    const char* rt_s; unsigned rt_l8; int rt_d[4], rt_c;
    {
        int u = b * 8 + w; if (u >= 1620) u -= 428;
        if (u < 1080) { const int m = u >= 540, uu = u - 540 * m, ks = uu / 27, cb = uu - ks * 27; rt_s = (const char*)((m ? wu : wg) + (size_t)ks * 32 * F + cb * 64); rt_l8 = F * 2; rt_c = (int)WS_CS + (cb * 8 + m) * 1280 + ks * 64;
#pragma unroll
            for (int n = 0; n < 4; ++n) rt_d[n] = (int)WS_WGU + ((cb * 4 + n) * 40 + ks * 2 + m) * 1024; }
        else { const int uu = u - 1080, ks = uu / 10, cb = uu - ks * 10, kh2 = ks >= 27; rt_s = (const char*)(wd + (size_t)ks * 32 * D + cb * 64); rt_l8 = D * 2; rt_c = -1;
#pragma unroll
            for (int n = 0; n < 4; ++n) { const int n16 = cb * 4 + n, ng2 = n16 / 5, nn = n16 - ng2 * 5; rt_d[n] = (int)WS_WD + (((ng2 * 2 + kh2) * 27 + ks - 27 * kh2) * 5 + nn) * 1024; } }
    }
    u64 pf0, pf1, pf2, pf3;
    unsigned tch1 = 0, tch2 = 0;
#define TOUCH(dst, ww, rs_, rl_) do { const char* tp_ = lane < 32 ? (rs_) + (size_t)lane * (rl_) : (const char*)wo + ((r32 * 8 + (ww)) * 25 + (lane < 57 ? lane - 32 : 24)) * 128; \
        asm volatile("global_load_dword %0, %1, off" : "=&v"(dst) : "v"(tp_) : "memory"); } while (0)
    asm volatile("s_barrier" ::: "memory");
    if (w < 7) TOUCH(tch1, w, rt_s, rt_l8);
    if (w == 6) { int u = b * 8 + 7; if (u >= 1620) u -= 428; const char* s7; unsigned l7;
        if (u < 1080) { const int m = u >= 540, uu = u - 540 * m, ks = uu / 27, cb = uu - ks * 27; s7 = (const char*)((m ? wu : wg) + (size_t)ks * 32 * F + cb * 64); l7 = F * 2; }
        else { const int uu = u - 1080, ks = uu / 10, cb = uu - ks * 10; s7 = (const char*)(wd + (size_t)ks * 32 * D + cb * 64); l7 = D * 2; }
        TOUCH(tch2, 7, s7, l7); }
    {
        f32x16 acc = {};
        const ldscp xb = lds + L_XT + (tj * 32 + (lane & 31)) * XT_STRIDE + (lane >> 5) * 16;
#pragma unroll
        for (int s = 0; s < 16; ++s) acc = MFMA32(wf[s], ldsr16(xb + s * 32), acc);
        if (w == 7) WAITV(0); else if (w == 6) WAITV(2); else WAITV(1);
        if (w == 7) { const char* fp = flg + FL_KV + lane * 8;
            asm volatile("global_load_dwordx2 %0, %4, off sc1\n\tglobal_load_dwordx2 %1, %4, off offset:512 sc1\n\tglobal_load_dwordx2 %2, %4, off offset:1024 sc1\n\tglobal_load_dwordx2 %3, %4, off offset:1536 sc1"
                         : "=&v"(pf0), "=&v"(pf1), "=&v"(pf2), "=&v"(pf3) : "v"(fp) : "memory"); }
        BAR();
        asm volatile("" :: "v"(tch0), "v"(tall));
        { const unsigned ab = lds0 + L_RING + aoff, a4 = lds0 + L_WKV + aoff, xa = lds0 + L_XT + (tj * 32 + (lane & 31)) * XT_STRIDE + (lane >> 5) * 16;
          asm volatile(QP_ASM : "+{v[16:31]}"(acc) : [ab] "v"(ab), [a4] "v"(a4), [xb] "v"(xa) : QB_CLOBBERS); }
        u32x4 m; m[0] = pk2(acc[0] * QSCALE, acc[1] * QSCALE); m[1] = pk2(acc[2] * QSCALE, acc[3] * QSCALE); m[2] = pk2(acc[4] * QSCALE, acc[5] * QSCALE); m[3] = pk2(acc[6] * QSCALE, acc[7] * QSCALE);
        u32x2 rr; rr[0] = pk2(acc[8] * QSCALE, acc[9] * QSCALE); rr[1] = pk2(acc[10] * QSCALE, acc[11] * QSCALE);
        *(LAS u32x4*)(lds + L_QX + w * 1536 + lane * 16) = m; *(LAS u32x2*)(lds + L_QX + w * 1536 + 1024 + lane * 8) = rr;
        float n2 = dot2(m[0], m[0], dot2(m[1], m[1], dot2(m[2], m[2], dot2(m[3], m[3], 0.f))));
        if (lane < 32) n2 = dot2(rr[0], rr[0], dot2(rr[1], rr[1], n2));
        n2 = x16_max(row16_max(x32_sum(n2)));
        if (lane == 0) *(LAS float*)(lds + L_CTL + 16 + w * 4) = n2;
    }
    STAMP(4);
    if (w == 7) {
        asm volatile("s_waitcnt vmcnt(0)" : "+v"(pf0), "+v"(pf1), "+v"(pf2), "+v"(pf3) :: "memory");
        const u64 tg = TAG(1); unsigned mx;
        if (__all((pf0 >> 16) == tg && (pf1 >> 16) == tg && (pf2 >> 16) == tg && (pf3 >> 16) == tg)) {
            const unsigned a0 = (unsigned)pf0 & 0xffffu, a1 = (unsigned)pf1 & 0xffffu, a2 = (unsigned)pf2 & 0xffffu, a3 = (unsigned)pf3 & 0xffffu;
            mx = max(max(a0, a1), max(a2, a3)); BEHIND_POLL();
        } else mx = poll_flags<4>(flg + FL_KV, lane, 256, tg);
        float km = __uint_as_float(mx << 16); km = x32_max(x16_max(row16_max(km)));
        if (lane == 0) *(LAS float*)(lds + L_CTL) = km;
    }
    BAR();
    {
    const int lane = fresh_lane();
    const int rg = w & 1, kp = w >> 1, half = lane >> 5;
    const float kmax2 = *(const LAS float*)(lds + L_CTL);
    bool slow;
    { const f32x4 ma = *(const LAS f32x4*)(lds + L_CTL + 16), mb4 = *(const LAS f32x4*)(lds + L_CTL + 32);
      const float mq = fmaxf(fmaxf(fmaxf(ma[0], ma[1]), fmaxf(ma[2], ma[3])), fmaxf(fmaxf(mb4[0], mb4[1]), fmaxf(mb4[2], mb4[3])));
      slow = __builtin_amdgcn_readfirstlane(!(__builtin_amdgcn_sqrtf(mq * kmax2) * 1.01f + 1e-3f <= 50.f)) != 0; }
    const char* kb = ws + WS_KF + (size_t)kp * 16 * 1536; const char* vb = ws + WS_VF + (size_t)kp * 16 * 2048;
    const unsigned o16 = lane * 16, o8 = lane * 8;
    f32x16 zk0, zk1, zk2; f32x8 zk3;
    if (!slow) asm volatile(ATTN_PRO_ASM : [kb] "+s"(kb), [vb] "+s"(vb), "=&{v[200:215]}"(zk0), "=&{v[216:231]}"(zk1), "=&{v[232:247]}"(zk2), "=&{v[248:255]}"(zk3) : [o16] "v"(o16), [o8] "v"(o8) : "s40", "s41", "s42", "s43", "scc", "memory");
    u32x4 qm4[4]; u32x2 qr2[4];
#pragma unroll
    for (int qi = 0; qi < 4; ++qi) { const int pw = (qi & 1) * 4 + rg * 2 + (qi >> 1);
        qm4[qi] = *(const LAS u32x4*)(lds + L_QX + pw * 1536 + lane * 16); qr2[qi] = *(const LAS u32x2*)(lds + L_QX + pw * 1536 + 1024 + lane * 8); }
    const unsigned rt_v = (unsigned)((lane >> 3) * rt_l8 + (lane & 7) * 16), rt_l88 = rt_l8 * 8;
    const char* fill_s; { constexpr int FILL = (int)(WS_WGU - WS_OF); const int o6 = (b * 8 + w) * 6080; fill_s = ws + WS_OF + (o6 < FILL - 6144 ? o6 : FILL - 6144); }
    const char* wo_s = (const char*)wo + hg * 160;
    const unsigned wo_v = (unsigned)((lane / 10) * (D * 2) + (lane % 10) * 16);
    float qn2[4];
#pragma unroll
    for (int qi = 0; qi < 4; ++qi) { const u32x4 m = qm4[qi]; const u32x2 r = qr2[qi];
        float n2 = dot2(m[0], m[0], dot2(m[1], m[1], dot2(m[2], m[2], dot2(m[3], m[3], 0.f))));
        if (!half) n2 = dot2(r[0], r[0], dot2(r[1], r[1], n2));
        qn2[qi] = x32_sum(n2); }
    STAMP(5);
    bf16x8 qm[4]; s16x4 qr[4]; f32x4 mrow;
#pragma unroll
    for (int qi = 0; qi < 4; ++qi) {
        u32x2 r = qr2[qi];
        const float bound = __builtin_amdgcn_sqrtf(qn2[qi] * kmax2) * 1.01f + 1e-3f;
        const unsigned mb = pk2(-bound, 0.f) & 0xffffu;
        mrow[qi] = -bflo(mb);
        if (half) { r[0] = mb; r[1] = 0u; }
        qm[qi] = __builtin_bit_cast(bf16x8, qm4[qi]); qr[qi] = __builtin_bit_cast(s16x4, r);
    }
    f32x16 o[4];
#pragma unroll
    for (int qi = 0; qi < 4; ++qi) o[qi] = f32x16{};
    const unsigned rt_m = lds0 + L_RT + w * 4096;
#define RETILE_OUT() do { const int l_ = fresh_lane(), g_ = l_ >> 4; const ldscp tb_ = lds + L_RT + w * 4096 + (4 * g_ + ((l_ & 15) >> 2)) * 128 + (l_ & 3) * 8; \
        const u32x4 one_ = {0x3F803F80u, 0x3F803F80u, 0x3F803F80u, 0x3F803F80u}; const int co_ = ((l_ & 15) == 0 && rt_c >= 0) ? rt_c + g_ * 16 : 0x7ff00000; \
        _Pragma("unroll") for (int n_ = 0; n_ < 4; ++n_) { const bf16x8 f_ = cat8(trd(tb_ + n_ * 32), trd(tb_ + 16 * 128 + n_ * 32)); \
            __builtin_amdgcn_raw_buffer_store_b128(__builtin_bit_cast(u32x4, f_), rs, rt_d[n_] + l_ * 16, 0, SC1); \
            const f32x4 cs_ = MFMA16(f_, __builtin_bit_cast(bf16x8, one_), (f32x4{0.f, 0.f, 0.f, 0.f})); \
            __builtin_amdgcn_raw_buffer_store_b128(__builtin_bit_cast(u32x4, cs_), rs, co_ + n_ * 2560, 0, SC1); } \
        asm volatile("s_waitcnt lgkmcnt(0)" ::: "memory"); } while (0)
    STAMP(6);
    if (!slow) {
        unsigned wr = (unsigned)(w * 84 < 634 ? w * 84 : 634); const unsigned lw = lds0 + L_WO;
        f32x16 z0, z1; f32x8 z2;
#define SEG_IN [qm0] "v"(qm[0]), [qm1] "v"(qm[1]), [qm2] "v"(qm[2]), [qm3] "v"(qm[3]), [qr0] "v"(qr[0]), [qr1] "v"(qr[1]), [qr2] "v"(qr[2]), [qr3] "v"(qr[3]), [o16] "v"(o16), [o8] "v"(o8)
#define SEG_O [o0] "+v"(o[0]), [o1] "+v"(o[1]), [o2] "+v"(o[2]), [o3] "+v"(o[3]), [kb] "+s"(kb), [vb] "+s"(vb)
#define SEG_K "+{v[200:215]}"(zk0), "+{v[216:231]}"(zk1), "+{v[232:247]}"(zk2), "+{v[248:255]}"(zk3)
#define SEG_Z "+{v[160:175]}"(z0), "+{v[176:191]}"(z1), "+{v[192:199]}"(z2), SEG_K
        asm volatile(ATTN_SEG0_ASM : SEG_O, "=&{v[160:175]}"(z0), "=&{v[176:191]}"(z1), "=&{v[192:199]}"(z2), SEG_K
                     : SEG_IN, [pf] "s"(fill_s) : ATTN_SEG_CLOBBERS);
        asm volatile("" :: "v"(tch1), "v"(tch2));
        asm volatile(ATTN_SEG1_ASM : SEG_O, [wr] "+s"(wr), SEG_Z : SEG_IN, [wo] "s"(wo_s), [ov] "v"(wo_v), [lw] "s"(lw), [pf] "s"(fill_s), [rs] "s"(rt_s), [rm] "s"(rt_m), [rv] "v"(rt_v), [rl] "s"(rt_l88) : ATTN_SEG_CLOBBERS);
        asm volatile(ATTN_WAIT_RT ::: "memory");
        RETILE_OUT();
        asm volatile(ATTN_SEG2_ASM : SEG_O, [wr] "+s"(wr), SEG_Z : SEG_IN, [wo] "s"(wo_s), [ov] "v"(wo_v), [lw] "s"(lw) : ATTN_SEG_CLOBBERS);
        asm volatile(ATTN_WAIT_ST ::: "memory");
        BAR();
        if (w == 0 && fresh_lane() == 0) set_flag(flg + FL_RT + b * 8, TAG(2), 0u);
        { u32x8 pl; const char* fl = flg + FL_RT;
          asm volatile(ATTN_SEG3_ASM : SEG_O, SEG_Z, "=&{v[144:151]}"(pl) : SEG_IN, [fl] "s"(fl) : ATTN_SEG_CLOBBERS);
          const u64 tg = TAG(2);
          const bool ok = (((u64)pl[1] << 16) | (pl[0] >> 16)) == tg && (((u64)pl[3] << 16) | (pl[2] >> 16)) == tg && (((u64)pl[5] << 16) | (pl[4] >> 16)) == tg && (((u64)pl[7] << 16) | (pl[6] >> 16)) == tg;
          if (!__all(ok)) (void)poll_flags<4>(flg + FL_RT, fresh_lane(), 256, tg); else BEHIND_POLL(); }
    } else {
#pragma unroll
        for (int i = 0; i < 4; ++i) glds16s(rt_s + (size_t)i * rt_l88, rt_v, rt_m + i * 1024);
        if (lane < 60) for (int i = 0; i < 14; ++i) { const int r0 = (w * 14 + i) * 6 < 634 ? (w * 14 + i) * 6 : 634; glds16s(wo_s + (size_t)r0 * (D * 2), wo_v, lds0 + L_WO + r0 * 160); }
#pragma unroll
        for (int i = 0; i < 6; ++i) __builtin_amdgcn_raw_buffer_store_b128(u32x4{~0u, ~0u, ~0u, ~0u}, rs, (int)(fill_s - ws) + i * 1024 + lane * 16, 0, SC1);
        DRAIN();
        asm volatile("" :: "v"(tch1), "v"(tch2));
        RETILE_OUT();
        DRAIN(); BAR();
        if (w == 0 && lane == 0) set_flag(flg + FL_RT + b * 8, TAG(2), 0u);
#pragma unroll
        for (int qi = 0; qi < 4; ++qi) { mrow[qi] = -1e30f; if (half) { s16x4 z = {0, 0, 0, 0}; qr[qi] = z; } }
        for (int t = kp * 16; t < kp * 16 + 16; ++t) {
            const bf16x8 km = __builtin_bit_cast(bf16x8, __builtin_amdgcn_raw_buffer_load_b128(rs, (int)WS_KF + t * 1536 + lane * 16, 0, SC1));
            const s16x4 kq = __builtin_bit_cast(s16x4, __builtin_amdgcn_raw_buffer_load_b64(rs, (int)WS_KF + t * 1536 + 1024 + lane * 8, 0, SC1));
            const bf16x8 v0 = __builtin_bit_cast(bf16x8, __builtin_amdgcn_raw_buffer_load_b128(rs, (int)WS_VF + t * 2048 + lane * 16, 0, SC1));
            const bf16x8 v1 = __builtin_bit_cast(bf16x8, __builtin_amdgcn_raw_buffer_load_b128(rs, (int)WS_VF + t * 2048 + 1024 + lane * 16, 0, SC1));
#pragma unroll
            for (int qi = 0; qi < 4; ++qi) {
                f32x16 s = MFMA32K8(kq, qr[qi], f32x16{});
                s = MFMA32(km, qm[qi], s);
                float mx = s[0];
#pragma unroll
                for (int j = 1; j < 16; ++j) mx = fmaxf(mx, s[j]);
                mx = x32_max(mx);
                const float mn = fmaxf(mrow[qi], mx), al = __builtin_amdgcn_exp2f(mrow[qi] - mn); mrow[qi] = mn;
                u32x4 p0, p1;
#pragma unroll
                for (int j = 0; j < 4; ++j) { p0[j] = pk2(__builtin_amdgcn_exp2f(s[2 * j] - mn), __builtin_amdgcn_exp2f(s[2 * j + 1] - mn)); p1[j] = pk2(__builtin_amdgcn_exp2f(s[8 + 2 * j] - mn), __builtin_amdgcn_exp2f(s[9 + 2 * j] - mn)); }
#pragma unroll
                for (int j = 0; j < 16; ++j) o[qi][j] *= al;
                o[qi] = MFMA32(v0, __builtin_bit_cast(bf16x8, p0), o[qi]);
                o[qi] = MFMA32(v1, __builtin_bit_cast(bf16x8, p1), o[qi]);
            }
        }
        (void)poll_flags<4>(flg + FL_RT, lane, 256, TAG(2));
    }
    STAMP(7);
    u32x2 xr[5];
    if (w < 4) {
#pragma unroll
        for (int n = 0; n < 5; ++n) xr[n] = *(const u32x2*)(x + (size_t)(tt * 64 + w * 16 + (lane & 15)) * D + hg * 80 + n * 16 + 4 * (lane >> 4)); }
    float r[12];
    {
        const int hi2 = kp >> 1, lo = kp & 1;
        f32x4 k0[3], k1[3], g0[3], g1[3]; float mk0, mk1, mg0, mg1;
        if (hi2) {
#pragma unroll
            for (int i = 0; i < 3; ++i) { k0[i] = f32x4{o[2][4 * i], o[2][4 * i + 1], o[2][4 * i + 2], o[2][4 * i + 3]}; k1[i] = f32x4{o[3][4 * i], o[3][4 * i + 1], o[3][4 * i + 2], o[3][4 * i + 3]};
                                          g0[i] = f32x4{o[0][4 * i], o[0][4 * i + 1], o[0][4 * i + 2], o[0][4 * i + 3]}; g1[i] = f32x4{o[1][4 * i], o[1][4 * i + 1], o[1][4 * i + 2], o[1][4 * i + 3]}; }
            mk0 = mrow[2]; mk1 = mrow[3]; mg0 = mrow[0]; mg1 = mrow[1];
        } else {
#pragma unroll
            for (int i = 0; i < 3; ++i) { k0[i] = f32x4{o[0][4 * i], o[0][4 * i + 1], o[0][4 * i + 2], o[0][4 * i + 3]}; k1[i] = f32x4{o[1][4 * i], o[1][4 * i + 1], o[1][4 * i + 2], o[1][4 * i + 3]};
                                          g0[i] = f32x4{o[2][4 * i], o[2][4 * i + 1], o[2][4 * i + 2], o[2][4 * i + 3]}; g1[i] = f32x4{o[3][4 * i], o[3][4 * i + 1], o[3][4 * i + 2], o[3][4 * i + 3]}; }
            mk0 = mrow[0]; mk1 = mrow[1]; mg0 = mrow[2]; mg1 = mrow[3];
        }
        { const ldsp p = lds + L_SCR + w * 2 * A_TILE;
#pragma unroll
          for (int i = 0; i < 3; ++i) { *(LAS f32x4*)(p + i * 1024 + lane * 16) = g0[i]; *(LAS f32x4*)(p + A_TILE + i * 1024 + lane * 16) = g1[i]; }
          if (slow) { *(LAS float*)(p + 3072 + lane * 4) = mg0; *(LAS float*)(p + A_TILE + 3072 + lane * 4) = mg1; } }
        BAR();
        const ldsp p = lds + L_SCR + (w ^ 4) * 2 * A_TILE;
        if (!slow) {
#pragma unroll
            for (int i = 0; i < 3; ++i) { k0[i] += *(const LAS f32x4*)(p + i * 1024 + lane * 16); k1[i] += *(const LAS f32x4*)(p + A_TILE + i * 1024 + lane * 16); }
        } else {
            const float a0 = *(const LAS float*)(p + 3072 + lane * 4), a1 = *(const LAS float*)(p + A_TILE + 3072 + lane * 4), x0 = fmaxf(mk0, a0), x1 = fmaxf(mk1, a1);
            const float s0 = __builtin_amdgcn_exp2f(mk0 - x0), t0 = __builtin_amdgcn_exp2f(a0 - x0), s1 = __builtin_amdgcn_exp2f(mk1 - x1), t1 = __builtin_amdgcn_exp2f(a1 - x1);
#pragma unroll
            for (int i = 0; i < 3; ++i) { k0[i] = k0[i] * s0 + *(const LAS f32x4*)(p + i * 1024 + lane * 16) * t0; k1[i] = k1[i] * s1 + *(const LAS f32x4*)(p + A_TILE + i * 1024 + lane * 16) * t1; }
            mk0 = x0; mk1 = x1;
        }
        f32x4 kk[3]; float mk;
        if (lo) {
#pragma unroll
            for (int i = 0; i < 3; ++i) { *(LAS f32x4*)(p + i * 1024 + lane * 16) = k0[i]; kk[i] = k1[i]; }
            mk = mk1; if (slow) *(LAS float*)(p + 3072 + lane * 4) = mk0;
        } else {
#pragma unroll
            for (int i = 0; i < 3; ++i) { *(LAS f32x4*)(p + i * 1024 + lane * 16) = k1[i]; kk[i] = k0[i]; }
            mk = mk0; if (slow) *(LAS float*)(p + 3072 + lane * 4) = mk1;
        }
        BAR();
        const ldscp pq = lds + L_SCR + (w ^ 2 ^ 4) * 2 * A_TILE;
        if (!slow) {
#pragma unroll
            for (int i = 0; i < 3; ++i) kk[i] += *(const LAS f32x4*)(pq + i * 1024 + lane * 16);
        } else {
            const float a0 = *(const LAS float*)(pq + 3072 + lane * 4), x0 = fmaxf(mk, a0), s0 = __builtin_amdgcn_exp2f(mk - x0), t0 = __builtin_amdgcn_exp2f(a0 - x0);
#pragma unroll
            for (int i = 0; i < 3; ++i) kk[i] = kk[i] * s0 + *(const LAS f32x4*)(pq + i * 1024 + lane * 16) * t0;
        }
#pragma unroll
        for (int i = 0; i < 3; ++i) { r[4 * i] = kk[i][0]; r[4 * i + 1] = kk[i][1]; r[4 * i + 2] = kk[i][2]; r[4 * i + 3] = kk[i][3]; }
    }
    {
        const float l = x32_sum(half ? r[8] : 0.f), inv = __builtin_amdgcn_rcpf(l);
        const int hl = rg * 2 + (kp >> 1), T = (kp & 1) * 2 + ((lane & 31) >> 4);
        const ldsp img = lds + L_SCR + (2 * T + 1) * A_TILE + (lane & 15) * 16;
        *(LAS u32x4*)(img + (hl >> 1) * 1024 + ((hl & 1) * 2 + half) * 256) = u32x4{pk2s(r[0] * inv, r[1] * inv), pk2s(r[2] * inv, r[3] * inv), pk2s(r[4] * inv, r[5] * inv), pk2s(r[6] * inv, r[7] * inv)};
        if (!half) *(LAS u32x2*)(img + 2048 + (hl >> 1) * 256 + (hl & 1) * 8) = u32x2{pk2s(r[8] * inv, r[9] * inv), pk2s(r[10] * inv, r[11] * inv)};
        BAR();
        const int s0 = 5 * (hg >> 1);
        { const int T2 = w >> 1, ab = w & 1; const u32x4 vv = *(const LAS u32x4*)(lds + L_SCR + (2 * T2 + 1) * A_TILE + ab * 1024 + lane * 16);
          __builtin_amdgcn_raw_buffer_store_b128(vv, rs, (int)WS_OF + ((tt * 4 + T2) * 20 + s0 + (hg & 1) * 3 + ab) * 1024 + lane * 16, 0, SC1); }
        if (w < 2) { const int T2 = 2 * w + half; const u32x4 vv = *(const LAS u32x4*)(lds + L_SCR + (2 * T2 + 1) * A_TILE + 2048 + (lane & 31) * 16);
          __builtin_amdgcn_raw_buffer_store_b128(vv, rs, (int)WS_OF + ((tt * 4 + T2) * 20 + s0 + 2) * 1024 + (hg & 1) * 512 + (lane & 31) * 16, 0, SC1); }
    }
    if (w < 4) asm volatile("" : "+v"(xr[0]), "+v"(xr[1]), "+v"(xr[2]), "+v"(xr[3]), "+v"(xr[4]));
    STAMP(8);
    const int tl = w & 3, kh = w >> 2, g = lane >> 4;
    const int tok = tt * 64 + tl * 16 + (lane & 15);
    STAMP(9);
    int ft = nt * 8 + w; const bool fvalid = ft < 108; if (!fvalid) ft = 107;
    const bool gu = hg < 7;
    const char* wgb = ws + WS_WGU + (size_t)ft * 40960; const unsigned off16 = lane * 16;
    f32x16 w0, w1, w2, w3, w4, w5;
#define GU_PRE1() asm volatile(GU_PRE1_ASM : "=&{v[160:175]}"(w0), "=&{v[176:191]}"(w1) : [wb] "s"(wgb), [off] "v"(off16) : "s60", "s61", "scc", "memory")
#define GU_PRE2() asm volatile(GU_PRE2_ASM : "=&{v[240:255]}"(w5) : [wb] "s"(wgb20), [off] "v"(off16) : "s60", "s61", "scc", "memory")
    const char* wgb8 = wgb + 8 * 1024; const char* wgb20 = wgb + 20 * 1024;
    if (gu) GU_PRE1();
    {
        f32x16 a0; f32x4 a1;
        { const char* ob = ws + WS_OF + ((size_t)(tt * 4 + tl) * 20 + kh * 10) * 1024; const int q4 = (lane & 15) >> 2, p4 = lane & 3;
          const unsigned lr = lds0 + L_WO + (kh * 320 + (g >> 1) * 20 + (g & 1) * 4 + q4) * 160 + p4 * 8, lz = lds0 + L_WO + (kh * 320 + 40 * g + 16 + q4) * 160 + p4 * 8;
          asm volatile(OP_MAIN_ASM : "=&{v[16:31]}"(a0), "=&{v[32:35]}"(a1), "=&{v[192:207]}"(w2), "=&{v[208:223]}"(w3), "=&{v[224:239]}"(w4)
                       : [ob] "s"(ob), [wb] "s"(wgb8), [off] "v"(off16), [lr] "v"(lr), [lz] "v"(lz) : OP_MAIN_CLOBBERS); }
        STAMP(10);
        f32x4 acc[5] = {f32x4{a0[0], a0[1], a0[2], a0[3]}, f32x4{a0[4], a0[5], a0[6], a0[7]}, f32x4{a0[8], a0[9], a0[10], a0[11]}, f32x4{a0[12], a0[13], a0[14], a0[15]}, a1};
        const ldsp red = lds + L_SCR + tl * 5 * 1024 + lane * 16;
        if (kh == 1) {
#pragma unroll
            for (int n = 0; n < 5; ++n) *(LAS f32x4*)(red + n * 1024) = acc[n]; }
        BAR();
        if (kh == 0) {
            u32x2 hv[5]; float s = 0.f, sq = 0.f;
            const int hb = (int)WS_HF + (tt * 4 + tl) * 20 * 1024 + (lane & 15) * 16 + g * 256;
#pragma unroll
            for (int n = 0; n < 5; ++n) { const f32x4 a = acc[n] + *(const LAS f32x4*)(red + n * 1024);
                hv[n][0] = pk2s(a[0] + bflo(xr[n][0]), a[1] + bfhi(xr[n][0])); hv[n][1] = pk2s(a[2] + bflo(xr[n][1]), a[3] + bfhi(xr[n][1]));
                s = dot2(hv[n][0], 0x3F803F80u, dot2(hv[n][1], 0x3F803F80u, s)); sq = dot2(hv[n][0], hv[n][0], dot2(hv[n][1], hv[n][1], sq)); }
            const int t16 = hg * 5;
            if ((hg & 1) == 0) {
                __builtin_amdgcn_raw_buffer_store_b128(u32x4{hv[0][0], hv[0][1], hv[1][0], hv[1][1]}, rs, hb + (t16 >> 1) * 1024, 0, SC1);
                __builtin_amdgcn_raw_buffer_store_b128(u32x4{hv[2][0], hv[2][1], hv[3][0], hv[3][1]}, rs, hb + ((t16 >> 1) + 1) * 1024, 0, SC1);
                __builtin_amdgcn_raw_buffer_store_b64(hv[4], rs, hb + ((t16 >> 1) + 2) * 1024, 0, SC1);
            } else {
                __builtin_amdgcn_raw_buffer_store_b64(hv[0], rs, hb + (t16 >> 1) * 1024 + 8, 0, SC1);
                __builtin_amdgcn_raw_buffer_store_b128(u32x4{hv[1][0], hv[1][1], hv[2][0], hv[2][1]}, rs, hb + ((t16 >> 1) + 1) * 1024, 0, SC1);
                __builtin_amdgcn_raw_buffer_store_b128(u32x4{hv[3][0], hv[3][1], hv[4][0], hv[4][1]}, rs, hb + ((t16 >> 1) + 2) * 1024, 0, SC1);
            }
            const float mean = x32_sum(x16_sum(s)) * (1.f / 80.f);
            const float m2 = fmaxf(x32_sum(x16_sum(sq)) - 80.f * mean * mean, 0.f);
            __builtin_amdgcn_raw_buffer_store_b64(u32x2{nos(__float_as_uint(mean)), nos(__float_as_uint(m2))}, rs, g == 0 ? (int)WS_ST + (tok * 8 + hg) * 8 : 0x7ff00000, 0, SC1);
        }
        BAR();
    }
    if (gu) GU_PRE2();
    STAMP(11);
    const int ksb = (w & 3) * 14 - ((w & 3) == 3);
    const char* wdb = ws + WS_WD + (size_t)hg * 276480 + (size_t)(ksb + (w >> 2)) * 5120;
    const unsigned lw5 = lds0 + (w >> 2) * 20480 + (w & 3) * 5120, st2 = 10240;
    if (gu) {
        STAMP(12);
        f32x16 c0, c1, c2, c3, st, u0, u1;
        { const char* hb = ws + WS_HF + (size_t)(G * 8 + w) * 20 * 1024; const char* wb2 = wgb + 24 * 1024; const char* sb = ws + WS_ST + (size_t)(G * 128 + w * 16) * 64; const char* cb = ws + WS_CS + (size_t)ft * 2560;
          const unsigned wo1k = w * 1024, offg = fresh_lane() * 16;
          asm volatile(GU_MAIN_ASM
              : "=&{v[16:31]}"(c0), "=&{v[32:47]}"(c1), "=&{v[48:63]}"(c2), "=&{v[64:79]}"(c3), "=&{v[112:127]}"(st), "=&{v[128:143]}"(u0), "=&{v[144:159]}"(u1),
                "+{v[160:175]}"(w0), "+{v[176:191]}"(w1), "+{v[192:207]}"(w2), "+{v[208:223]}"(w3), "+{v[224:239]}"(w4), "+{v[240:255]}"(w5)
              : [wb] "s"(wb2), [hb] "s"(hb), [wd] "s"(wdb), [sb] "s"(sb), [cb] "s"(cb), [off] "v"(offg), [l0] "s"(lds0), [wo] "s"(wo1k), [lw] "s"(lw5)
              : GU_MAIN_CLOBBERS); }
        STAMP(13);
        const int lane = fresh_lane(), g = lane >> 4;
        const float mean = x32_sum(x16_sum(st[0] + st[2])) * 0.125f;
        const float d0 = st[0] - mean, d1 = st[2] - mean;
        const float m2 = x32_sum(x16_sum(st[1] + st[3] + 80.f * (d0 * d0 + d1 * d1)));
        const float rstd2 = __builtin_amdgcn_rcpf(m2 * (1.f / D) + LN_EPS);
        float cg[4], cu[4];
#pragma unroll
        for (int j = 0; j < 4; ++j) { const bool two = (lane & 15) < 4; cg[j] = row16_sum(st[4 + j] + (two ? st[8 + j] : 0.f)); cu[j] = row16_sum(st[12 + j] + (two ? w4[j] : 0.f)); }
        BAR();
        if (g == 0) *(LAS f32x2*)(lds + L_STAT + (w * 16 + (lane & 15)) * 8) = f32x2{-mean, rstd2};
        BAR();
        u32x4 ha, hb2, hc, hd;
        const f32x2 cg01 = {cg[0], cg[1]}, cg23 = {cg[2], cg[3]}, cu01 = {cu[0], cu[1]}, cu23 = {cu[2], cu[3]};
#pragma unroll
        for (int t = 0; t < 8; ++t) {
            const f32x2 sv = *(const LAS f32x2*)(lds + L_STAT + (t * 16 + (lane & 15)) * 8);
            const f32x16& c = t < 2 ? c0 : t < 4 ? c1 : t < 6 ? c2 : c3; const int o8 = (t & 1) * 8;
            const f32x2 nm = {sv[0], sv[0]}, r2 = {sv[1], sv[1]};
            const f32x2 g01 = __builtin_elementwise_fma(nm, cg01, f32x2{c[o8], c[o8 + 1]}), g23 = __builtin_elementwise_fma(nm, cg23, f32x2{c[o8 + 2], c[o8 + 3]});
            const f32x2 u01 = __builtin_elementwise_fma(nm, cu01, f32x2{c[o8 + 4], c[o8 + 5]}), u23 = __builtin_elementwise_fma(nm, cu23, f32x2{c[o8 + 6], c[o8 + 7]});
            const f32x2 h01 = (g01 * u01) * r2, h23 = (g23 * u23) * r2;
            u32x4& hx = t < 2 ? ha : t < 4 ? hb2 : t < 6 ? hc : hd; hx[(t & 1) * 2] = pk2s(h01[0], h01[1]); hx[(t & 1) * 2 + 1] = pk2s(h23[0], h23[1]);
        }
        const int odd = w & 1;
        const int hb = fvalid ? (int)WS_HH + ((G * 8 + odd * 4) * 54 + (ft >> 1)) * 1024 + lane * 16 : 0x7ff00000;
        const ldsp exw = lds + L_EX + w * 2048 + lane * 16; const ldscp exr = lds + L_EX + (w ^ 1) * 2048 + lane * 16;
        if (odd) {
            *(LAS u32x4*)exw = ha; *(LAS u32x4*)(exw + 1024) = hb2;
            BAR();
            const u32x4 got0 = *(const LAS u32x4*)exr, got1 = *(const LAS u32x4*)(exr + 1024);
            __builtin_amdgcn_raw_buffer_store_b128(u32x4{got0[0], got0[1], hc[0], hc[1]}, rs, hb, 0, SC1);
            __builtin_amdgcn_raw_buffer_store_b128(u32x4{got0[2], got0[3], hc[2], hc[3]}, rs, hb + 54 * 1024, 0, SC1);
            __builtin_amdgcn_raw_buffer_store_b128(u32x4{got1[0], got1[1], hd[0], hd[1]}, rs, hb + 2 * 54 * 1024, 0, SC1);
            __builtin_amdgcn_raw_buffer_store_b128(u32x4{got1[2], got1[3], hd[2], hd[3]}, rs, hb + 3 * 54 * 1024, 0, SC1);
        } else {
            *(LAS u32x4*)exw = hc; *(LAS u32x4*)(exw + 1024) = hd;
            BAR();
            const u32x4 got0 = *(const LAS u32x4*)exr, got1 = *(const LAS u32x4*)(exr + 1024);
            __builtin_amdgcn_raw_buffer_store_b128(u32x4{ha[0], ha[1], got0[0], got0[1]}, rs, hb, 0, SC1);
            __builtin_amdgcn_raw_buffer_store_b128(u32x4{ha[2], ha[3], got0[2], got0[3]}, rs, hb + 54 * 1024, 0, SC1);
            __builtin_amdgcn_raw_buffer_store_b128(u32x4{hb2[0], hb2[1], got1[0], got1[1]}, rs, hb + 2 * 54 * 1024, 0, SC1);
            __builtin_amdgcn_raw_buffer_store_b128(u32x4{hb2[2], hb2[3], got1[2], got1[3]}, rs, hb + 3 * 54 * 1024, 0, SC1);
        }
        BAR();
        { const unsigned offc = fresh_lane() * 16;
          asm volatile(DN_COMMIT4 : "+{v[128:143]}"(u0), "+{v[144:159]}"(u1), "+{v[160:175]}"(w0), "+{v[176:191]}"(w1), "+{v[192:207]}"(w2), "+{v[224:239]}"(w4), "+{v[240:255]}"(w5)
                       : [off] "v"(offc), [lw] "s"(lw5) : DN_COMMIT4_CLOBBERS); }
    } else {
        f32x16 z0, z1, z2, y0, y1, y2; const unsigned offe = fresh_lane() * 16; const char* wb2 = wdb + 2 * 10240;
        asm volatile(DN_PRE01_ISSUE : "=&{v[112:127]}"(z0), "=&{v[128:143]}"(z1), "=&{v[144:159]}"(z2) : [wb] "s"(wdb), [off] "v"(offe), [st] "s"(st2) : DN_PRE01_ISSUE_CLOBBERS);
        asm volatile(DN_PRE23_ISSUE : "=&{v[160:175]}"(y0), "=&{v[176:191]}"(y1), "=&{v[192:207]}"(y2) : [wb] "s"(wb2), [off] "v"(offe), [st] "s"(st2) : DN_PRE23_ISSUE_CLOBBERS);
        asm volatile("s_waitcnt vmcnt(0)" ::: "memory");
        asm volatile(DN_PRE01_COMMIT : "+{v[112:127]}"(z0), "+{v[128:143]}"(z1), "+{v[144:159]}"(z2) : [off] "v"(offe), [lw] "s"(lw5) : DN_PRE01_COMMIT_CLOBBERS);
        asm volatile(DN_PRE23_COMMIT : "+{v[160:175]}"(y0), "+{v[176:191]}"(y1), "+{v[192:207]}"(y2) : [off] "v"(offe), [lw] "s"(lw5) : DN_PRE23_COMMIT_CLOBBERS);
    }
    STAMP(14);
    {
        const int lane0 = fresh_lane(); const unsigned off16b = lane0 * 16;
        const int wm = w & 1, kq = w >> 1;
        const int ftile = 2 * wm + (kq >> 1), n0 = (kq & 1) * 3;
        const char* rp[3];
#pragma unroll
        for (int i = 0; i < 3; ++i) { const int k = hg * 80 + (n0 + i < 5 ? n0 + i : 4) * 16; rp[i] = ws + WS_HF + (size_t)(tt * 4 + ftile) * 20 * 1024 + (k >> 5) * 1024 + ((k >> 4) & 1) * 8; }
        BAR();
        STAMP(15);
        f32x16 a0, a1, hvv; f32x4 a2, a3;
        { const int kb0 = kq * 14 - (kq == 3);
          const char* hb = ws + WS_HH + ((size_t)(tt * 4 + 2 * wm) * 54 + kb0) * 1024; const char* wb4 = wdb + 4 * 10240; const unsigned l0 = lds0 + kq * 5120, skip = kq >> 1;
          asm volatile(DN_MAIN_ASM : "=&{v[16:31]}"(a0), "=&{v[32:47]}"(a1), "=&{v[48:51]}"(a2), "=&{v[52:55]}"(a3), "=&{v[224:239]}"(hvv)
              : [r0] "s"(rp[0]), [r1] "s"(rp[1]), [r2] "s"(rp[2]), [wb] "s"(wb4), [hb] "s"(hb), [off] "v"(off16b), [l0] "s"(l0), [lw] "s"(lw5), [st] "s"(st2), [skip] "s"(skip) : DN_MAIN_CLOBBERS); }
        STAMP(16);
        const int lane = fresh_lane(), g = lane >> 4;
        f32x4 acc[2][5];
#pragma unroll
        for (int n = 0; n < 4; ++n) acc[0][n] = f32x4{a0[4 * n], a0[4 * n + 1], a0[4 * n + 2], a0[4 * n + 3]};
        acc[0][4] = f32x4{a1[0], a1[1], a1[2], a1[3]};
#pragma unroll
        for (int n = 0; n < 3; ++n) acc[1][n] = f32x4{a1[4 + 4 * n], a1[5 + 4 * n], a1[6 + 4 * n], a1[7 + 4 * n]};
        acc[1][3] = a2; acc[1][4] = a3;
#pragma unroll
        for (int t = 0; t < 2; ++t)
#pragma unroll
            for (int n = 0; n < 5; ++n) *(LAS f32x4*)(lds + (w >> 2) * 122880 + (((w & 3) * 2 + t) * 5 + n) * 1024 + lane * 16) = acc[t][n];
        BAR();
        const int tok = tt * 64 + ftile * 16 + (lane & 15);
#pragma unroll
        for (int i = 0; i < 3; ++i) if (n0 + i < 5) {
            f32x4 a = {0.f, 0.f, 0.f, 0.f};
#pragma unroll
            for (int q = 0; q < 4; ++q) { const int ww = q * 2 + wm; a += *(const LAS f32x4*)(lds + (ww >> 2) * 122880 + (((ww & 3) * 2 + (kq >> 1)) * 5 + n0 + i) * 1024 + lane * 16); }
            const unsigned h0 = __float_as_uint(hvv[2 * i]), h1 = __float_as_uint(hvv[2 * i + 1]);
            const u32x2 vv = {pk2(a[0] + bflo(h0), a[1] + bfhi(h0)), pk2(a[2] + bflo(h1), a[3] + bfhi(h1))};
            *(u32x2*)(out + (size_t)tok * D + hg * 80 + (n0 + i) * 16 + 4 * g) = vv; }
    }
    }
    if (b == 0 && w == 0 && fresh_lane() == 0) __hip_atomic_store((gu64*)(flg + FL_NONCE), nonce + 16, RLX_AGENT);
    STAMP(17);
}

extern "C" void kernel_launch(void* const* d_in, const int* in_sizes, int n_in, void* d_out, int out_size, void* d_ws, size_t ws_size, hipStream_t stream) {
    const u16* x = (const u16*)d_in[0]; const u16* wq = (const u16*)d_in[1]; const u16* wk = (const u16*)d_in[2]; const u16* wv = (const u16*)d_in[3]; const u16* wo = (const u16*)d_in[4];
    const u16* wg = (const u16*)d_in[6]; const u16* wu = (const u16*)d_in[7]; const u16* wd = (const u16*)d_in[8];
    (void)hipFuncSetAttribute((const void*)k_block, hipFuncAttributeMaxDynamicSharedMemorySize, LDS_BYTES);
    k_block<<<256, 512, LDS_BYTES, stream>>>(x, wq, wk, wv, wo, wg, wu, wd, (char*)d_ws, (u16*)d_out);
}
```
